```python
import torch

def _softmax(x, axis=-1):
    return torch.softmax(x, dim=axis)
def _gelu(x):
    return torch.nn.functional.gelu(x, approximate="tanh")
def _silu(x):
    return torch.nn.functional.silu(x)
def _sigmoid(x):
    return torch.sigmoid(x)
def _layernorm(x, axis=-1, eps=1e-5):
    m = x.mean(dim=axis, keepdim=True)
    v = x.var(dim=axis, keepdim=True, unbiased=False)
    return (x - m) / torch.sqrt(v + eps)
def _scatter_add(tgt, idx, src, axis=0):
    return tgt.clone().index_add_(axis, idx, src)
def _segment_sum(src, seg, n):
    out = torch.zeros((n,) + src.shape[1:], dtype=src.dtype)
    return out.index_add_(0, seg, src)
def _pad(x, pw):
    flat = tuple(v for pair in reversed(pw) for v in pair)
    return torch.nn.functional.pad(x, flat)

kernel_name = 'standalone__gqa_block_cached__near_ridge'

def setup_inputs():

    _w = (lambda a: (lambda t: t.to(torch.bfloat16) if t.is_floating_point() else t)(torch.as_tensor(a)))
    B, Sq, D = 8, 144, 640
    Hq, Hkv, Sk, Dh = 32, 32, 1024, 128
    assert Hq % Hkv == 0, (Hq, Hkv)
    return {
        "x": _w((lambda *s: torch.randn(*s, dtype=torch.bfloat16) * 0.8)(B, Sq, D)),
        "k_cache": _w((lambda *s: torch.randn(*s, dtype=torch.bfloat16) * 0.8)(B, Hkv, Sk, Dh)),
        "v_cache": _w((lambda *s: torch.randn(*s, dtype=torch.bfloat16) * 0.05)(B, Hkv, Sk, Dh)),
        "wq": _w((lambda *s: torch.randn(*s, dtype=torch.bfloat16) * 0.8)(D, Hq * Dh) * (D ** -0.5)),
        "wo": _w((lambda *s: torch.randn(*s, dtype=torch.bfloat16) * 0.05)(Hq * Dh, D) * ((Hq * Dh) ** -0.5)),
    }

def reference(x, k_cache, v_cache, wq, wo):
    B, Sq, D = x.shape
    Hq, Dh = 32, 128
    Hkv = k_cache.shape[1]
    rep = Hq // Hkv
    q = (lambda x, *d: x.permute(*d))((x @ wq).reshape(B, Sq, Hq, Dh), 0, 2, 1, 3)
    k = (lambda x, n, axis: torch.repeat_interleave(x, n, dim=axis))(k_cache, rep, axis=1)
    v = (lambda x, n, axis: torch.repeat_interleave(x, n, dim=axis))(v_cache, rep, axis=1)
    scores = torch.einsum("bhqd,bhkd->bhqk", q, k) * (Dh ** -0.5)
    p = _softmax(scores, axis=-1)
    o = torch.einsum("bhqk,bhkd->bhqd", p, v)
    return (lambda x, *d: x.permute(*d))(o, 0, 2, 1, 3).reshape(B, Sq, Hq * Dh) @ wo
```

```cpp
#include <hip/hip_runtime.h>
#include <hip/hip_bf16.h>
typedef unsigned short u16;
__device__ __forceinline__ float bf2f(u16 b){ return __uint_as_float(((unsigned)b)<<16); }
__device__ __forceinline__ u16 f2bf(float f){ unsigned u=__float_as_uint(f); u += 0x7fffu + ((u>>16)&1u); return (u16)(u>>16); }
constexpr int B=8,SQ=144,D=640,H=32,SK=1024,DH=128;
__global__ void qproj(const u16* x,const u16* wq,u16* q){
  int idx=blockIdx.x*256+threadIdx.x; int n=idx%4096, m=idx/4096;
  float acc=0; for(int k=0;k<D;k++) acc+=bf2f(x[m*D+k])*bf2f(wq[k*4096+n]);
  q[idx]=f2bf(acc);
}
__global__ void attn(const u16* q,const u16* k,const u16* v,u16* o){
  int blk=blockIdx.x; int qi=blk%SQ, h=(blk/SQ)%H, b=blk/(SQ*H);
  __shared__ float s[SK]; __shared__ float qs[DH]; __shared__ float red[256];
  int t=threadIdx.x;
  if(t<DH) qs[t]=bf2f(q[(b*SQ+qi)*4096+h*DH+t]);
  __syncthreads();
  const u16* kb=k+(size_t)(b*H+h)*SK*DH; const u16* vb=v+(size_t)(b*H+h)*SK*DH;
  float mx=-1e30f;
  for(int j=t;j<SK;j+=256){ float a=0; for(int d=0;d<DH;d++) a+=qs[d]*bf2f(kb[j*DH+d]); a*=0.08838834764831845f; s[j]=a; mx=fmaxf(mx,a);}
  red[t]=mx; __syncthreads(); for(int o2=128;o2>0;o2>>=1){ if(t<o2) red[t]=fmaxf(red[t],red[t+o2]); __syncthreads(); }
  mx=red[0]; __syncthreads();
  float sum=0; for(int j=t;j<SK;j+=256){ float e=__expf(s[j]-mx); s[j]=e; sum+=e; }
  red[t]=sum; __syncthreads(); for(int o2=128;o2>0;o2>>=1){ if(t<o2) red[t]+=red[t+o2]; __syncthreads(); }
  sum=red[0];
  if(t<DH){ float a=0; for(int j=0;j<SK;j++) a+=s[j]*bf2f(vb[j*DH+t]); o[(b*SQ+qi)*4096+h*DH+t]=f2bf(a/sum);}
}
__global__ void oproj(const u16* o,const u16* wo,u16* out){
  int idx=blockIdx.x*256+threadIdx.x; int n=idx%D, m=idx/D;
  float acc=0; for(int k=0;k<4096;k++) acc+=bf2f(o[m*4096+k])*bf2f(wo[k*D+n]);
  out[idx]=f2bf(acc);
}
extern "C" void kernel_launch(void* const* d_in, const int* in_sizes, int n_in, void* d_out, int out_size, void* d_ws, size_t ws_size, hipStream_t stream){
  const u16* x=(const u16*)d_in[0]; const u16* k=(const u16*)d_in[1]; const u16* v=(const u16*)d_in[2]; const u16* wq=(const u16*)d_in[3]; const u16* wo=(const u16*)d_in[4];
  u16* q=(u16*)d_ws; u16* o=q+1152*4096;
  qproj<<<1152*4096/256,256,0,stream>>>(x,wq,q);
  attn<<<B*H*SQ,256,0,stream>>>(q,k,v,o);
  oproj<<<1152*640/256,256,0,stream>>>(o,wo,(u16*)d_out);
}
```

```cpp
#include <hip/hip_runtime.h>
#include <hip/hip_bf16.h>
typedef unsigned short u16;

__global__ __launch_bounds__(512, 2) void gqa_block_kernel(const u16* __restrict__ x, const u16* __restrict__ kc, const u16* __restrict__ vc, const u16* __restrict__ wq,
                                                          const u16* __restrict__ wo, u16* __restrict__ out, char* __restrict__ ws) {
  __shared__ __attribute__((aligned(16))) char smem[163840];
  unsigned tid = threadIdx.x;
  asm volatile(
".macro M d,a,b,c\nv_mfma_f32_16x16x32_bf16 v[\\d:\\d+3], v[\\a:\\a+3], v[\\b:\\b+3], v[\\c:\\c+3]\n.endm\n.macro R d,a,o\n"
"ds_read_b128 v[\\d:\\d+3], v\\a offset:\\o\n.endm\n.macro T d,a,o\nds_read_b64_tr_b16 v[\\d:\\d+1], v\\a offset:\\o\n.endm\n.macro E d\n"
"v_exp_f32 v\\d, v\\d\n.endm\n.macro C d,a,b\nv_cvt_pk_bf16_f32 v\\d, v\\a, v\\b\n.endm\n.macro W n\ns_waitcnt lgkmcnt(\\n)\n.endm\n.Lbeg_1_%=:\n"
"v_lshrrev_b32 v1, 6, v0\nv_and_b32 v255, 63, v0\nv_readfirstlane_b32 s19, v1\ns_mov_b32 s35, 0\n.Lrs_16_%=:\ns_memrealtime s[96:97]\n"
"s_load_dwordx8 s[4:11], s[0:1], 0x0\ns_load_dwordx4 s[12:15], s[0:1], 0x20\ns_load_dwordx2 s[16:17], s[0:1], 0x30\ns_and_b32 s29, s18, 7\n"
"s_lshr_b32 s24, s18, 3\ns_lshr_b32 s59, s29, 1\ns_lshl_b32 s26, s59, 3\ns_and_b32 s58, s24, 7\ns_add_u32 s22, s26, s58\ns_and_b32 s26, s29, 1\n"
"s_lshl_b32 s26, s26, 2\ns_lshr_b32 s28, s24, 3\ns_add_u32 s21, s26, s28\ns_and_b32 s20, s19, 3\ns_and_b32 s57, s19, 1\ns_lshr_b32 s25, s19, 1\n"
"s_lshl_b32 s56, s21, 5\ns_add_u32 s56, s56, s22\ns_cmp_lt_u32 s19, 4\ns_cbranch_scc0 .Lw_17_%=\ns_cmp_eq_u32 s19, 0\ns_cbranch_scc0 .Lw_18_%=\n"
"s_getpc_b64 s[48:49]\n.Lpc_19_%=:\ns_add_u32 s48, s48, .Lbeg_1_%=-.Lpc_19_%=\ns_addc_u32 s49, s49, -1\ns_and_b32 s49, s49, 65535\n"
"s_mov_b32 s50, .Lend_2_%=-.Lbeg_1_%=\ns_mov_b32 s51, 131072\nv_and_b32 v254, 31, v255\nv_lshlrev_b32 v254, 5, v254\nv_add_u32 v254, s24, v254\n"
"v_lshlrev_b32 v254, 7, v254\nv_lshrrev_b32 v253, 5, v255\nv_lshlrev_b32 v253, 6, v253\nv_add_u32 v2, v254, v253\n"
"buffer_load_dword v215, v2, s[48:51], 0 offen\n.Lw_18_%=:\ns_branch .Lq0_3_%=\n.Lw_17_%=:\ns_mul_i32 s27, s58, 5\ns_mul_i32 s28, s28, 10\n"
"s_add_u32 s27, s27, s28\ns_lshr_b32 s23, s27, 2\ns_cmp_ge_u32 s23, 10\ns_cselect_b32 s27, 10, 0\ns_sub_u32 s23, s23, s27\n.Lq1_4_%=:\n"
"s_lshl_b32 s31, s20, 11\ns_mul_i32 s29, s20, 40960\nv_lshrrev_b32 v254, 3, v255\nv_mul_u32_u24 v254, 1280, v254\nv_and_b32 v253, 7, v255\n"
"v_lshrrev_b32 v252, 4, v255\nv_xor_b32 v253, v253, v252\nv_lshlrev_b32 v253, 4, v253\nv_add_u32 v254, v254, v253\nv_add_u32 v226, s29, v254\n"
"v_xor_b32 v254, 64, v226\nv_add_u32 v227, 9216, v254\nv_add_u32 v228, 18432, v226\nv_add_u32 v229, 18432, v227\ns_add_u32 s52, s23, 0\n"
"s_cmp_ge_u32 s52, 10\ns_cselect_b32 s26, 10, 0\ns_sub_u32 s52, s52, s26\ns_mul_i32 s27, s21, 184320\nW 0\ns_add_u32 s70, s96, 270\n"
"s_add_u32 s40, s4, s27\ns_addc_u32 s41, s5, 0\ns_and_b32 s41, s41, 65535\ns_mov_b32 s42, 184320\ns_mov_b32 s43, 131072\ns_lshl_b32 s26, s31, 1\n"
"s_add_u32 m0, s26, 145408\ns_lshl_b32 s53, s52, 7\nbuffer_load_dwordx4 v226, s[40:43], s53 offen lds\n"
"buffer_load_dwordx4 v227, s[40:43], s53 offen offset:1024 lds\nbuffer_load_dwordx4 v228, s[40:43], s53 offen offset:2048 lds\n"
"buffer_load_dwordx4 v229, s[40:43], s53 offen offset:3072 lds\nv_mov_b32 v254, s57\nv_lshlrev_b32 v251, 3, v254\nv_add_u32 v251, 128, v251\n"
"v_lshrrev_b32 v254, 3, v255\nv_add_u32 v251, v251, v254\nv_mul_u32_u24 v251, 1280, v251\nv_and_b32 v254, 7, v255\nv_mov_b32 v252, s57\n"
"v_lshlrev_b32 v253, 2, v252\nv_lshrrev_b32 v252, 4, v255\nv_add_u32 v253, v253, v252\nv_xor_b32 v254, v254, v253\nv_lshlrev_b32 v254, 4, v254\n"
"v_add_u32 v230, v251, v254\ns_cmp_ge_u32 s19, 6\ns_cbranch_scc0 .Lw_20_%=\ns_lshr_b32 s26, s31, 1\ns_add_u32 m0, s26, 159744\ns_nop 0\n"
"buffer_load_dwordx4 v230, s[40:43], s53 offen lds\n.Lw_20_%=:\ns_setprio 1\nv_mov_b32 v254, s20\nv_lshlrev_b32 v251, 3, v254\n"
"v_lshrrev_b32 v254, 2, v255\nv_and_b32 v254, 7, v254\nv_add_u32 v251, v251, v254\nv_lshlrev_b32 v251, 13, v251\nv_lshrrev_b32 v254, 5, v255\n"
"v_lshlrev_b32 v254, 6, v254\nv_add_u32 v251, v251, v254\nv_and_b32 v254, 3, v255\nv_mov_b32 v252, s20\nv_lshlrev_b32 v253, 1, v252\n"
"v_lshrrev_b32 v252, 4, v255\nv_and_b32 v252, 1, v252\nv_add_u32 v253, v253, v252\nv_and_b32 v253, 3, v253\nv_xor_b32 v254, v254, v253\n"
"v_lshlrev_b32 v254, 4, v254\nv_add_u32 v251, v251, v254\nv_add_u32 v231, 1024, v251\nv_subrev_u32 v232, 896, v231\ns_lshl_b32 s27, s22, 8\n"
"s_add_u32 s44, s10, s27\ns_addc_u32 s45, s11, 0\ns_sub_u32 s44, s44, 1024\ns_subb_u32 s45, s45, 0\ns_and_b32 s45, s45, 65535\ns_mov_b32 s46, 0\n"
"s_mov_b32 s47, 131072\ns_sub_u32 s46, 5243904, s27\ns_lshl_b32 s53, s52, 19\ns_add_u32 m0, s31, 59392\ns_nop 0\n"
"buffer_load_dwordx4 v231, s[44:47], s53 offen lds\nbuffer_load_dwordx4 v232, s[44:47], s53 offen offset:1024 lds\ns_lshl_b32 s53, s52, 19\n"
"s_add_u32 m0, s31, 67584\ns_add_u32 s53, s53, 262144\nbuffer_load_dwordx4 v231, s[44:47], s53 offen lds\n"
"buffer_load_dwordx4 v232, s[44:47], s53 offen offset:1024 lds\ns_lshl_b32 s26, s21, 8\ns_add_u32 s26, s26, 33619968\ns_load_dword s69, s[16:17], s26\n"
"v_and_b32 v254, 15, v255\nv_lshlrev_b32 v254, 7, v254\nv_add_u32 v254, 43008, v254\nv_lshrrev_b32 v253, 4, v255\nv_and_b32 v252, 15, v255\n"
"v_lshrrev_b32 v252, 1, v252\nv_xor_b32 v253, v253, v252\nv_lshlrev_b32 v253, 4, v253\nv_add_u32 v94, v254, v253\nv_xor_b32 v95, 64, v94\n"
"v_add_u32 v220, 75776, v94\nv_add_u32 v221, 75776, v95\nv_lshrrev_b32 v254, 4, v255\nv_lshlrev_b32 v254, 11, v254\nv_add_u32 v254, 51200, v254\n"
"v_mov_b32 v252, s20\nv_lshlrev_b32 v253, 9, v252\nv_add_u32 v254, v254, v253\nv_lshrrev_b32 v253, 2, v255\nv_and_b32 v253, 3, v253\n"
"v_lshlrev_b32 v253, 6, v253\nv_add_u32 v254, v254, v253\nv_and_b32 v253, 1, v255\nv_lshlrev_b32 v253, 3, v253\nv_add_u32 v254, v254, v253\n"
"v_lshrrev_b32 v253, 1, v255\nv_and_b32 v253, 1, v253\nv_lshrrev_b32 v252, 3, v255\nv_and_b32 v252, 2, v252\nv_xor_b32 v253, v253, v252\n"
"v_lshlrev_b32 v253, 4, v253\nv_add_u32 v222, v254, v253\nv_xor_b32 v254, 16, v222\nv_add_u32 v223, 256, v254\nv_xor_b32 v224, 32, v222\n"
"v_xor_b32 v225, 32, v223\ns_lshl_b32 s27, s56, 18\ns_add_u32 s60, s6, s27\ns_addc_u32 s61, s7, 0\ns_sub_u32 s60, s60, 1024\ns_subb_u32 s61, s61, 0\n"
"s_and_b32 s61, s61, 65535\ns_mov_b32 s62, 263168\ns_mov_b32 s63, 131072\ns_add_u32 s36, s8, s27\ns_addc_u32 s37, s9, 0\ns_sub_u32 s36, s36, 1024\n"
"s_subb_u32 s37, s37, 0\ns_and_b32 s37, s37, 65535\ns_mov_b32 s38, 263168\ns_mov_b32 s39, 131072\ns_lshl_b32 s30, s20, 14\ns_lshl_b32 s55, s20, 13\n"
"s_mul_i32 s26, s18, 2654435761\ns_lshr_b32 s26, s26, 11\ns_and_b32 s95, s26, 258048\ns_and_b32 s26, s26, 0\ns_xor_b32 s30, s30, s26\n"
"s_and_b32 s68, s21, 1\ns_mul_i32 s26, s68, 270\ns_add_u32 s70, s70, s26\ns_sub_u32 s94, 9729, s26\ns_mul_i32 s68, s68, -16\ns_add_u32 s68, s68, 480\n"
"v_lshrrev_b32 v254, 2, v255\nv_and_b32 v254, 7, v254\nv_lshlrev_b32 v254, 8, v254\nv_lshrrev_b32 v253, 5, v255\nv_lshlrev_b32 v253, 6, v253\n"
"v_add_u32 v254, v254, v253\nv_and_b32 v253, 3, v255\nv_lshlrev_b32 v253, 4, v253\nv_add_u32 v254, v254, v253\nv_add_u32 v247, 1024, v254\n"
"v_subrev_u32 v248, 896, v247\nv_xor_b32 v249, 32, v247\nv_xor_b32 v250, 32, v248\nv_and_b32 v254, 15, v255\nv_lshlrev_b32 v254, 8, v254\n"
"v_add_u32 v254, 147456, v254\nv_mov_b32 v252, s20\nv_lshlrev_b32 v253, 2, v252\nv_lshrrev_b32 v252, 5, v255\nv_add_u32 v253, v253, v252\n"
"v_and_b32 v252, 15, v255\nv_xor_b32 v253, v253, v252\nv_lshlrev_b32 v253, 4, v253\nv_add_u32 v254, v254, v253\nv_lshrrev_b32 v253, 4, v255\n"
"v_and_b32 v253, 1, v253\nv_lshlrev_b32 v253, 3, v253\nv_add_u32 v233, v254, v253\nv_xor_b32 v234, 32, v233\ns_mul_i32 s27, s57, 12288\n"
"s_mov_b32 s28, 0\nv_and_b32 v254, 15, v255\nv_lshlrev_b32 v254, 8, v254\nv_add_u32 v254, 126976, v254\nv_lshrrev_b32 v253, 4, v255\n"
"v_and_b32 v252, 15, v255\nv_xor_b32 v253, v253, v252\nv_lshlrev_b32 v253, 4, v253\nv_add_u32 v254, v254, v253\nv_add_u32 v235, s27, v254\n"
"v_xor_b32 v236, 64, v235\nv_xor_b32 v237, 128, v235\nv_xor_b32 v238, 192, v235\nv_lshrrev_b32 v254, 2, v255\nv_and_b32 v254, 3, v254\n"
"v_lshlrev_b32 v254, 11, v254\nv_and_b32 v253, 3, v255\nv_lshlrev_b32 v253, 6, v253\nv_add_u32 v254, v254, v253\nv_lshrrev_b32 v253, 4, v255\n"
"v_lshrrev_b32 v252, 1, v255\nv_and_b32 v252, 2, v252\nv_xor_b32 v253, v253, v252\nv_lshlrev_b32 v253, 4, v253\nv_add_u32 v239, v254, v253\n"
"v_add_u32 v244, s28, v239\nv_lshrrev_b32 v254, 4, v255\nv_lshlrev_b32 v254, 11, v254\nv_add_u32 v254, 32768, v254\nv_lshrrev_b32 v253, 2, v255\n"
"v_and_b32 v253, 3, v253\nv_lshlrev_b32 v253, 6, v253\nv_add_u32 v254, v254, v253\nv_and_b32 v253, 1, v255\nv_lshlrev_b32 v253, 3, v253\n"
"v_add_u32 v254, v254, v253\nv_lshrrev_b32 v253, 1, v255\nv_and_b32 v253, 1, v253\nv_lshrrev_b32 v252, 3, v255\nv_and_b32 v252, 2, v252\n"
"v_xor_b32 v253, v253, v252\nv_lshlrev_b32 v253, 4, v253\nv_add_u32 v254, v254, v253\nv_add_u32 v245, s28, v254\nv_xor_b32 v246, 32, v245\n"
"v_mov_b32 v240, 1065369472\nv_mov_b32 v241, 1065369472\nv_mov_b32 v242, 1065369472\nv_mov_b32 v243, 1065369472\nv_mov_b32 v252, 1040356334\n"
"v_mov_b32 v253, 1040356334\ns_add_u32 s52, s23, 1\ns_cmp_ge_u32 s52, 10\ns_cselect_b32 s26, 10, 0\ns_sub_u32 s52, s52, s26\ns_lshl_b32 s26, s31, 1\n"
"s_add_u32 m0, s26, 108544\ns_lshl_b32 s53, s52, 7\nbuffer_load_dwordx4 v226, s[40:43], s53 offen lds\n"
"buffer_load_dwordx4 v227, s[40:43], s53 offen offset:1024 lds\nbuffer_load_dwordx4 v228, s[40:43], s53 offen offset:2048 lds\n"
"buffer_load_dwordx4 v229, s[40:43], s53 offen offset:3072 lds\ns_cmp_ge_u32 s19, 6\ns_cbranch_scc0 .Lw_21_%=\ns_lshr_b32 s26, s31, 1\n"
"s_add_u32 m0, s26, 122880\ns_nop 0\nbuffer_load_dwordx4 v230, s[40:43], s53 offen lds\n.Lw_21_%=:\ns_lshl_b32 s53, s52, 19\ns_add_u32 m0, s31, 75776\n"
"s_nop 0\nbuffer_load_dwordx4 v231, s[44:47], s53 offen lds\nbuffer_load_dwordx4 v232, s[44:47], s53 offen offset:1024 lds\ns_lshl_b32 s53, s52, 19\n"
"s_add_u32 m0, s31, 83968\ns_add_u32 s53, s53, 262144\nbuffer_load_dwordx4 v231, s[44:47], s53 offen lds\n"
"buffer_load_dwordx4 v232, s[44:47], s53 offen offset:1024 lds\nW 0\ns_or_b32 s34, s69, 1073758208\ns_add_u32 s34, s34, 1\n"
"s_and_b32 s34, s34, 3221209087\ns_waitcnt vmcnt(8)\nW 0\ns_barrier\nT 136,222,8192\nT 138,223,8192\nR 144,220,36864\nT 140,224,8192\nT 142,225,8192\n"
"R 148,220,38912\nR 152,220,40960\nR 156,220,43008\nT 164,222,16384\nT 166,223,16384\nR 172,221,36864\nT 168,224,16384\nT 170,225,16384\n"
"R 176,221,38912\nR 180,221,40960\nR 184,221,43008\nW 13\nv_mfma_f32_16x16x32_bf16 v[96:99], v[136:139], v[144:147], 0\ns_add_u32 s52, s23, 2\n"
"s_cmp_ge_u32 s52, 10\ns_cselect_b32 s26, 10, 0\ns_sub_u32 s52, s52, s26\ns_lshl_b32 s26, s31, 1\ns_add_u32 m0, s26, 32768\ns_lshl_b32 s53, s52, 7\n"
"buffer_load_dwordx4 v226, s[40:43], s53 offen lds\nW 11\nv_mfma_f32_16x16x32_bf16 v[112:115], v[140:143], v[144:147], 0\n"
"buffer_load_dwordx4 v227, s[40:43], s53 offen offset:1024 lds\nW 10\nv_mfma_f32_16x16x32_bf16 v[116:119], v[140:143], v[148:151], 0\n"
"buffer_load_dwordx4 v228, s[40:43], s53 offen offset:2048 lds\nv_mfma_f32_16x16x32_bf16 v[100:103], v[136:139], v[148:151], 0\n"
"buffer_load_dwordx4 v229, s[40:43], s53 offen offset:3072 lds\ns_cmp_ge_u32 s19, 6\ns_cbranch_scc0 .Lw_22_%=\ns_lshr_b32 s26, s31, 1\n"
"s_add_u32 m0, s26, 47104\ns_nop 0\nbuffer_load_dwordx4 v230, s[40:43], s53 offen lds\n.Lw_22_%=:\nW 9\n"
"v_mfma_f32_16x16x32_bf16 v[104:107], v[136:139], v[152:155], 0\ns_lshl_b32 s53, s52, 19\ns_add_u32 m0, s31, 92160\ns_nop 0\n"
"buffer_load_dwordx4 v231, s[44:47], s53 offen lds\nv_mfma_f32_16x16x32_bf16 v[120:123], v[140:143], v[152:155], 0\n"
"buffer_load_dwordx4 v232, s[44:47], s53 offen offset:1024 lds\nW 8\nv_mfma_f32_16x16x32_bf16 v[124:127], v[140:143], v[156:159], 0\n"
"s_lshl_b32 s53, s52, 19\ns_add_u32 m0, s31, 100352\ns_add_u32 s53, s53, 262144\nbuffer_load_dwordx4 v231, s[44:47], s53 offen lds\n"
"v_mfma_f32_16x16x32_bf16 v[108:111], v[136:139], v[156:159], 0\nbuffer_load_dwordx4 v232, s[44:47], s53 offen offset:1024 lds\ns_waitcnt vmcnt(8)\nW 0\n"
"s_barrier\nM 96,164,172,96\nT 192,222,24576\nT 194,223,24576\ns_add_u32 s52, s23, 3\ns_cmp_ge_u32 s52, 10\ns_cselect_b32 s26, 10, 0\n"
"s_sub_u32 s52, s52, s26\ns_lshl_b32 s26, s31, 1\ns_add_u32 m0, s26, 126976\ns_lshl_b32 s53, s52, 7\nbuffer_load_dwordx4 v226, s[40:43], s53 offen lds\n"
"M 112,168,172,112\nR 200,220,0\nT 196,224,24576\nbuffer_load_dwordx4 v227, s[40:43], s53 offen offset:1024 lds\nM 116,168,176,116\nT 198,225,24576\n"
"R 204,220,2048\nbuffer_load_dwordx4 v228, s[40:43], s53 offen offset:2048 lds\nM 100,164,176,100\nR 208,220,4096\nR 212,220,6144\n"
"buffer_load_dwordx4 v229, s[40:43], s53 offen offset:3072 lds\ns_cmp_ge_u32 s19, 6\ns_cbranch_scc0 .Lw_23_%=\ns_lshr_b32 s26, s31, 1\n"
"s_add_u32 m0, s26, 141312\ns_nop 0\nbuffer_load_dwordx4 v230, s[40:43], s53 offen lds\n.Lw_23_%=:\nM 104,164,180,104\nT 136,222,32768\nT 138,223,32768\n"
"s_lshl_b32 s53, s52, 19\ns_add_u32 m0, s31, 51200\ns_nop 0\nbuffer_load_dwordx4 v231, s[44:47], s53 offen lds\nM 120,168,180,120\nR 144,221,0\n"
"T 140,224,32768\nbuffer_load_dwordx4 v232, s[44:47], s53 offen offset:1024 lds\nM 124,168,184,124\nT 142,225,32768\nR 148,221,2048\n"
"s_lshl_b32 s53, s52, 19\ns_add_u32 m0, s31, 59392\ns_add_u32 s53, s53, 262144\nbuffer_load_dwordx4 v231, s[44:47], s53 offen lds\nM 108,164,184,108\n"
"R 152,221,4096\nR 156,221,6144\nbuffer_load_dwordx4 v232, s[44:47], s53 offen offset:1024 lds\ns_waitcnt vmcnt(8)\nW 0\ns_barrier\nM 96,192,200,96\n"
"T 164,222,40960\nT 166,223,40960\ns_add_u32 s52, s23, 4\ns_cmp_ge_u32 s52, 10\ns_cselect_b32 s26, 10, 0\ns_sub_u32 s52, s52, s26\n"
"s_lshl_b32 s26, s31, 1\ns_add_u32 m0, s26, 145408\ns_lshl_b32 s53, s52, 7\nbuffer_load_dwordx4 v226, s[40:43], s53 offen lds\nM 112,196,200,112\n"
"R 172,94,0\nT 168,224,40960\nbuffer_load_dwordx4 v227, s[40:43], s53 offen offset:1024 lds\nM 116,196,204,116\nT 170,225,40960\nR 176,94,2048\n"
"buffer_load_dwordx4 v228, s[40:43], s53 offen offset:2048 lds\nM 100,192,204,100\nR 180,94,4096\nR 184,94,6144\n"
"buffer_load_dwordx4 v229, s[40:43], s53 offen offset:3072 lds\ns_cmp_ge_u32 s19, 6\ns_cbranch_scc0 .Lw_24_%=\ns_lshr_b32 s26, s31, 1\n"
"s_add_u32 m0, s26, 159744\ns_nop 0\nbuffer_load_dwordx4 v230, s[40:43], s53 offen lds\n.Lw_24_%=:\nM 104,192,208,104\ns_lshl_b32 s53, s52, 19\n"
"s_add_u32 m0, s31, 67584\ns_nop 0\nbuffer_load_dwordx4 v231, s[44:47], s53 offen lds\nM 120,196,208,120\n"
"buffer_load_dwordx4 v232, s[44:47], s53 offen offset:1024 lds\nM 124,196,212,124\ns_lshl_b32 s53, s52, 19\ns_add_u32 m0, s31, 75776\n"
"s_add_u32 s53, s53, 262144\nbuffer_load_dwordx4 v231, s[44:47], s53 offen lds\nM 108,192,212,108\n"
"buffer_load_dwordx4 v232, s[44:47], s53 offen offset:1024 lds\nM 96,136,144,96\nT 192,222,49152\nT 194,223,49152\ns_add_u32 s52, s23, 5\n"
"s_cmp_ge_u32 s52, 10\ns_cselect_b32 s26, 10, 0\ns_sub_u32 s52, s52, s26\ns_lshl_b32 s26, s31, 1\ns_add_u32 m0, s26, 108544\ns_lshl_b32 s53, s52, 7\n"
"buffer_load_dwordx4 v226, s[40:43], s53 offen lds\nM 112,140,144,112\nR 200,95,0\nT 196,224,49152\n"
"buffer_load_dwordx4 v227, s[40:43], s53 offen offset:1024 lds\nM 116,140,148,116\nT 198,225,49152\nR 204,95,2048\n"
"buffer_load_dwordx4 v228, s[40:43], s53 offen offset:2048 lds\nM 100,136,148,100\nR 208,95,4096\nR 212,95,6144\n"
"buffer_load_dwordx4 v229, s[40:43], s53 offen offset:3072 lds\ns_cmp_ge_u32 s19, 6\ns_cbranch_scc0 .Lw_25_%=\ns_lshr_b32 s26, s31, 1\n"
"s_add_u32 m0, s26, 122880\ns_nop 0\nbuffer_load_dwordx4 v230, s[40:43], s53 offen lds\n.Lw_25_%=:\nM 104,136,152,104\ns_lshl_b32 s53, s52, 19\n"
"s_add_u32 m0, s31, 83968\ns_nop 0\nbuffer_load_dwordx4 v231, s[44:47], s53 offen lds\nM 120,140,152,120\n"
"buffer_load_dwordx4 v232, s[44:47], s53 offen offset:1024 lds\nM 124,140,156,124\nM 108,136,156,108\ns_waitcnt vmcnt(14)\nW 0\ns_barrier\n"
"M 96,164,172,96\nT 136,222,0\nT 138,223,0\ns_bitcmp1_b32 s21, 0\ns_cbranch_scc0 .Lw_26_%=\ns_xor_b32 s53, s55, 0\ns_add_u32 m0, s53, 0\n"
"s_add_u32 s53, s53, 0\ns_xor_b32 s53, s53, s95\nbuffer_load_dwordx4 v247, s[60:63], s53 offen nt lds\n"
"buffer_load_dwordx4 v248, s[60:63], s53 offen offset:1024 nt lds\nbuffer_load_dwordx4 v249, s[60:63], s53 offen offset:2048 nt lds\n"
"buffer_load_dwordx4 v250, s[60:63], s53 offen offset:3072 nt lds\n.Lw_26_%=:\nM 112,168,172,112\nR 144,220,18432\nT 140,224,0\ns_bitcmp1_b32 s21, 0\n"
"s_cbranch_scc0 .Lw_27_%=\ns_xor_b32 s53, s55, 4096\ns_add_u32 m0, s53, 0\ns_add_u32 s53, s53, 0\ns_xor_b32 s53, s53, s95\n"
"buffer_load_dwordx4 v247, s[60:63], s53 offen nt lds\nbuffer_load_dwordx4 v248, s[60:63], s53 offen offset:1024 nt lds\n"
"buffer_load_dwordx4 v249, s[60:63], s53 offen offset:2048 nt lds\nbuffer_load_dwordx4 v250, s[60:63], s53 offen offset:3072 nt lds\n.Lw_27_%=:\n"
"M 116,168,176,116\nT 142,225,0\nR 148,220,20480\ns_lshl_b32 s53, s52, 19\ns_add_u32 m0, s31, 92160\ns_add_u32 s53, s53, 262144\n"
"buffer_load_dwordx4 v231, s[44:47], s53 offen lds\nM 100,164,176,100\nR 152,220,22528\nR 156,220,24576\n"
"buffer_load_dwordx4 v232, s[44:47], s53 offen offset:1024 lds\nM 104,164,180,104\ns_add_u32 s52, s23, 6\ns_cmp_ge_u32 s52, 10\n"
"s_cselect_b32 s26, 10, 0\ns_sub_u32 s52, s52, s26\ns_lshl_b32 s26, s31, 1\ns_add_u32 m0, s26, 32768\ns_lshl_b32 s53, s52, 7\n"
"buffer_load_dwordx4 v226, s[40:43], s53 offen lds\nM 120,168,180,120\nbuffer_load_dwordx4 v227, s[40:43], s53 offen offset:1024 lds\nM 124,168,184,124\n"
"buffer_load_dwordx4 v228, s[40:43], s53 offen offset:2048 lds\nM 108,164,184,108\nbuffer_load_dwordx4 v229, s[40:43], s53 offen offset:3072 lds\n"
"s_cmp_ge_u32 s19, 6\ns_cbranch_scc0 .Lw_28_%=\ns_lshr_b32 s26, s31, 1\ns_add_u32 m0, s26, 47104\ns_nop 0\n"
"buffer_load_dwordx4 v230, s[40:43], s53 offen lds\n.Lw_28_%=:\nM 96,192,200,96\nT 164,222,8192\nT 166,223,8192\ns_lshl_b32 s53, s52, 19\n"
"s_add_u32 m0, s31, 100352\ns_nop 0\nbuffer_load_dwordx4 v231, s[44:47], s53 offen lds\nM 112,196,200,112\nR 172,221,18432\nT 168,224,8192\n"
"buffer_load_dwordx4 v232, s[44:47], s53 offen offset:1024 lds\nM 116,196,204,116\nT 170,225,8192\nR 176,221,20480\nM 100,192,204,100\nR 180,221,22528\n"
"R 184,221,24576\nM 104,192,208,104\nM 120,196,208,120\nM 124,196,212,124\nM 108,192,212,108\ns_waitcnt vmcnt(14)\nW 0\ns_barrier\nM 96,136,144,96\n"
"T 192,222,16384\nT 194,223,16384\ns_lshl_b32 s53, s52, 19\ns_add_u32 m0, s31, 51200\ns_add_u32 s53, s53, 262144\n"
"buffer_load_dwordx4 v231, s[44:47], s53 offen lds\nM 112,140,144,112\nR 200,220,36864\nT 196,224,16384\n"
"buffer_load_dwordx4 v232, s[44:47], s53 offen offset:1024 lds\nM 116,140,148,116\nT 198,225,16384\nR 204,220,38912\ns_add_u32 s52, s23, 7\n"
"s_cmp_ge_u32 s52, 10\ns_cselect_b32 s26, 10, 0\ns_sub_u32 s52, s52, s26\ns_lshl_b32 s26, s31, 1\ns_add_u32 m0, s26, 126976\ns_lshl_b32 s53, s52, 7\n"
"buffer_load_dwordx4 v226, s[40:43], s53 offen lds\nM 100,136,148,100\nR 208,220,40960\nR 212,220,43008\n"
"buffer_load_dwordx4 v227, s[40:43], s53 offen offset:1024 lds\nM 104,136,152,104\nbuffer_load_dwordx4 v228, s[40:43], s53 offen offset:2048 lds\n"
"M 120,140,152,120\nbuffer_load_dwordx4 v229, s[40:43], s53 offen offset:3072 lds\ns_cmp_ge_u32 s19, 6\ns_cbranch_scc0 .Lw_29_%=\n"
"s_lshr_b32 s26, s31, 1\ns_add_u32 m0, s26, 141312\ns_nop 0\nbuffer_load_dwordx4 v230, s[40:43], s53 offen lds\n.Lw_29_%=:\nM 124,140,156,124\n"
"s_lshl_b32 s53, s52, 19\ns_add_u32 m0, s31, 59392\ns_nop 0\nbuffer_load_dwordx4 v231, s[44:47], s53 offen lds\nM 108,136,156,108\n"
"buffer_load_dwordx4 v232, s[44:47], s53 offen offset:1024 lds\nM 96,164,172,96\nT 136,222,24576\nT 138,223,24576\nM 112,168,172,112\nR 144,221,36864\n"
"T 140,224,24576\nM 116,168,176,116\nT 142,225,24576\nR 148,221,38912\nM 100,164,176,100\nR 152,221,40960\nR 156,221,43008\nM 104,164,180,104\n"
"M 120,168,180,120\nM 124,168,184,124\nM 108,164,184,108\ns_waitcnt vmcnt(14)\nW 0\ns_barrier\nM 96,192,200,96\nT 164,222,32768\nT 166,223,32768\n"
"s_lshl_b32 s53, s52, 19\ns_add_u32 m0, s31, 67584\ns_add_u32 s53, s53, 262144\nbuffer_load_dwordx4 v231, s[44:47], s53 offen lds\nM 112,196,200,112\n"
"R 172,220,0\nT 168,224,32768\nbuffer_load_dwordx4 v232, s[44:47], s53 offen offset:1024 lds\nM 116,196,204,116\nT 170,225,32768\nR 176,220,2048\n"
"s_add_u32 s52, s23, 8\ns_cmp_ge_u32 s52, 10\ns_cselect_b32 s26, 10, 0\ns_sub_u32 s52, s52, s26\ns_lshl_b32 s26, s31, 1\ns_add_u32 m0, s26, 145408\n"
"s_lshl_b32 s53, s52, 7\nbuffer_load_dwordx4 v226, s[40:43], s53 offen lds\nM 100,192,204,100\nR 180,220,4096\nR 184,220,6144\n"
"buffer_load_dwordx4 v227, s[40:43], s53 offen offset:1024 lds\nM 104,192,208,104\nbuffer_load_dwordx4 v228, s[40:43], s53 offen offset:2048 lds\n"
"M 120,196,208,120\nbuffer_load_dwordx4 v229, s[40:43], s53 offen offset:3072 lds\ns_cmp_ge_u32 s19, 6\ns_cbranch_scc0 .Lw_30_%=\n"
"s_lshr_b32 s26, s31, 1\ns_add_u32 m0, s26, 159744\ns_nop 0\nbuffer_load_dwordx4 v230, s[40:43], s53 offen lds\n.Lw_30_%=:\nM 124,196,212,124\n"
"s_lshl_b32 s53, s52, 19\ns_add_u32 m0, s31, 75776\ns_nop 0\nbuffer_load_dwordx4 v231, s[44:47], s53 offen lds\nM 108,192,212,108\n"
"buffer_load_dwordx4 v232, s[44:47], s53 offen offset:1024 lds\nM 96,136,144,96\nT 192,222,40960\nT 194,223,40960\nM 112,140,144,112\nR 200,221,0\n"
"T 196,224,40960\nM 116,140,148,116\nT 198,225,40960\nR 204,221,2048\nM 100,136,148,100\nR 208,221,4096\nR 212,221,6144\nM 104,136,152,104\n"
"M 120,140,152,120\nM 124,140,156,124\nM 108,136,156,108\ns_waitcnt vmcnt(14)\nW 0\ns_barrier\nM 96,164,172,96\nT 136,222,49152\nT 138,223,49152\n"
"s_lshl_b32 s53, s52, 19\ns_add_u32 m0, s31, 83968\ns_add_u32 s53, s53, 262144\nbuffer_load_dwordx4 v231, s[44:47], s53 offen lds\nM 112,168,172,112\n"
"R 144,94,0\nT 140,224,49152\nbuffer_load_dwordx4 v232, s[44:47], s53 offen offset:1024 lds\nM 116,168,176,116\nT 142,225,49152\nR 148,94,2048\n"
"s_add_u32 s52, s23, 9\ns_cmp_ge_u32 s52, 10\ns_cselect_b32 s26, 10, 0\ns_sub_u32 s52, s52, s26\ns_lshl_b32 s26, s31, 1\ns_add_u32 m0, s26, 108544\n"
"s_lshl_b32 s53, s52, 7\nbuffer_load_dwordx4 v226, s[40:43], s53 offen lds\nM 100,164,176,100\nR 152,94,4096\nR 156,94,6144\n"
"buffer_load_dwordx4 v227, s[40:43], s53 offen offset:1024 lds\nM 104,164,180,104\nbuffer_load_dwordx4 v228, s[40:43], s53 offen offset:2048 lds\n"
"M 120,168,180,120\nbuffer_load_dwordx4 v229, s[40:43], s53 offen offset:3072 lds\ns_cmp_ge_u32 s19, 6\ns_cbranch_scc0 .Lw_31_%=\n"
"s_lshr_b32 s26, s31, 1\ns_add_u32 m0, s26, 122880\ns_nop 0\nbuffer_load_dwordx4 v230, s[40:43], s53 offen lds\n.Lw_31_%=:\nM 124,168,184,124\n"
"s_lshl_b32 s53, s52, 19\ns_add_u32 m0, s31, 92160\ns_nop 0\nbuffer_load_dwordx4 v231, s[44:47], s53 offen lds\nM 108,164,184,108\n"
"buffer_load_dwordx4 v232, s[44:47], s53 offen offset:1024 lds\nM 96,192,200,96\nT 164,222,0\nT 166,223,0\nM 112,196,200,112\nR 172,95,0\nT 168,224,0\n"
"M 116,196,204,116\nT 170,225,0\nR 176,95,2048\nM 100,192,204,100\nR 180,95,4096\nR 184,95,6144\nM 104,192,208,104\nM 120,196,208,120\n"
"M 124,196,212,124\nM 108,192,212,108\ns_waitcnt vmcnt(14)\nW 0\ns_barrier\nM 96,136,144,96\nT 192,222,8192\nT 194,223,8192\ns_lshl_b32 s53, s52, 19\n"
"s_add_u32 m0, s31, 100352\ns_add_u32 s53, s53, 262144\nbuffer_load_dwordx4 v231, s[44:47], s53 offen lds\nM 112,140,144,112\nR 200,220,18432\n"
"T 196,224,8192\nbuffer_load_dwordx4 v232, s[44:47], s53 offen offset:1024 lds\nM 116,140,148,116\nT 198,225,8192\nR 204,220,20480\n"
"s_bitcmp0_b32 s21, 0\ns_cbranch_scc0 .Lw_32_%=\ns_xor_b32 s53, s55, 0\ns_add_u32 m0, s53, 0\ns_add_u32 s53, s53, 0\ns_xor_b32 s53, s53, s95\n"
"buffer_load_dwordx4 v247, s[60:63], s53 offen nt lds\nbuffer_load_dwordx4 v248, s[60:63], s53 offen offset:1024 nt lds\n"
"buffer_load_dwordx4 v249, s[60:63], s53 offen offset:2048 nt lds\nbuffer_load_dwordx4 v250, s[60:63], s53 offen offset:3072 nt lds\n.Lw_32_%=:\n"
"M 100,136,148,100\nR 208,220,22528\nR 212,220,24576\ns_bitcmp0_b32 s21, 0\ns_cbranch_scc0 .Lw_33_%=\ns_xor_b32 s53, s55, 4096\ns_add_u32 m0, s53, 0\n"
"s_add_u32 s53, s53, 0\ns_xor_b32 s53, s53, s95\nbuffer_load_dwordx4 v247, s[60:63], s53 offen nt lds\n"
"buffer_load_dwordx4 v248, s[60:63], s53 offen offset:1024 nt lds\nbuffer_load_dwordx4 v249, s[60:63], s53 offen offset:2048 nt lds\n"
"buffer_load_dwordx4 v250, s[60:63], s53 offen offset:3072 nt lds\n.Lw_33_%=:\nM 104,136,152,104\nM 120,140,152,120\nM 124,140,156,124\n"
"M 108,136,156,108\nM 96,164,172,96\nT 136,222,16384\nT 138,223,16384\nM 112,168,172,112\nR 144,221,18432\nT 140,224,16384\nM 116,168,176,116\n"
"T 142,225,16384\nR 148,221,20480\nM 100,164,176,100\nR 152,221,22528\nR 156,221,24576\nM 104,164,180,104\nM 120,168,180,120\nM 124,168,184,124\n"
"M 108,164,184,108\ns_bitcmp1_b32 s21, 0\ns_cbranch_scc0 .Lw_34_%=\ns_waitcnt vmcnt(8)\n.Lw_34_%=:\ns_waitcnt vmcnt(16)\nW 0\ns_barrier\n"
"M 96,192,200,96\nT 164,222,24576\nT 166,223,24576\nM 112,196,200,112\nR 172,220,36864\nT 168,224,24576\nM 116,196,204,116\nT 170,225,24576\n"
"R 176,220,38912\nM 100,192,204,100\nR 180,220,40960\nR 184,220,43008\nM 104,192,208,104\nM 120,196,208,120\nM 124,196,212,124\nM 108,192,212,108\n"
"M 96,136,144,96\nT 192,222,32768\nT 194,223,32768\ns_xor_b32 s53, s55, 0\ns_add_u32 m0, s53, 32768\ns_add_u32 s53, s53, 0\ns_xor_b32 s53, s53, s95\n"
"buffer_load_dwordx4 v247, s[36:39], s53 offen nt lds\nbuffer_load_dwordx4 v248, s[36:39], s53 offen offset:1024 nt lds\n"
"buffer_load_dwordx4 v249, s[36:39], s53 offen offset:2048 nt lds\nbuffer_load_dwordx4 v250, s[36:39], s53 offen offset:3072 nt lds\nM 112,140,144,112\n"
"R 200,221,36864\nT 196,224,32768\nM 116,140,148,116\nT 198,225,32768\nR 204,221,38912\nM 100,136,148,100\nR 208,221,40960\nR 212,221,43008\n"
"M 104,136,152,104\nM 120,140,152,120\nM 124,140,156,124\nM 108,136,156,108\ns_bitcmp1_b32 s21, 0\ns_cbranch_scc0 .Lw_35_%=\ns_waitcnt vmcnt(4)\n"
".Lw_35_%=:\ns_waitcnt vmcnt(12)\nW 0\ns_barrier\nM 96,164,172,96\nT 136,222,40960\nT 138,223,40960\nM 112,168,172,112\nR 144,220,0\nT 140,224,40960\n"
"M 116,168,176,116\nT 142,225,40960\nR 148,220,2048\nM 100,164,176,100\nR 152,220,4096\nR 156,220,6144\nM 104,164,180,104\nM 120,168,180,120\n"
"M 124,168,184,124\nM 108,164,184,108\nM 96,192,200,96\nT 164,222,49152\nT 166,223,49152\nM 112,196,200,112\nR 172,221,0\nT 168,224,49152\n"
"M 116,196,204,116\nT 170,225,49152\nR 176,221,2048\nM 100,192,204,100\nR 180,221,4096\nR 184,221,6144\nM 104,192,208,104\nM 120,196,208,120\n"
"M 124,196,212,124\nM 108,192,212,108\nW 13\nM 96,136,144,96\ns_xor_b32 s53, s55, 4096\ns_add_u32 m0, s53, 32768\ns_add_u32 s53, s53, 0\n"
"s_xor_b32 s53, s53, s95\nbuffer_load_dwordx4 v247, s[36:39], s53 offen nt lds\nbuffer_load_dwordx4 v248, s[36:39], s53 offen offset:1024 nt lds\n"
"buffer_load_dwordx4 v249, s[36:39], s53 offen offset:2048 nt lds\nbuffer_load_dwordx4 v250, s[36:39], s53 offen offset:3072 nt lds\nW 11\n"
"M 112,140,144,112\nW 10\nM 116,140,148,116\nM 100,136,148,100\nW 9\nM 104,136,152,104\nM 120,140,152,120\nW 8\nM 124,140,156,124\nM 108,136,156,108\n"
"W 5\nM 96,164,172,96\nW 3\nM 112,168,172,112\nW 2\nM 116,168,176,116\nM 100,164,176,100\nW 1\nM 104,164,180,104\nM 120,168,180,120\n"
"v_pk_mul_f32 v[96:97], v[96:97], v[252:253]\nv_pk_mul_f32 v[98:99], v[98:99], v[252:253]\nC 96,96,97\nC 97,98,99\nds_write_b64 v233, v[96:97]\n"
"v_pk_mul_f32 v[112:113], v[112:113], v[252:253]\nv_pk_mul_f32 v[114:115], v[114:115], v[252:253]\nC 112,112,113\nC 113,114,115\n"
"ds_write_b64 v234, v[112:113]\nW 2\nM 124,168,184,124\nM 108,164,184,108\nv_pk_mul_f32 v[100:101], v[100:101], v[252:253]\n"
"v_pk_mul_f32 v[102:103], v[102:103], v[252:253]\nC 100,100,101\nC 101,102,103\nds_write_b64 v233, v[100:101] offset:4096\n"
"v_pk_mul_f32 v[116:117], v[116:117], v[252:253]\nv_pk_mul_f32 v[118:119], v[118:119], v[252:253]\nC 116,116,117\nC 117,118,119\n"
"ds_write_b64 v234, v[116:117] offset:4096\nv_pk_mul_f32 v[104:105], v[104:105], v[252:253]\nv_pk_mul_f32 v[106:107], v[106:107], v[252:253]\n"
"C 104,104,105\nC 105,106,107\nds_write_b64 v233, v[104:105] offset:8192\nv_pk_mul_f32 v[120:121], v[120:121], v[252:253]\n"
"v_pk_mul_f32 v[122:123], v[122:123], v[252:253]\nC 120,120,121\nC 121,122,123\nds_write_b64 v234, v[120:121] offset:8192\n"
"v_pk_mul_f32 v[108:109], v[108:109], v[252:253]\nv_pk_mul_f32 v[110:111], v[110:111], v[252:253]\nC 108,108,109\nC 109,110,111\n"
"ds_write_b64 v233, v[108:109] offset:12288\nv_pk_mul_f32 v[124:125], v[124:125], v[252:253]\nv_pk_mul_f32 v[126:127], v[126:127], v[252:253]\n"
"C 124,124,125\nC 125,126,127\nds_write_b64 v234, v[124:125] offset:12288\ns_setprio 0\ns_waitcnt vmcnt(8)\nW 0\ns_barrier\nR 144,239,0\nR 96,235,0\n"
"R 112,235,4096\nR 128,235,8192\nR 148,239,512\nR 100,236,0\nR 116,236,4096\nR 132,236,8192\nR 152,239,1024\nR 104,237,0\nR 120,237,4096\n"
"R 136,237,8192\nW 10\nv_mfma_f32_16x16x32_bf16 v[168:171], v[144:147], v[96:99], 0\nW 9\n"
"v_mfma_f32_16x16x32_bf16 v[172:175], v[144:147], v[112:115], 0\nW 8\nv_mfma_f32_16x16x32_bf16 v[176:179], v[144:147], v[128:131], 0\nR 156,239,1536\n"
"R 108,238,0\nR 124,238,4096\nR 140,238,8192\nW 10\nM 168,148,100,168\nW 9\nM 172,148,116,172\nW 8\nM 176,148,132,176\nW 6\nM 168,152,104,168\nW 5\n"
"M 172,152,120,172\nW 4\nM 176,152,136,176\nW 0\ns_barrier\nM 168,156,108,168\nM 172,156,124,172\nM 176,156,140,176\ns_cmp_lg_u32 s35, 0\n"
"s_cbranch_scc1 .Lmx_13_%=\n.Lmr1_15_%=:\ns_bitcmp1_b32 s19, 1\ns_cbranch_scc1 .Lv3_8_%=\ns_setprio 1\nR 192,244,0\nR 196,244,256\nR 200,244,512\n"
"R 204,244,768\ns_bitcmp0_b32 s21, 0\ns_cbranch_scc0 .Lw_36_%=\ns_xor_b32 s53, s30, 0\ns_add_u32 m0, s53, 65536\ns_add_u32 s53, s53, 32768\n"
"s_xor_b32 s53, s53, s95\nbuffer_load_dwordx4 v247, s[60:63], s53 offen nt lds\nbuffer_load_dwordx4 v248, s[60:63], s53 offen offset:1024 nt lds\n"
"buffer_load_dwordx4 v249, s[60:63], s53 offen offset:2048 nt lds\nbuffer_load_dwordx4 v250, s[60:63], s53 offen offset:3072 nt lds\n.Lw_36_%=:\n"
"s_bitcmp0_b32 s21, 0\ns_cbranch_scc0 .Lw_37_%=\ns_xor_b32 s53, s30, 4096\ns_add_u32 m0, s53, 65536\ns_add_u32 s53, s53, 32768\ns_xor_b32 s53, s53, s95\n"
"buffer_load_dwordx4 v247, s[60:63], s53 offen nt lds\nbuffer_load_dwordx4 v248, s[60:63], s53 offen offset:1024 nt lds\n"
"buffer_load_dwordx4 v249, s[60:63], s53 offen offset:2048 nt lds\nbuffer_load_dwordx4 v250, s[60:63], s53 offen offset:3072 nt lds\n.Lw_37_%=:\n"
"s_bitcmp0_b32 s21, 0\ns_cbranch_scc0 .Lw_38_%=\ns_xor_b32 s53, s30, 8192\ns_add_u32 m0, s53, 65536\ns_add_u32 s53, s53, 32768\ns_xor_b32 s53, s53, s95\n"
"buffer_load_dwordx4 v247, s[60:63], s53 offen nt lds\nbuffer_load_dwordx4 v248, s[60:63], s53 offen offset:1024 nt lds\n"
"buffer_load_dwordx4 v249, s[60:63], s53 offen offset:2048 nt lds\nbuffer_load_dwordx4 v250, s[60:63], s53 offen offset:3072 nt lds\n.Lw_38_%=:\n"
"s_bitcmp0_b32 s21, 0\ns_cbranch_scc0 .Lw_39_%=\ns_xor_b32 s53, s30, 12288\ns_add_u32 m0, s53, 65536\ns_add_u32 s53, s53, 32768\n"
"s_xor_b32 s53, s53, s95\nbuffer_load_dwordx4 v247, s[60:63], s53 offen nt lds\nbuffer_load_dwordx4 v248, s[60:63], s53 offen offset:1024 nt lds\n"
"buffer_load_dwordx4 v249, s[60:63], s53 offen offset:2048 nt lds\nbuffer_load_dwordx4 v250, s[60:63], s53 offen offset:3072 nt lds\n.Lw_39_%=:\n"
"s_bitcmp0_b32 s21, 0\ns_cbranch_scc0 .Lw_40_%=\ns_xor_b32 s53, s30, 0\ns_add_u32 m0, s53, 98304\ns_add_u32 s53, s53, 32768\ns_xor_b32 s53, s53, s95\n"
"buffer_load_dwordx4 v247, s[36:39], s53 offen nt lds\nbuffer_load_dwordx4 v248, s[36:39], s53 offen offset:1024 nt lds\n"
"buffer_load_dwordx4 v249, s[36:39], s53 offen offset:2048 nt lds\nbuffer_load_dwordx4 v250, s[36:39], s53 offen offset:3072 nt lds\n.Lw_40_%=:\n"
"s_bitcmp0_b32 s21, 0\ns_cbranch_scc0 .Lw_41_%=\ns_xor_b32 s53, s30, 4096\ns_add_u32 m0, s53, 98304\ns_add_u32 s53, s53, 32768\ns_xor_b32 s53, s53, s95\n"
"buffer_load_dwordx4 v247, s[36:39], s53 offen nt lds\nbuffer_load_dwordx4 v248, s[36:39], s53 offen offset:1024 nt lds\n"
"buffer_load_dwordx4 v249, s[36:39], s53 offen offset:2048 nt lds\nbuffer_load_dwordx4 v250, s[36:39], s53 offen offset:3072 nt lds\n.Lw_41_%=:\n"
"s_bitcmp0_b32 s21, 0\ns_cbranch_scc0 .Lw_42_%=\ns_xor_b32 s53, s30, 8192\ns_add_u32 m0, s53, 98304\ns_add_u32 s53, s53, 32768\ns_xor_b32 s53, s53, s95\n"
"buffer_load_dwordx4 v247, s[36:39], s53 offen nt lds\nbuffer_load_dwordx4 v248, s[36:39], s53 offen offset:1024 nt lds\n"
"buffer_load_dwordx4 v249, s[36:39], s53 offen offset:2048 nt lds\nbuffer_load_dwordx4 v250, s[36:39], s53 offen offset:3072 nt lds\n.Lw_42_%=:\n"
"s_bitcmp0_b32 s21, 0\ns_cbranch_scc0 .Lw_43_%=\ns_xor_b32 s53, s30, 12288\ns_add_u32 m0, s53, 98304\ns_add_u32 s53, s53, 32768\n"
"s_xor_b32 s53, s53, s95\nbuffer_load_dwordx4 v247, s[36:39], s53 offen nt lds\nbuffer_load_dwordx4 v248, s[36:39], s53 offen offset:1024 nt lds\n"
"buffer_load_dwordx4 v249, s[36:39], s53 offen offset:2048 nt lds\nbuffer_load_dwordx4 v250, s[36:39], s53 offen offset:3072 nt lds\n.Lw_43_%=:\n"
"s_cmp_eq_u32 s35, 0\ns_cbranch_scc0 .Lw_44_%=\nv_max3_f32 v180, v168, v169, v170\nv_max_f32 v180, v180, v171\nv_max3_f32 v181, v172, v173, v174\n"
"v_max_f32 v181, v181, v175\nv_max3_f32 v182, v176, v177, v178\nv_max_f32 v182, v182, v179\nv_mov_b32 v183, v180\nv_mov_b32 v184, v181\n"
"v_mov_b32 v185, v182\nv_permlane16_swap_b32 v180, v183\nv_permlane16_swap_b32 v181, v184\nv_permlane16_swap_b32 v182, v185\nv_max_f32 v180, v180, v183\n"
"v_max_f32 v181, v181, v184\nv_max_f32 v182, v182, v185\nv_mov_b32 v183, v180\nv_mov_b32 v184, v181\nv_mov_b32 v185, v182\n"
"v_permlane32_swap_b32 v180, v183\nv_permlane32_swap_b32 v181, v184\nv_permlane32_swap_b32 v182, v185\nv_max_f32 v180, v180, v183\n"
"v_max_f32 v181, v181, v184\nv_max_f32 v182, v182, v185\nv_sub_f32 v228, 0, v180\nv_sub_f32 v229, 0, v180\nv_sub_f32 v230, 0, v180\n"
"v_sub_f32 v231, 0, v180\nv_sub_f32 v232, 0, v181\nv_sub_f32 v233, 0, v181\nv_sub_f32 v234, 0, v181\nv_sub_f32 v235, 0, v181\nv_sub_f32 v236, 0, v182\n"
"v_sub_f32 v237, 0, v182\nv_sub_f32 v238, 0, v182\nv_sub_f32 v239, 0, v182\n.Lw_44_%=:\nW 3\nM 144,192,96,228\nM 152,192,112,232\nM 160,192,128,236\n"
"R 208,244,1024\ns_xor_b32 s53, s30, 0\ns_add_u32 m0, s53, 131072\ns_add_u32 s53, s53, 65536\ns_xor_b32 s53, s53, s95\n"
"buffer_load_dwordx4 v247, s[60:63], s53 offen nt lds\nbuffer_load_dwordx4 v248, s[60:63], s53 offen offset:1024 nt lds\n"
"buffer_load_dwordx4 v249, s[60:63], s53 offen offset:2048 nt lds\nbuffer_load_dwordx4 v250, s[60:63], s53 offen offset:3072 nt lds\nW 3\n"
"M 164,196,128,236\nM 156,196,112,232\nM 148,196,96,228\nR 192,244,1280\nW 3\nM 144,200,100,144\nM 152,200,116,152\nM 160,200,132,160\nR 196,244,1536\n"
"s_xor_b32 s53, s30, 4096\ns_add_u32 m0, s53, 131072\ns_add_u32 s53, s53, 65536\ns_xor_b32 s53, s53, s95\n"
"buffer_load_dwordx4 v247, s[60:63], s53 offen nt lds\nbuffer_load_dwordx4 v248, s[60:63], s53 offen offset:1024 nt lds\n"
"buffer_load_dwordx4 v249, s[60:63], s53 offen offset:2048 nt lds\nbuffer_load_dwordx4 v250, s[60:63], s53 offen offset:3072 nt lds\nW 3\n"
"M 164,204,132,164\nM 156,204,116,156\nM 148,204,100,148\nR 200,244,1792\nW 3\nM 144,208,104,144\nM 152,208,120,152\nM 160,208,136,160\n"
"s_xor_b32 s53, s30, 8192\ns_add_u32 m0, s53, 131072\ns_add_u32 s53, s53, 65536\ns_xor_b32 s53, s53, s95\n"
"buffer_load_dwordx4 v247, s[60:63], s53 offen nt lds\nbuffer_load_dwordx4 v248, s[60:63], s53 offen offset:1024 nt lds\n"
"buffer_load_dwordx4 v249, s[60:63], s53 offen offset:2048 nt lds\nbuffer_load_dwordx4 v250, s[60:63], s53 offen offset:3072 nt lds\n"
"s_xor_b32 s53, s30, 12288\ns_add_u32 m0, s53, 131072\ns_add_u32 s53, s53, 65536\ns_xor_b32 s53, s53, s95\n"
"buffer_load_dwordx4 v247, s[60:63], s53 offen nt lds\nbuffer_load_dwordx4 v248, s[60:63], s53 offen offset:1024 nt lds\n"
"buffer_load_dwordx4 v249, s[60:63], s53 offen offset:2048 nt lds\nbuffer_load_dwordx4 v250, s[60:63], s53 offen offset:3072 nt lds\n"
"s_bitcmp1_b32 s21, 0\ns_cbranch_scc0 .Lw_45_%=\ns_waitcnt vmcnt(16)\n.Lw_45_%=:\ns_waitcnt vmcnt(32)\nW 0\ns_barrier\nv_add_u32 v244, 0x10000, v244\n"
"R 204,244,0\nM 164,192,136,164\nM 156,192,120,156\nM 148,192,104,148\nR 208,244,256\nM 144,196,108,144\nM 152,196,124,152\nM 160,196,140,160\n"
"R 192,244,512\ns_xor_b32 s53, s30, 0\ns_add_u32 m0, s53, 0\ns_add_u32 s53, s53, 65536\ns_xor_b32 s53, s53, s95\ns_mul_i32 s52, s68, 10\n"
"s_lshr_b32 s52, s52, 3\ns_min_u32 s52, s52, s94\n.Lpace_46_%=:\ns_memrealtime s[96:97]\nW 0\ns_sub_u32 s96, s96, s70\ns_cmp_lt_i32 s96, s52\n"
"s_cbranch_scc0 .Lpz_47_%=\ns_sleep 1\ns_branch .Lpace_46_%=\n.Lpz_47_%=:\ns_sub_u32 s96, s96, 6\ns_max_i32 s96, s96, s52\ns_add_u32 s55, s96, 55\n"
"buffer_load_dwordx4 v247, s[36:39], s53 offen nt lds\nbuffer_load_dwordx4 v248, s[36:39], s53 offen offset:1024 nt lds\n"
"buffer_load_dwordx4 v249, s[36:39], s53 offen offset:2048 nt lds\nbuffer_load_dwordx4 v250, s[36:39], s53 offen offset:3072 nt lds\nM 164,200,140,164\n"
"M 156,200,124,156\nM 148,200,108,148\nR 196,244,768\nM 168,204,96,228\nE 144\nE 145\nM 176,204,112,232\nE 146\nC 144,144,145\nM 184,204,128,236\n"
"R 200,244,1024\nE 147\ns_xor_b32 s53, s30, 4096\ns_add_u32 m0, s53, 0\ns_add_u32 s53, s53, 65536\ns_xor_b32 s53, s53, s95\n"
"buffer_load_dwordx4 v247, s[36:39], s53 offen nt lds\nbuffer_load_dwordx4 v248, s[36:39], s53 offen offset:1024 nt lds\n"
"buffer_load_dwordx4 v249, s[36:39], s53 offen offset:2048 nt lds\nbuffer_load_dwordx4 v250, s[36:39], s53 offen offset:3072 nt lds\nM 188,208,128,236\n"
"E 152\nC 145,146,147\nM 180,208,112,232\nE 153\nE 154\nM 172,208,96,228\nR 204,244,1280\nC 152,152,153\nM 168,192,100,168\nE 155\nE 160\n"
"M 176,192,116,176\nE 161\nC 153,154,155\nM 184,192,132,184\nR 208,244,1536\nC 160,160,161\ns_xor_b32 s53, s30, 8192\ns_add_u32 m0, s53, 0\n"
"s_add_u32 s53, s53, 65536\ns_xor_b32 s53, s53, s95\ns_mul_i32 s52, s68, 11\ns_lshr_b32 s52, s52, 3\ns_min_u32 s52, s52, s94\ns_max_i32 s52, s52, s55\n"
".Lpace_48_%=:\ns_memrealtime s[96:97]\nW 0\ns_sub_u32 s96, s96, s70\ns_cmp_lt_i32 s96, s52\ns_cbranch_scc0 .Lpz_49_%=\ns_sleep 1\n"
"s_branch .Lpace_48_%=\n.Lpz_49_%=:\ns_sub_u32 s96, s96, 6\ns_max_i32 s96, s96, s52\ns_add_u32 s55, s96, 55\n"
"buffer_load_dwordx4 v247, s[36:39], s53 offen nt lds\nbuffer_load_dwordx4 v248, s[36:39], s53 offen offset:1024 nt lds\n"
"buffer_load_dwordx4 v249, s[36:39], s53 offen offset:2048 nt lds\nbuffer_load_dwordx4 v250, s[36:39], s53 offen offset:3072 nt lds\nM 188,196,132,188\n"
"E 162\nE 163\nM 180,196,116,180\nE 148\nC 161,162,163\nM 172,196,100,172\nR 192,244,1792\nE 149\nM 168,200,104,168\nT 196,245,0\nT 198,245,256\n"
"C 146,148,149\nM 176,200,120,176\nE 150\nE 151\nM 184,200,136,184\nC 147,150,151\nE 156\ns_xor_b32 s53, s30, 12288\ns_add_u32 m0, s53, 0\n"
"s_add_u32 s53, s53, 65536\ns_xor_b32 s53, s53, s95\nbuffer_load_dwordx4 v247, s[36:39], s53 offen nt lds\n"
"buffer_load_dwordx4 v248, s[36:39], s53 offen offset:1024 nt lds\nbuffer_load_dwordx4 v249, s[36:39], s53 offen offset:2048 nt lds\n"
"buffer_load_dwordx4 v250, s[36:39], s53 offen offset:3072 nt lds\nM 188,204,136,188\nT 200,246,0\nT 202,246,256\nE 157\nM 180,204,120,180\nE 158\n"
"C 154,156,157\nM 172,204,104,172\nE 159\nM 168,208,108,168\nT 204,245,512\nT 206,245,768\nC 155,158,159\nM 176,208,124,176\nE 164\nE 165\n"
"M 184,208,140,184\nC 162,164,165\nE 166\nW 6\nM 188,192,140,188\nT 208,246,512\nT 210,246,768\nE 167\nM 180,192,124,180\nC 163,166,167\n"
"M 172,192,108,172\nW 6\nv_mfma_f32_16x16x32_bf16 v[0:3], v[196:199], v[144:147], 0\nT 192,245,1024\nT 194,245,1280\nE 168\n"
"v_mfma_f32_16x16x32_bf16 v[212:215], v[240:243], v[144:147], 0\nE 169\nE 170\nv_mfma_f32_16x16x32_bf16 v[32:35], v[196:199], v[152:155], 0\nE 171\n"
"v_mfma_f32_16x16x32_bf16 v[64:67], v[196:199], v[160:163], 0\nC 168,168,169\nW 6\nv_mfma_f32_16x16x32_bf16 v[68:71], v[200:203], v[160:163], 0\n"
"T 196,246,1024\nT 198,246,1280\nv_mfma_f32_16x16x32_bf16 v[36:39], v[200:203], v[152:155], 0\nC 169,170,171\n"
"v_mfma_f32_16x16x32_bf16 v[216:219], v[240:243], v[152:155], 0\nE 176\nv_mfma_f32_16x16x32_bf16 v[4:7], v[200:203], v[144:147], 0\nW 6\n"
"v_mfma_f32_16x16x32_bf16 v[8:11], v[204:207], v[144:147], 0\nT 200,245,1536\nT 202,245,1792\n"
"v_mfma_f32_16x16x32_bf16 v[40:43], v[204:207], v[152:155], 0\nE 177\nv_mfma_f32_16x16x32_bf16 v[72:75], v[204:207], v[160:163], 0\nE 178\n"
"v_mfma_f32_16x16x32_bf16 v[220:223], v[240:243], v[160:163], 0\nC 176,176,177\nW 6\nv_mfma_f32_16x16x32_bf16 v[76:79], v[208:211], v[160:163], 0\n"
"T 204,246,1536\nT 206,246,1792\nv_mfma_f32_16x16x32_bf16 v[44:47], v[208:211], v[152:155], 0\nE 179\n"
"v_mfma_f32_16x16x32_bf16 v[12:15], v[208:211], v[144:147], 0\nC 177,178,179\nW 6\nv_mfma_f32_16x16x32_bf16 v[16:19], v[192:195], v[144:147], 0\nE 184\n"
"v_mfma_f32_16x16x32_bf16 v[48:51], v[192:195], v[152:155], 0\nE 185\nv_mfma_f32_16x16x32_bf16 v[80:83], v[192:195], v[160:163], 0\nC 184,184,185\n"
"s_waitcnt vmcnt(16)\nW 0\ns_barrier\nv_add_u32 v244, 0x10000, v244\nR 208,244,0\nv_mfma_f32_16x16x32_bf16 v[84:87], v[196:199], v[160:163], 0\nE 186\n"
"v_mfma_f32_16x16x32_bf16 v[52:55], v[196:199], v[152:155], 0\nE 187\nv_mfma_f32_16x16x32_bf16 v[20:23], v[196:199], v[144:147], 0\nR 192,244,256\n"
"v_mfma_f32_16x16x32_bf16 v[24:27], v[200:203], v[144:147], 0\nE 172\nv_mfma_f32_16x16x32_bf16 v[56:59], v[200:203], v[152:155], 0\nE 173\n"
"v_mfma_f32_16x16x32_bf16 v[88:91], v[200:203], v[160:163], 0\nR 196,244,512\ns_xor_b32 s53, s30, 0\ns_add_u32 m0, s53, 32768\n"
"s_add_u32 s53, s53, 98304\ns_xor_b32 s53, s53, s95\ns_mul_i32 s52, s68, 12\ns_lshr_b32 s52, s52, 3\ns_min_u32 s52, s52, s94\ns_max_i32 s52, s52, s55\n"
".Lpace_50_%=:\ns_memrealtime s[96:97]\nW 0\ns_sub_u32 s96, s96, s70\ns_cmp_lt_i32 s96, s52\ns_cbranch_scc0 .Lpz_51_%=\ns_sleep 1\n"
"s_branch .Lpace_50_%=\n.Lpz_51_%=:\ns_sub_u32 s96, s96, 6\ns_max_i32 s96, s96, s52\ns_add_u32 s55, s96, 55\n"
"buffer_load_dwordx4 v247, s[60:63], s53 offen nt lds\nbuffer_load_dwordx4 v248, s[60:63], s53 offen offset:1024 nt lds\n"
"buffer_load_dwordx4 v249, s[60:63], s53 offen offset:2048 nt lds\nbuffer_load_dwordx4 v250, s[60:63], s53 offen offset:3072 nt lds\n"
"s_and_b32 s71, s21, 3\ns_lshl_b32 s71, s71, 1\ns_mul_i32 s28, s58, 160\ns_mul_i32 s27, s59, 1310720\ns_add_u32 s27, s27, s28\ns_add_u32 s44, s12, s27\n"
"s_addc_u32 s45, s13, 0\ns_and_b32 s45, s45, 65535\ns_mov_b32 s46, 0\ns_mov_b32 s47, 131072\n"
"v_mfma_f32_16x16x32_bf16 v[92:95], v[204:207], v[160:163], 0\nE 174\nv_mfma_f32_16x16x32_bf16 v[60:63], v[204:207], v[152:155], 0\nE 175\n"
"v_mfma_f32_16x16x32_bf16 v[28:31], v[204:207], v[144:147], 0\nR 200,244,768\nM 144,208,96,228\nE 180\nM 152,208,112,232\nE 181\nM 160,208,128,236\n"
"R 204,244,1024\ns_xor_b32 s53, s30, 4096\ns_add_u32 m0, s53, 32768\ns_add_u32 s53, s53, 98304\ns_xor_b32 s53, s53, s95\n"
"buffer_load_dwordx4 v247, s[60:63], s53 offen nt lds\nbuffer_load_dwordx4 v248, s[60:63], s53 offen offset:1024 nt lds\n"
"buffer_load_dwordx4 v249, s[60:63], s53 offen offset:2048 nt lds\nbuffer_load_dwordx4 v250, s[60:63], s53 offen offset:3072 nt lds\n"
"s_sub_u32 s46, 1310720, s28\ns_mul_i32 s31, s20, 5120\ns_mul_i32 s27, s21, 184320\ns_add_u32 s27, s27, s28\ns_add_u32 s40, s14, s27\n"
"s_addc_u32 s41, s15, 0\ns_and_b32 s41, s41, 65535\ns_mov_b32 s42, 0\ns_mov_b32 s43, 131072\ns_sub_u32 s42, 184320, s28\nM 164,192,128,236\nE 182\n"
"M 156,192,112,232\nE 183\nM 148,192,96,228\nR 208,244,1280\nM 144,196,100,144\nE 188\nM 152,196,116,152\nE 189\nM 160,196,132,160\nR 192,244,1536\n"
"s_xor_b32 s53, s30, 8192\ns_add_u32 m0, s53, 32768\ns_add_u32 s53, s53, 98304\ns_xor_b32 s53, s53, s95\ns_mul_i32 s52, s68, 13\ns_lshr_b32 s52, s52, 3\n"
"s_min_u32 s52, s52, s94\ns_max_i32 s52, s52, s55\n.Lpace_52_%=:\ns_memrealtime s[96:97]\nW 0\ns_sub_u32 s96, s96, s70\ns_cmp_lt_i32 s96, s52\n"
"s_cbranch_scc0 .Lpz_53_%=\ns_sleep 1\ns_branch .Lpace_52_%=\n.Lpz_53_%=:\ns_sub_u32 s96, s96, 6\ns_max_i32 s96, s96, s52\ns_add_u32 s55, s96, 55\n"
"buffer_load_dwordx4 v247, s[60:63], s53 offen nt lds\nbuffer_load_dwordx4 v248, s[60:63], s53 offen offset:1024 nt lds\n"
"buffer_load_dwordx4 v249, s[60:63], s53 offen offset:2048 nt lds\nbuffer_load_dwordx4 v250, s[60:63], s53 offen offset:3072 nt lds\n"
"s_add_u32 s48, s16, 0\ns_addc_u32 s49, s17, 0\ns_and_b32 s49, s49, 65535\ns_mov_b32 s50, 67108864\ns_mov_b32 s51, 131072\ns_mul_i32 s64, s56, 36864\n"
"s_and_b32 s26, s56, 4294967288\ns_mul_i32 s65, s26, 36864\ns_lshl_b32 s66, s26, 8\ns_add_u32 s66, s66, 33554432\nM 164,200,132,164\nE 190\n"
"M 156,200,116,156\nE 191\nM 148,200,100,148\nR 196,244,1792\nM 144,204,104,144\nv_add_u32 v245, 0x10000, v245\nv_add_u32 v246, 0x10000, v246\n"
"M 152,204,120,152\nT 200,245,0\nT 202,245,256\nM 160,204,136,160\nC 185,186,187\ns_xor_b32 s53, s30, 12288\ns_add_u32 m0, s53, 32768\n"
"s_add_u32 s53, s53, 98304\ns_xor_b32 s53, s53, s95\nbuffer_load_dwordx4 v247, s[60:63], s53 offen nt lds\n"
"buffer_load_dwordx4 v248, s[60:63], s53 offen offset:1024 nt lds\nbuffer_load_dwordx4 v249, s[60:63], s53 offen offset:2048 nt lds\n"
"buffer_load_dwordx4 v250, s[60:63], s53 offen offset:3072 nt lds\ns_lshl_b32 s26, s21, 3\ns_add_u32 s26, s26, s58\ns_mul_i32 s67, s26, 196608\n"
"s_add_u32 s67, s67, 16777216\ns_mov_b32 s72, 98304\ns_mul_i32 s26, s57, 24576\ns_add_u32 s72, s72, s26\ns_mov_b32 s75, 159808\ns_mul_i32 s26, s57, 192\n"
"s_add_u32 s75, s75, s26\nM 164,208,136,164\nT 204,246,0\nT 206,246,256\nM 156,208,120,156\nC 170,172,173\nC 171,174,175\nM 148,208,104,148\n"
"C 178,180,181\nM 144,192,108,144\nT 208,245,512\nT 210,245,768\nM 152,192,124,152\nC 179,182,183\nC 186,188,189\nM 160,192,140,160\nC 187,190,191\n"
"s_xor_b32 s53, s30, 0\ns_add_u32 m0, s53, 65536\ns_add_u32 s53, s53, 98304\ns_xor_b32 s53, s53, s95\ns_mul_i32 s52, s68, 14\ns_lshr_b32 s52, s52, 3\n"
"s_min_u32 s52, s52, s94\ns_max_i32 s52, s52, s55\n.Lpace_54_%=:\ns_memrealtime s[96:97]\nW 0\ns_sub_u32 s96, s96, s70\ns_cmp_lt_i32 s96, s52\n"
"s_cbranch_scc0 .Lpz_55_%=\ns_sleep 1\ns_branch .Lpace_54_%=\n.Lpz_55_%=:\ns_sub_u32 s96, s96, 6\ns_max_i32 s96, s96, s52\ns_add_u32 s55, s96, 55\n"
"buffer_load_dwordx4 v247, s[36:39], s53 offen nt lds\nbuffer_load_dwordx4 v248, s[36:39], s53 offen offset:1024 nt lds\n"
"buffer_load_dwordx4 v249, s[36:39], s53 offen offset:2048 nt lds\nbuffer_load_dwordx4 v250, s[36:39], s53 offen offset:3072 nt lds\n"
"s_mov_b32 s76, 4096\ns_mul_i32 s26, s57, 12288\ns_add_u32 s76, s76, s26\ns_mov_b32 s73, 159744\ns_mul_i32 s26, s19, 192\ns_add_u32 s73, s73, s26\n"
"s_add_u32 s26, s71, 0\ns_cmp_ge_u32 s26, 7\ns_cselect_b32 s27, -6, 1\ns_add_u32 s26, s26, s27\nM 164,196,140,164\nT 192,246,512\nT 194,246,768\n"
"M 156,196,124,156\nM 148,196,108,148\nM 0,200,168,0\nT 196,245,1024\nT 198,245,1280\nE 144\nM 212,240,168,212\nE 145\nE 146\nM 32,200,176,32\nE 147\n"
"M 64,200,184,64\nC 144,144,145\ns_xor_b32 s53, s30, 4096\ns_add_u32 m0, s53, 65536\ns_add_u32 s53, s53, 98304\ns_xor_b32 s53, s53, s95\n"
"buffer_load_dwordx4 v247, s[36:39], s53 offen nt lds\nbuffer_load_dwordx4 v248, s[36:39], s53 offen offset:1024 nt lds\n"
"buffer_load_dwordx4 v249, s[36:39], s53 offen offset:2048 nt lds\nbuffer_load_dwordx4 v250, s[36:39], s53 offen offset:3072 nt lds\n"
"s_add_u32 s26, s26, s58\ns_and_b32 s26, s26, 7\ns_mul_i32 s5, s26, 36864\ns_add_u32 s5, s5, s65\ns_add_u32 s26, s71, 1\ns_cmp_ge_u32 s26, 7\n"
"s_cselect_b32 s27, -6, 1\ns_add_u32 s26, s26, s27\ns_add_u32 s26, s26, s58\ns_and_b32 s26, s26, 7\nM 68,204,184,68\nT 200,246,1024\nT 202,246,1280\n"
"M 36,204,176,36\nC 145,146,147\nM 216,240,176,216\nE 152\nM 4,204,168,4\nM 8,208,168,8\nT 204,245,1536\nT 206,245,1792\nM 40,208,176,40\nE 153\n"
"M 72,208,184,72\nE 154\nM 220,240,184,220\nC 152,152,153\ns_xor_b32 s53, s30, 8192\ns_add_u32 m0, s53, 65536\ns_add_u32 s53, s53, 98304\n"
"s_xor_b32 s53, s53, s95\ns_mul_i32 s52, s68, 15\ns_lshr_b32 s52, s52, 3\ns_min_u32 s52, s52, s94\ns_max_i32 s52, s52, s55\n.Lpace_56_%=:\n"
"s_memrealtime s[96:97]\nW 0\ns_sub_u32 s96, s96, s70\ns_cmp_lt_i32 s96, s52\ns_cbranch_scc0 .Lpz_57_%=\ns_sleep 1\ns_branch .Lpace_56_%=\n.Lpz_57_%=:\n"
"s_sub_u32 s96, s96, 6\ns_max_i32 s96, s96, s52\ns_add_u32 s55, s96, 55\nbuffer_load_dwordx4 v247, s[36:39], s53 offen nt lds\n"
"buffer_load_dwordx4 v248, s[36:39], s53 offen offset:1024 nt lds\nbuffer_load_dwordx4 v249, s[36:39], s53 offen offset:2048 nt lds\n"
"buffer_load_dwordx4 v250, s[36:39], s53 offen offset:3072 nt lds\ns_mul_i32 s6, s26, 36864\ns_add_u32 s6, s6, s65\ns_add_u32 s26, s71, 2\n"
"s_cmp_ge_u32 s26, 7\ns_cselect_b32 s27, -6, 1\ns_add_u32 s26, s26, s27\ns_add_u32 s26, s26, s58\ns_and_b32 s26, s26, 7\ns_mul_i32 s7, s26, 36864\n"
"s_add_u32 s7, s7, s65\nM 76,192,184,76\nT 208,246,1536\nT 210,246,1792\nM 44,192,176,44\nE 155\nM 12,192,168,12\nC 153,154,155\nM 16,196,168,16\nE 160\n"
"M 48,196,176,48\nE 161\nM 80,196,184,80\nC 160,160,161\ns_xor_b32 s53, s30, 12288\ns_add_u32 m0, s53, 65536\ns_add_u32 s53, s53, 98304\n"
"s_xor_b32 s53, s53, s95\nbuffer_load_dwordx4 v247, s[36:39], s53 offen nt lds\nbuffer_load_dwordx4 v248, s[36:39], s53 offen offset:1024 nt lds\n"
"buffer_load_dwordx4 v249, s[36:39], s53 offen offset:2048 nt lds\nbuffer_load_dwordx4 v250, s[36:39], s53 offen offset:3072 nt lds\n"
"s_add_u32 s26, s71, 3\ns_cmp_ge_u32 s26, 7\ns_cselect_b32 s27, -6, 1\ns_add_u32 s26, s26, s27\ns_add_u32 s26, s26, s58\ns_and_b32 s26, s26, 7\n"
"s_mul_i32 s8, s26, 36864\ns_add_u32 s8, s8, s65\ns_add_u32 s26, s71, 4\ns_cmp_ge_u32 s26, 7\ns_cselect_b32 s27, -6, 1\ns_waitcnt vmcnt(16)\nW 0\n"
"s_barrier\nv_add_u32 v244, 0xfffe8000, v244\nR 192,244,0\nM 84,200,184,84\nE 162\nM 52,200,176,52\nE 163\nM 20,200,168,20\nR 196,244,256\n"
"M 24,204,168,24\nE 148\nM 56,204,176,56\nE 149\nM 88,204,184,88\nR 200,244,512\ns_xor_b32 s53, s30, 0\ns_add_u32 m0, s53, 98304\n"
"s_add_u32 s53, s53, 131072\ns_xor_b32 s53, s53, s95\ns_mul_i32 s52, s68, 16\ns_lshr_b32 s52, s52, 3\ns_min_u32 s52, s52, s94\ns_max_i32 s52, s52, s55\n"
".Lpace_58_%=:\ns_memrealtime s[96:97]\nW 0\ns_sub_u32 s96, s96, s70\ns_cmp_lt_i32 s96, s52\ns_cbranch_scc0 .Lpz_59_%=\ns_sleep 1\n"
"s_branch .Lpace_58_%=\n.Lpz_59_%=:\ns_sub_u32 s96, s96, 6\ns_max_i32 s96, s96, s52\ns_add_u32 s55, s96, 55\n"
"buffer_load_dwordx4 v247, s[60:63], s53 offen nt lds\nbuffer_load_dwordx4 v248, s[60:63], s53 offen offset:1024 nt lds\n"
"buffer_load_dwordx4 v249, s[60:63], s53 offen offset:2048 nt lds\nbuffer_load_dwordx4 v250, s[60:63], s53 offen offset:3072 nt lds\n"
"s_add_u32 s26, s26, s27\ns_add_u32 s26, s26, s58\ns_and_b32 s26, s26, 7\ns_mul_i32 s9, s26, 36864\ns_add_u32 s9, s9, s65\ns_add_u32 s26, s71, 5\n"
"s_cmp_ge_u32 s26, 7\ns_cselect_b32 s27, -6, 1\ns_add_u32 s26, s26, s27\ns_add_u32 s26, s26, s58\nM 92,208,184,92\nE 150\nM 60,208,176,60\nE 151\n"
"M 28,208,168,28\nR 204,244,768\nM 168,192,96,228\nE 156\nM 176,192,112,232\nE 157\nM 184,192,128,236\nR 208,244,1024\ns_xor_b32 s53, s30, 4096\n"
"s_add_u32 m0, s53, 98304\ns_add_u32 s53, s53, 131072\ns_xor_b32 s53, s53, s95\nbuffer_load_dwordx4 v247, s[60:63], s53 offen nt lds\n"
"buffer_load_dwordx4 v248, s[60:63], s53 offen offset:1024 nt lds\nbuffer_load_dwordx4 v249, s[60:63], s53 offen offset:2048 nt lds\n"
"buffer_load_dwordx4 v250, s[60:63], s53 offen offset:3072 nt lds\ns_and_b32 s26, s26, 7\ns_mul_i32 s10, s26, 36864\ns_add_u32 s10, s10, s65\n"
"s_add_u32 s26, s71, 6\ns_cmp_ge_u32 s26, 7\ns_cselect_b32 s27, -6, 1\ns_add_u32 s26, s26, s27\ns_add_u32 s26, s26, s58\ns_and_b32 s26, s26, 7\n"
"s_mul_i32 s11, s26, 36864\nM 188,196,128,236\nE 158\nM 180,196,112,232\nE 159\nM 172,196,96,228\nR 192,244,1280\nM 168,200,100,168\nE 164\n"
"M 176,200,116,176\nE 165\nM 184,200,132,184\nR 196,244,1536\ns_xor_b32 s53, s30, 8192\ns_add_u32 m0, s53, 98304\ns_add_u32 s53, s53, 131072\n"
"s_xor_b32 s53, s53, s95\ns_mul_i32 s52, s68, 17\ns_lshr_b32 s52, s52, 3\ns_min_u32 s52, s52, s94\ns_max_i32 s52, s52, s55\n.Lpace_60_%=:\n"
"s_memrealtime s[96:97]\nW 0\ns_sub_u32 s96, s96, s70\ns_cmp_lt_i32 s96, s52\ns_cbranch_scc0 .Lpz_61_%=\ns_sleep 1\ns_branch .Lpace_60_%=\n.Lpz_61_%=:\n"
"s_sub_u32 s96, s96, 6\ns_max_i32 s96, s96, s52\ns_add_u32 s55, s96, 55\nbuffer_load_dwordx4 v247, s[60:63], s53 offen nt lds\n"
"buffer_load_dwordx4 v248, s[60:63], s53 offen offset:1024 nt lds\nbuffer_load_dwordx4 v249, s[60:63], s53 offen offset:2048 nt lds\n"
"buffer_load_dwordx4 v250, s[60:63], s53 offen offset:3072 nt lds\ns_add_u32 s11, s11, s65\ns_mov_b32 s4, 0\ns_mov_b32 s77, 0\ns_mov_b32 s78, 16384\n"
"s_mul_i32 s26, s57, 6144\ns_add_u32 s78, s78, s26\ns_mov_b32 s79, 32768\ns_mul_i32 s26, s57, 2048\ns_add_u32 s79, s79, s26\ns_mov_b32 s80, 0\n"
"M 188,204,132,188\nE 166\nM 180,204,116,180\nE 167\nM 172,204,100,172\nR 200,244,1792\nM 168,208,104,168\nv_add_u32 v245, 0xfffe8000, v245\n"
"v_add_u32 v246, 0xfffe8000, v246\nM 176,208,120,176\nT 204,245,0\nT 206,245,256\nM 184,208,136,184\nC 161,162,163\ns_xor_b32 s53, s30, 12288\n"
"s_add_u32 m0, s53, 98304\ns_add_u32 s53, s53, 131072\ns_xor_b32 s53, s53, s95\nbuffer_load_dwordx4 v247, s[60:63], s53 offen nt lds\n"
"buffer_load_dwordx4 v248, s[60:63], s53 offen offset:1024 nt lds\nbuffer_load_dwordx4 v249, s[60:63], s53 offen offset:2048 nt lds\n"
"buffer_load_dwordx4 v250, s[60:63], s53 offen offset:3072 nt lds\ns_mul_i32 s26, s57, 10240\ns_add_u32 s80, s80, s26\ns_mul_i32 s28, s59, 12288\n"
"s_lshl_b32 s56, s19, 10\ns_lshl_b32 s29, s28, 2\ns_add_u32 s29, s29, s67\ns_add_u32 s28, s28, s67\ns_xor_b32 s26, s59, 1\ns_mul_i32 s26, s26, 12288\n"
"s_add_u32 s23, s26, s29\nM 188,192,136,188\nT 208,246,0\nT 210,246,256\nM 180,192,120,180\nC 146,148,149\nC 147,150,151\nM 172,192,104,172\n"
"C 154,156,157\nM 168,196,108,168\nT 192,245,512\nT 194,245,768\nM 176,196,124,176\nC 155,158,159\nC 162,164,165\nM 184,196,140,184\nC 163,166,167\n"
"s_xor_b32 s53, s30, 0\ns_add_u32 m0, s53, 131072\ns_add_u32 s53, s53, 131072\ns_xor_b32 s53, s53, s95\ns_mul_i32 s52, s68, 18\ns_lshr_b32 s52, s52, 3\n"
"s_min_u32 s52, s52, s94\ns_max_i32 s52, s52, s55\n.Lpace_62_%=:\ns_memrealtime s[96:97]\nW 0\ns_sub_u32 s96, s96, s70\ns_cmp_lt_i32 s96, s52\n"
"s_cbranch_scc0 .Lpz_63_%=\ns_sleep 1\ns_branch .Lpace_62_%=\n.Lpz_63_%=:\ns_sub_u32 s96, s96, 6\ns_max_i32 s96, s96, s52\ns_add_u32 s55, s96, 55\n"
"buffer_load_dwordx4 v247, s[36:39], s53 offen nt lds\nbuffer_load_dwordx4 v248, s[36:39], s53 offen offset:1024 nt lds\n"
"buffer_load_dwordx4 v249, s[36:39], s53 offen offset:2048 nt lds\nbuffer_load_dwordx4 v250, s[36:39], s53 offen offset:3072 nt lds\n"
"s_lshl_b32 s26, s26, 2\ns_add_u32 s26, s26, s28\ns_add_u32 s12, s26, s56\ns_xor_b32 s26, s59, 2\ns_mul_i32 s26, s26, 12288\ns_add_u32 s24, s26, s29\n"
"s_lshl_b32 s26, s26, 2\ns_add_u32 s26, s26, s28\ns_add_u32 s13, s26, s56\ns_xor_b32 s26, s59, 3\nM 188,200,140,188\nT 196,246,512\nT 198,246,768\n"
"M 180,200,124,180\nM 172,200,108,172\nM 0,204,144,0\nT 200,245,1024\nT 202,245,1280\nE 168\nM 212,240,144,212\nE 169\nE 170\nM 32,204,152,32\nE 171\n"
"M 64,204,160,64\nC 168,168,169\ns_xor_b32 s53, s30, 4096\ns_add_u32 m0, s53, 131072\ns_add_u32 s53, s53, 131072\ns_xor_b32 s53, s53, s95\n"
"buffer_load_dwordx4 v247, s[36:39], s53 offen nt lds\nbuffer_load_dwordx4 v248, s[36:39], s53 offen offset:1024 nt lds\n"
"buffer_load_dwordx4 v249, s[36:39], s53 offen offset:2048 nt lds\nbuffer_load_dwordx4 v250, s[36:39], s53 offen offset:3072 nt lds\n"
"s_mul_i32 s26, s26, 12288\ns_add_u32 s54, s26, s29\ns_lshl_b32 s26, s26, 2\ns_add_u32 s26, s26, s28\ns_add_u32 s14, s26, s56\ns_lshl_b32 s26, s57, 10\n"
"s_add_u32 s26, s26, 9216\ns_mul_i32 s27, s25, 49152\ns_add_u32 s27, s27, s28\ns_add_u32 s15, s26, s27\nM 68,208,160,68\nT 204,246,1024\nT 206,246,1280\n"
"M 36,208,152,36\nC 169,170,171\nM 216,240,152,216\nE 176\nM 4,208,144,4\nM 8,192,144,8\nT 208,245,1536\nT 210,245,1792\nM 40,192,152,40\nE 177\n"
"M 72,192,160,72\nE 178\nM 220,240,160,220\nC 176,176,177\ns_xor_b32 s53, s30, 8192\ns_add_u32 m0, s53, 131072\ns_add_u32 s53, s53, 131072\n"
"s_xor_b32 s53, s53, s95\ns_mul_i32 s52, s68, 19\ns_lshr_b32 s52, s52, 3\ns_min_u32 s52, s52, s94\ns_max_i32 s52, s52, s55\n.Lpace_64_%=:\n"
"s_memrealtime s[96:97]\nW 0\ns_sub_u32 s96, s96, s70\ns_cmp_lt_i32 s96, s52\ns_cbranch_scc0 .Lpz_65_%=\ns_sleep 1\ns_branch .Lpace_64_%=\n.Lpz_65_%=:\n"
"s_sub_u32 s96, s96, 6\ns_max_i32 s96, s96, s52\ns_add_u32 s55, s96, 55\nbuffer_load_dwordx4 v247, s[36:39], s53 offen nt lds\n"
"buffer_load_dwordx4 v248, s[36:39], s53 offen offset:1024 nt lds\nbuffer_load_dwordx4 v249, s[36:39], s53 offen offset:2048 nt lds\n"
"buffer_load_dwordx4 v250, s[36:39], s53 offen offset:3072 nt lds\ns_cmp_eq_u32 s59, s25\ns_cselect_b32 s69, s26, 268435456\ns_cselect_b32 s99, s34, 0\n"
"s_xor_b32 s26, s59, s20\ns_mul_i32 s26, s26, 49152\ns_add_u32 s26, s26, s28\ns_add_u32 s22, s26, 8192\ns_cmp_eq_u32 s20, 0\n"
"s_cselect_b32 s98, 0, 268435456\ns_cselect_b32 s67, s34, 0\nM 76,196,160,76\nT 192,246,1536\nT 194,246,1792\nM 44,196,152,44\nE 179\nM 12,196,144,12\n"
"C 177,178,179\nM 16,200,144,16\nE 184\nM 48,200,152,48\nE 185\nM 80,200,160,80\nC 184,184,185\ns_xor_b32 s53, s30, 12288\ns_add_u32 m0, s53, 131072\n"
"s_add_u32 s53, s53, 131072\ns_xor_b32 s53, s53, s95\nbuffer_load_dwordx4 v247, s[36:39], s53 offen nt lds\n"
"buffer_load_dwordx4 v248, s[36:39], s53 offen offset:1024 nt lds\nbuffer_load_dwordx4 v249, s[36:39], s53 offen offset:2048 nt lds\n"
"buffer_load_dwordx4 v250, s[36:39], s53 offen offset:3072 nt lds\ns_add_u32 s98, s98, 8192\nv_lshrrev_b32 v254, 4, v255\nv_lshrrev_b32 v253, 2, v255\n"
"v_xor_b32 v254, v254, v253\nv_and_b32 v224, 3, v254\nv_cmp_eq_u32 vcc, 0, v224\nv_and_b32 v254, 1, v255\nv_lshlrev_b32 v254, 4, v254\n"
"v_mov_b32 v253, 16256\nv_lshlrev_b32 v225, v254, v253\ns_waitcnt vmcnt(16)\nW 0\ns_barrier\nv_add_u32 v244, 0x10000, v244\nR 196,244,0\n"
"M 84,204,160,84\nE 186\nM 52,204,152,52\nE 187\nM 20,204,144,20\nR 200,244,256\nM 24,208,144,24\nE 172\nM 56,208,152,56\nE 173\nM 88,208,160,88\n"
"R 204,244,512\ns_xor_b32 s53, s30, 0\ns_add_u32 m0, s53, 0\ns_add_u32 s53, s53, 163840\ns_xor_b32 s53, s53, s95\ns_mul_i32 s52, s68, 20\n"
"s_lshr_b32 s52, s52, 3\ns_min_u32 s52, s52, s94\ns_max_i32 s52, s52, s55\n.Lpace_66_%=:\ns_memrealtime s[96:97]\nW 0\ns_sub_u32 s96, s96, s70\n"
"s_cmp_lt_i32 s96, s52\ns_cbranch_scc0 .Lpz_67_%=\ns_sleep 1\ns_branch .Lpace_66_%=\n.Lpz_67_%=:\ns_sub_u32 s96, s96, 6\ns_max_i32 s96, s96, s52\n"
"s_add_u32 s55, s96, 55\nbuffer_load_dwordx4 v247, s[60:63], s53 offen nt lds\nbuffer_load_dwordx4 v248, s[60:63], s53 offen offset:1024 nt lds\n"
"buffer_load_dwordx4 v249, s[60:63], s53 offen offset:2048 nt lds\nbuffer_load_dwordx4 v250, s[60:63], s53 offen offset:3072 nt lds\n"
"v_cndmask_b32 v225, 0, v225, vcc\nv_and_b32 v224, 2, v255\nv_cmp_eq_u32 vcc, 0, v224\ns_nop 1\nv_cndmask_b32 v224, 0, v225, vcc\n"
"v_xor_b32 v225, v225, v224\nv_lshrrev_b32 v254, 2, v255\nv_mul_u32_u24 v254, 160, v254\nv_and_b32 v252, 3, v255\nv_lshlrev_b32 v252, 3, v252\n"
"v_add_u32 v254, v254, v252\nM 92,192,160,92\nE 174\nM 60,192,152,60\nE 175\nM 28,192,144,28\nR 208,244,768\nM 144,196,96,228\nE 180\nM 152,196,112,232\n"
"E 181\nM 160,196,128,236\nR 192,244,1024\ns_xor_b32 s53, s30, 4096\ns_add_u32 m0, s53, 0\ns_add_u32 s53, s53, 163840\ns_xor_b32 s53, s53, s95\n"
"buffer_load_dwordx4 v247, s[60:63], s53 offen nt lds\nbuffer_load_dwordx4 v248, s[60:63], s53 offen offset:1024 nt lds\n"
"buffer_load_dwordx4 v249, s[60:63], s53 offen offset:2048 nt lds\nbuffer_load_dwordx4 v250, s[60:63], s53 offen offset:3072 nt lds\n"
"v_add_u32 v254, s80, v254\nv_add_u32 v253, 36864, v254\nv_add_u32 v227, 5120, v253\nv_and_b32 v254, 15, v255\nv_mul_u32_u24 v254, 1280, v254\n"
"v_lshrrev_b32 v252, 4, v255\nv_lshl_add_u32 v254, v252, 3, v254\nv_lshlrev_b32 v252, 4, v255\nM 164,200,128,236\nE 182\nM 156,200,112,232\nE 183\n"
"M 148,200,96,228\nR 196,244,1280\nM 144,204,100,144\nE 188\nM 152,204,116,152\nE 189\nM 160,204,132,160\nR 200,244,1536\ns_xor_b32 s53, s30, 8192\n"
"s_add_u32 m0, s53, 0\ns_add_u32 s53, s53, 163840\ns_xor_b32 s53, s53, s95\ns_mul_i32 s52, s68, 21\ns_lshr_b32 s52, s52, 3\ns_min_u32 s52, s52, s94\n"
"s_max_i32 s52, s52, s55\n.Lpace_68_%=:\ns_memrealtime s[96:97]\nW 0\ns_sub_u32 s96, s96, s70\ns_cmp_lt_i32 s96, s52\ns_cbranch_scc0 .Lpz_69_%=\n"
"s_sleep 1\ns_branch .Lpace_68_%=\n.Lpz_69_%=:\ns_sub_u32 s96, s96, 6\ns_max_i32 s96, s96, s52\ns_add_u32 s55, s96, 55\n"
"buffer_load_dwordx4 v247, s[60:63], s53 offen nt lds\nbuffer_load_dwordx4 v248, s[60:63], s53 offen offset:1024 nt lds\n"
"buffer_load_dwordx4 v249, s[60:63], s53 offen offset:2048 nt lds\nbuffer_load_dwordx4 v250, s[60:63], s53 offen offset:3072 nt lds\nM 164,208,132,164\n"
"E 190\nM 156,208,116,156\nE 191\nM 148,208,100,148\nR 204,244,1792\nM 144,192,104,144\nv_add_u32 v245, 0x10000, v245\nv_add_u32 v246, 0x10000, v246\n"
"M 152,192,120,152\nT 208,245,0\nT 210,245,256\nM 160,192,136,160\nC 185,186,187\ns_xor_b32 s53, s30, 12288\ns_add_u32 m0, s53, 0\n"
"s_add_u32 s53, s53, 163840\ns_xor_b32 s53, s53, s95\nbuffer_load_dwordx4 v247, s[60:63], s53 offen nt lds\n"
"buffer_load_dwordx4 v248, s[60:63], s53 offen offset:1024 nt lds\nbuffer_load_dwordx4 v249, s[60:63], s53 offen offset:2048 nt lds\n"
"buffer_load_dwordx4 v250, s[60:63], s53 offen offset:3072 nt lds\nM 164,196,136,164\nT 192,246,0\nT 194,246,256\nM 156,196,120,156\nC 170,172,173\n"
"C 171,174,175\nM 148,196,104,148\nC 178,180,181\nM 144,200,108,144\nT 196,245,512\nT 198,245,768\nM 152,200,124,152\nC 179,182,183\nC 186,188,189\n"
"M 160,200,140,160\nC 187,190,191\ns_xor_b32 s53, s30, 0\ns_add_u32 m0, s53, 32768\ns_add_u32 s53, s53, 163840\ns_xor_b32 s53, s53, s95\n"
"s_mul_i32 s52, s68, 22\ns_lshr_b32 s52, s52, 3\ns_min_u32 s52, s52, s94\ns_max_i32 s52, s52, s55\n.Lpace_70_%=:\ns_memrealtime s[96:97]\nW 0\n"
"s_sub_u32 s96, s96, s70\ns_cmp_lt_i32 s96, s52\ns_cbranch_scc0 .Lpz_71_%=\ns_sleep 1\ns_branch .Lpace_70_%=\n.Lpz_71_%=:\ns_sub_u32 s96, s96, 6\n"
"s_max_i32 s96, s96, s52\ns_add_u32 s55, s96, 55\nbuffer_load_dwordx4 v247, s[36:39], s53 offen nt lds\n"
"buffer_load_dwordx4 v248, s[36:39], s53 offen offset:1024 nt lds\nbuffer_load_dwordx4 v249, s[36:39], s53 offen offset:2048 nt lds\n"
"buffer_load_dwordx4 v250, s[36:39], s53 offen offset:3072 nt lds\nM 164,204,140,164\nT 200,246,512\nT 202,246,768\nM 156,204,124,156\n"
"M 148,204,108,148\nM 0,208,168,0\nT 204,245,1024\nT 206,245,1280\nE 144\nM 212,240,168,212\nE 145\nE 146\nM 32,208,176,32\nE 147\nM 64,208,184,64\n"
"C 144,144,145\ns_xor_b32 s53, s30, 4096\ns_add_u32 m0, s53, 32768\ns_add_u32 s53, s53, 163840\ns_xor_b32 s53, s53, s95\n"
"buffer_load_dwordx4 v247, s[36:39], s53 offen nt lds\nbuffer_load_dwordx4 v248, s[36:39], s53 offen offset:1024 nt lds\n"
"buffer_load_dwordx4 v249, s[36:39], s53 offen offset:2048 nt lds\nbuffer_load_dwordx4 v250, s[36:39], s53 offen offset:3072 nt lds\nM 68,192,184,68\n"
"T 208,246,1024\nT 210,246,1280\nM 36,192,176,36\nC 145,146,147\nM 216,240,176,216\nE 152\nM 4,192,168,4\nM 8,196,168,8\nT 192,245,1536\nT 194,245,1792\n"
"M 40,196,176,40\nE 153\nM 72,196,184,72\nE 154\nM 220,240,184,220\nC 152,152,153\ns_xor_b32 s53, s30, 8192\ns_add_u32 m0, s53, 32768\n"
"s_add_u32 s53, s53, 163840\ns_xor_b32 s53, s53, s95\ns_mul_i32 s52, s68, 23\ns_lshr_b32 s52, s52, 3\ns_min_u32 s52, s52, s94\ns_max_i32 s52, s52, s55\n"
".Lpace_72_%=:\ns_memrealtime s[96:97]\nW 0\ns_sub_u32 s96, s96, s70\ns_cmp_lt_i32 s96, s52\ns_cbranch_scc0 .Lpz_73_%=\ns_sleep 1\n"
"s_branch .Lpace_72_%=\n.Lpz_73_%=:\ns_sub_u32 s96, s96, 6\ns_max_i32 s96, s96, s52\ns_add_u32 s55, s96, 55\n"
"buffer_load_dwordx4 v247, s[36:39], s53 offen nt lds\nbuffer_load_dwordx4 v248, s[36:39], s53 offen offset:1024 nt lds\n"
"buffer_load_dwordx4 v249, s[36:39], s53 offen offset:2048 nt lds\nbuffer_load_dwordx4 v250, s[36:39], s53 offen offset:3072 nt lds\nM 76,200,184,76\n"
"T 196,246,1536\nT 198,246,1792\nM 44,200,176,44\nE 155\nM 12,200,168,12\nC 153,154,155\nM 16,204,168,16\nE 160\nM 48,204,176,48\nE 161\n"
"M 80,204,184,80\nC 160,160,161\ns_xor_b32 s53, s30, 12288\ns_add_u32 m0, s53, 32768\ns_add_u32 s53, s53, 163840\ns_xor_b32 s53, s53, s95\n"
"buffer_load_dwordx4 v247, s[36:39], s53 offen nt lds\nbuffer_load_dwordx4 v248, s[36:39], s53 offen offset:1024 nt lds\n"
"buffer_load_dwordx4 v249, s[36:39], s53 offen offset:2048 nt lds\nbuffer_load_dwordx4 v250, s[36:39], s53 offen offset:3072 nt lds\n"
"s_waitcnt vmcnt(16)\nW 0\ns_barrier\nv_add_u32 v244, 0xfffe8000, v244\nR 200,244,0\nM 84,208,184,84\nE 162\nM 52,208,176,52\nE 163\nM 20,208,168,20\n"
"R 204,244,256\nM 24,192,168,24\nE 148\nM 56,192,176,56\nE 149\nM 88,192,184,88\nR 208,244,512\ns_xor_b32 s53, s30, 0\ns_add_u32 m0, s53, 65536\n"
"s_add_u32 s53, s53, 196608\ns_xor_b32 s53, s53, s95\ns_mul_i32 s52, s68, 24\ns_lshr_b32 s52, s52, 3\ns_min_u32 s52, s52, s94\ns_max_i32 s52, s52, s55\n"
".Lpace_74_%=:\ns_memrealtime s[96:97]\nW 0\ns_sub_u32 s96, s96, s70\ns_cmp_lt_i32 s96, s52\ns_cbranch_scc0 .Lpz_75_%=\ns_sleep 1\n"
"s_branch .Lpace_74_%=\n.Lpz_75_%=:\ns_sub_u32 s96, s96, 6\ns_max_i32 s96, s96, s52\ns_add_u32 s55, s96, 55\n"
"buffer_load_dwordx4 v247, s[60:63], s53 offen nt lds\nbuffer_load_dwordx4 v248, s[60:63], s53 offen offset:1024 nt lds\n"
"buffer_load_dwordx4 v249, s[60:63], s53 offen offset:2048 nt lds\nbuffer_load_dwordx4 v250, s[60:63], s53 offen offset:3072 nt lds\nM 92,196,184,92\n"
"E 150\nM 60,196,176,60\nE 151\nM 28,196,168,28\nR 192,244,768\nM 168,200,96,228\nE 156\nM 176,200,112,232\nE 157\nM 184,200,128,236\nR 196,244,1024\n"
"s_xor_b32 s53, s30, 4096\ns_add_u32 m0, s53, 65536\ns_add_u32 s53, s53, 196608\ns_xor_b32 s53, s53, s95\n"
"buffer_load_dwordx4 v247, s[60:63], s53 offen nt lds\nbuffer_load_dwordx4 v248, s[60:63], s53 offen offset:1024 nt lds\n"
"buffer_load_dwordx4 v249, s[60:63], s53 offen offset:2048 nt lds\nbuffer_load_dwordx4 v250, s[60:63], s53 offen offset:3072 nt lds\nM 188,204,128,236\n"
"E 158\nM 180,204,112,232\nE 159\nM 172,204,96,228\nR 200,244,1280\nM 168,208,100,168\nE 164\nM 176,208,116,176\nE 165\nM 184,208,132,184\n"
"R 204,244,1536\ns_xor_b32 s53, s30, 8192\ns_add_u32 m0, s53, 65536\ns_add_u32 s53, s53, 196608\ns_xor_b32 s53, s53, s95\ns_mul_i32 s52, s68, 25\n"
"s_lshr_b32 s52, s52, 3\ns_min_u32 s52, s52, s94\ns_max_i32 s52, s52, s55\n.Lpace_76_%=:\ns_memrealtime s[96:97]\nW 0\ns_sub_u32 s96, s96, s70\n"
"s_cmp_lt_i32 s96, s52\ns_cbranch_scc0 .Lpz_77_%=\ns_sleep 1\ns_branch .Lpace_76_%=\n.Lpz_77_%=:\ns_sub_u32 s96, s96, 6\ns_max_i32 s96, s96, s52\n"
"s_add_u32 s55, s96, 55\nbuffer_load_dwordx4 v247, s[60:63], s53 offen nt lds\nbuffer_load_dwordx4 v248, s[60:63], s53 offen offset:1024 nt lds\n"
"buffer_load_dwordx4 v249, s[60:63], s53 offen offset:2048 nt lds\nbuffer_load_dwordx4 v250, s[60:63], s53 offen offset:3072 nt lds\nM 188,192,132,188\n"
"E 166\nM 180,192,116,180\nE 167\nM 172,192,100,172\nR 208,244,1792\nM 168,196,104,168\nv_add_u32 v245, 0x10000, v245\nv_add_u32 v246, 0x10000, v246\n"
"M 176,196,120,176\nT 192,245,0\nT 194,245,256\nM 184,196,136,184\nC 161,162,163\ns_xor_b32 s53, s30, 12288\ns_add_u32 m0, s53, 65536\n"
"s_add_u32 s53, s53, 196608\ns_xor_b32 s53, s53, s95\nbuffer_load_dwordx4 v247, s[60:63], s53 offen nt lds\n"
"buffer_load_dwordx4 v248, s[60:63], s53 offen offset:1024 nt lds\nbuffer_load_dwordx4 v249, s[60:63], s53 offen offset:2048 nt lds\n"
"buffer_load_dwordx4 v250, s[60:63], s53 offen offset:3072 nt lds\nM 188,200,136,188\nT 196,246,0\nT 198,246,256\nM 180,200,120,180\nC 146,148,149\n"
"C 147,150,151\nM 172,200,104,172\nC 154,156,157\nM 168,204,108,168\nT 200,245,512\nT 202,245,768\nM 176,204,124,176\nC 155,158,159\nC 162,164,165\n"
"M 184,204,140,184\nC 163,166,167\ns_xor_b32 s53, s30, 0\ns_add_u32 m0, s53, 98304\ns_add_u32 s53, s53, 196608\ns_xor_b32 s53, s53, s95\n"
"s_mul_i32 s52, s68, 26\ns_lshr_b32 s52, s52, 3\ns_min_u32 s52, s52, s94\ns_max_i32 s52, s52, s55\n.Lpace_78_%=:\ns_memrealtime s[96:97]\nW 0\n"
"s_sub_u32 s96, s96, s70\ns_cmp_lt_i32 s96, s52\ns_cbranch_scc0 .Lpz_79_%=\ns_sleep 1\ns_branch .Lpace_78_%=\n.Lpz_79_%=:\ns_sub_u32 s96, s96, 6\n"
"s_max_i32 s96, s96, s52\ns_add_u32 s55, s96, 55\nbuffer_load_dwordx4 v247, s[36:39], s53 offen nt lds\n"
"buffer_load_dwordx4 v248, s[36:39], s53 offen offset:1024 nt lds\nbuffer_load_dwordx4 v249, s[36:39], s53 offen offset:2048 nt lds\n"
"buffer_load_dwordx4 v250, s[36:39], s53 offen offset:3072 nt lds\nM 188,208,140,188\nT 204,246,512\nT 206,246,768\nM 180,208,124,180\n"
"M 172,208,108,172\nM 0,192,144,0\nT 208,245,1024\nT 210,245,1280\nE 168\nM 212,240,144,212\nE 169\nE 170\nM 32,192,152,32\nE 171\nM 64,192,160,64\n"
"C 168,168,169\ns_xor_b32 s53, s30, 4096\ns_add_u32 m0, s53, 98304\ns_add_u32 s53, s53, 196608\ns_xor_b32 s53, s53, s95\n"
"buffer_load_dwordx4 v247, s[36:39], s53 offen nt lds\nbuffer_load_dwordx4 v248, s[36:39], s53 offen offset:1024 nt lds\n"
"buffer_load_dwordx4 v249, s[36:39], s53 offen offset:2048 nt lds\nbuffer_load_dwordx4 v250, s[36:39], s53 offen offset:3072 nt lds\nM 68,196,160,68\n"
"T 192,246,1024\nT 194,246,1280\nM 36,196,152,36\nC 169,170,171\nM 216,240,152,216\nE 176\nM 4,196,144,4\nM 8,200,144,8\nT 196,245,1536\nT 198,245,1792\n"
"M 40,200,152,40\nE 177\nM 72,200,160,72\nE 178\nM 220,240,160,220\nC 176,176,177\ns_xor_b32 s53, s30, 8192\ns_add_u32 m0, s53, 98304\n"
"s_add_u32 s53, s53, 196608\ns_xor_b32 s53, s53, s95\ns_mul_i32 s52, s68, 27\ns_lshr_b32 s52, s52, 3\ns_min_u32 s52, s52, s94\ns_max_i32 s52, s52, s55\n"
".Lpace_80_%=:\ns_memrealtime s[96:97]\nW 0\ns_sub_u32 s96, s96, s70\ns_cmp_lt_i32 s96, s52\ns_cbranch_scc0 .Lpz_81_%=\ns_sleep 1\n"
"s_branch .Lpace_80_%=\n.Lpz_81_%=:\ns_sub_u32 s96, s96, 6\ns_max_i32 s96, s96, s52\ns_add_u32 s55, s96, 55\n"
"buffer_load_dwordx4 v247, s[36:39], s53 offen nt lds\nbuffer_load_dwordx4 v248, s[36:39], s53 offen offset:1024 nt lds\n"
"buffer_load_dwordx4 v249, s[36:39], s53 offen offset:2048 nt lds\nbuffer_load_dwordx4 v250, s[36:39], s53 offen offset:3072 nt lds\nM 76,204,160,76\n"
"T 200,246,1536\nT 202,246,1792\nM 44,204,152,44\nE 179\nM 12,204,144,12\nC 177,178,179\nM 16,208,144,16\nE 184\nM 48,208,152,48\nE 185\n"
"M 80,208,160,80\nC 184,184,185\ns_xor_b32 s53, s30, 12288\ns_add_u32 m0, s53, 98304\ns_add_u32 s53, s53, 196608\ns_xor_b32 s53, s53, s95\n"
"buffer_load_dwordx4 v247, s[36:39], s53 offen nt lds\nbuffer_load_dwordx4 v248, s[36:39], s53 offen offset:1024 nt lds\n"
"buffer_load_dwordx4 v249, s[36:39], s53 offen offset:2048 nt lds\nbuffer_load_dwordx4 v250, s[36:39], s53 offen offset:3072 nt lds\n"
"s_waitcnt vmcnt(16)\nW 0\ns_barrier\nv_add_u32 v244, 0x10000, v244\nR 204,244,0\nM 84,192,160,84\nE 186\nM 52,192,152,52\nE 187\nM 20,192,144,20\n"
"R 208,244,256\nM 24,196,144,24\nE 172\nM 56,196,152,56\nE 173\nM 88,196,160,88\nR 192,244,512\ns_xor_b32 s53, s30, 0\ns_add_u32 m0, s53, 131072\n"
"s_add_u32 s53, s53, 229376\ns_xor_b32 s53, s53, s95\ns_mul_i32 s52, s68, 28\ns_lshr_b32 s52, s52, 3\ns_min_u32 s52, s52, s94\ns_max_i32 s52, s52, s55\n"
".Lpace_82_%=:\ns_memrealtime s[96:97]\nW 0\ns_sub_u32 s96, s96, s70\ns_cmp_lt_i32 s96, s52\ns_cbranch_scc0 .Lpz_83_%=\ns_sleep 1\n"
"s_branch .Lpace_82_%=\n.Lpz_83_%=:\ns_sub_u32 s96, s96, 6\ns_max_i32 s96, s96, s52\ns_add_u32 s55, s96, 55\n"
"buffer_load_dwordx4 v247, s[60:63], s53 offen nt lds\nbuffer_load_dwordx4 v248, s[60:63], s53 offen offset:1024 nt lds\n"
"buffer_load_dwordx4 v249, s[60:63], s53 offen offset:2048 nt lds\nbuffer_load_dwordx4 v250, s[60:63], s53 offen offset:3072 nt lds\nM 92,200,160,92\n"
"E 174\nM 60,200,152,60\nE 175\nM 28,200,144,28\nR 196,244,768\nM 144,204,96,228\nE 180\nM 152,204,112,232\nE 181\nM 160,204,128,236\nR 200,244,1024\n"
"s_xor_b32 s53, s30, 4096\ns_add_u32 m0, s53, 131072\ns_add_u32 s53, s53, 229376\ns_xor_b32 s53, s53, s95\n"
"buffer_load_dwordx4 v247, s[60:63], s53 offen nt lds\nbuffer_load_dwordx4 v248, s[60:63], s53 offen offset:1024 nt lds\n"
"buffer_load_dwordx4 v249, s[60:63], s53 offen offset:2048 nt lds\nbuffer_load_dwordx4 v250, s[60:63], s53 offen offset:3072 nt lds\nM 164,208,128,236\n"
"E 182\nM 156,208,112,232\nE 183\nM 148,208,96,228\nR 204,244,1280\nM 144,192,100,144\nE 188\nM 152,192,116,152\nE 189\nM 160,192,132,160\n"
"R 208,244,1536\ns_xor_b32 s53, s30, 8192\ns_add_u32 m0, s53, 131072\ns_add_u32 s53, s53, 229376\ns_xor_b32 s53, s53, s95\ns_mul_i32 s52, s68, 29\n"
"s_lshr_b32 s52, s52, 3\ns_min_u32 s52, s52, s94\ns_max_i32 s52, s52, s55\n.Lpace_84_%=:\ns_memrealtime s[96:97]\nW 0\ns_sub_u32 s96, s96, s70\n"
"s_cmp_lt_i32 s96, s52\ns_cbranch_scc0 .Lpz_85_%=\ns_sleep 1\ns_branch .Lpace_84_%=\n.Lpz_85_%=:\ns_sub_u32 s96, s96, 6\ns_max_i32 s96, s96, s52\n"
"s_add_u32 s55, s96, 55\nbuffer_load_dwordx4 v247, s[60:63], s53 offen nt lds\nbuffer_load_dwordx4 v248, s[60:63], s53 offen offset:1024 nt lds\n"
"buffer_load_dwordx4 v249, s[60:63], s53 offen offset:2048 nt lds\nbuffer_load_dwordx4 v250, s[60:63], s53 offen offset:3072 nt lds\nM 164,196,132,164\n"
"E 190\nM 156,196,116,156\nE 191\nM 148,196,100,148\nR 192,244,1792\nM 144,200,104,144\nv_add_u32 v245, 0xfffe8000, v245\n"
"v_add_u32 v246, 0xfffe8000, v246\nM 152,200,120,152\nT 196,245,0\nT 198,245,256\nM 160,200,136,160\nC 185,186,187\ns_xor_b32 s53, s30, 12288\n"
"s_add_u32 m0, s53, 131072\ns_add_u32 s53, s53, 229376\ns_xor_b32 s53, s53, s95\nbuffer_load_dwordx4 v247, s[60:63], s53 offen nt lds\n"
"buffer_load_dwordx4 v248, s[60:63], s53 offen offset:1024 nt lds\nbuffer_load_dwordx4 v249, s[60:63], s53 offen offset:2048 nt lds\n"
"buffer_load_dwordx4 v250, s[60:63], s53 offen offset:3072 nt lds\nM 164,204,136,164\nT 200,246,0\nT 202,246,256\nM 156,204,120,156\nC 170,172,173\n"
"C 171,174,175\nM 148,204,104,148\nC 178,180,181\nM 144,208,108,144\nT 204,245,512\nT 206,245,768\nM 152,208,124,152\nC 179,182,183\nC 186,188,189\n"
"M 160,208,140,160\nC 187,190,191\ns_xor_b32 s53, s30, 0\ns_add_u32 m0, s53, 0\ns_add_u32 s53, s53, 229376\ns_xor_b32 s53, s53, s95\n"
"s_mul_i32 s52, s68, 30\ns_lshr_b32 s52, s52, 3\ns_min_u32 s52, s52, s94\ns_max_i32 s52, s52, s55\n.Lpace_86_%=:\ns_memrealtime s[96:97]\nW 0\n"
"s_sub_u32 s96, s96, s70\ns_cmp_lt_i32 s96, s52\ns_cbranch_scc0 .Lpz_87_%=\ns_sleep 1\ns_branch .Lpace_86_%=\n.Lpz_87_%=:\ns_sub_u32 s96, s96, 6\n"
"s_max_i32 s96, s96, s52\ns_add_u32 s55, s96, 55\nbuffer_load_dwordx4 v247, s[36:39], s53 offen nt lds\n"
"buffer_load_dwordx4 v248, s[36:39], s53 offen offset:1024 nt lds\nbuffer_load_dwordx4 v249, s[36:39], s53 offen offset:2048 nt lds\n"
"buffer_load_dwordx4 v250, s[36:39], s53 offen offset:3072 nt lds\nM 164,192,140,164\nT 208,246,512\nT 210,246,768\nM 156,192,124,156\n"
"M 148,192,108,148\nM 0,196,168,0\nT 192,245,1024\nT 194,245,1280\nE 144\nM 212,240,168,212\nE 145\nE 146\nM 32,196,176,32\nE 147\nM 64,196,184,64\n"
"C 144,144,145\ns_xor_b32 s53, s30, 4096\ns_add_u32 m0, s53, 0\ns_add_u32 s53, s53, 229376\ns_xor_b32 s53, s53, s95\n"
"buffer_load_dwordx4 v247, s[36:39], s53 offen nt lds\nbuffer_load_dwordx4 v248, s[36:39], s53 offen offset:1024 nt lds\n"
"buffer_load_dwordx4 v249, s[36:39], s53 offen offset:2048 nt lds\nbuffer_load_dwordx4 v250, s[36:39], s53 offen offset:3072 nt lds\nM 68,200,184,68\n"
"T 196,246,1024\nT 198,246,1280\nM 36,200,176,36\nC 145,146,147\nM 216,240,176,216\nE 152\nM 4,200,168,4\nM 8,204,168,8\nT 200,245,1536\nT 202,245,1792\n"
"M 40,204,176,40\nE 153\nM 72,204,184,72\nE 154\nM 220,240,184,220\nC 152,152,153\nW 6\nM 76,208,184,76\nT 204,246,1536\nT 206,246,1792\n"
"M 44,208,176,44\nE 155\nM 12,208,168,12\nC 153,154,155\nW 6\nM 16,192,168,16\nE 160\nM 48,192,176,48\nE 161\nM 80,192,184,80\nC 160,160,161\n"
"s_waitcnt vmcnt(8)\nW 0\ns_barrier\nv_add_u32 v244, 0x10000, v244\nR 208,244,0\nM 84,196,184,84\nE 162\nM 52,196,176,52\nE 163\nM 20,196,168,20\n"
"R 192,244,256\nM 24,200,168,24\nE 148\nM 56,200,176,56\nE 149\nM 88,200,184,88\nR 196,244,512\ns_xor_b32 s53, s30, 8192\ns_add_u32 m0, s53, 0\n"
"s_add_u32 s53, s53, 229376\ns_xor_b32 s53, s53, s95\ns_mul_i32 s52, s68, 31\ns_lshr_b32 s52, s52, 3\ns_min_u32 s52, s52, s94\ns_max_i32 s52, s52, s55\n"
".Lpace_88_%=:\ns_memrealtime s[96:97]\nW 0\ns_sub_u32 s96, s96, s70\ns_cmp_lt_i32 s96, s52\ns_cbranch_scc0 .Lpz_89_%=\ns_sleep 1\n"
"s_branch .Lpace_88_%=\n.Lpz_89_%=:\ns_sub_u32 s96, s96, 6\ns_max_i32 s96, s96, s52\ns_add_u32 s55, s96, 55\n"
"buffer_load_dwordx4 v247, s[36:39], s53 offen nt lds\nbuffer_load_dwordx4 v248, s[36:39], s53 offen offset:1024 nt lds\n"
"buffer_load_dwordx4 v249, s[36:39], s53 offen offset:2048 nt lds\nbuffer_load_dwordx4 v250, s[36:39], s53 offen offset:3072 nt lds\nM 92,204,184,92\n"
"E 150\nM 60,204,176,60\nE 151\nM 28,204,168,28\nR 200,244,768\nM 168,208,96,228\nE 156\nM 176,208,112,232\nE 157\nM 184,208,128,236\nR 204,244,1024\n"
"s_xor_b32 s53, s30, 12288\ns_add_u32 m0, s53, 0\ns_add_u32 s53, s53, 229376\ns_xor_b32 s53, s53, s95\n"
"buffer_load_dwordx4 v247, s[36:39], s53 offen nt lds\nbuffer_load_dwordx4 v248, s[36:39], s53 offen offset:1024 nt lds\n"
"buffer_load_dwordx4 v249, s[36:39], s53 offen offset:2048 nt lds\nbuffer_load_dwordx4 v250, s[36:39], s53 offen offset:3072 nt lds\nM 188,192,128,236\n"
"E 158\nM 180,192,112,232\nE 159\nM 172,192,96,228\nR 208,244,1280\nM 168,196,100,168\nE 164\nM 176,196,116,176\nE 165\nM 184,196,132,184\n"
"R 192,244,1536\nW 3\nM 188,200,132,188\nE 166\nM 180,200,116,180\nE 167\nM 172,200,100,172\nR 196,244,1792\nW 3\nM 168,204,104,168\n"
"v_add_u32 v245, 0x10000, v245\nv_add_u32 v246, 0x10000, v246\nM 176,204,120,176\nT 200,245,0\nT 202,245,256\nM 184,204,136,184\nC 161,162,163\nW 4\n"
"M 188,208,136,188\nT 204,246,0\nT 206,246,256\nC 146,148,149\nM 180,208,120,180\nC 147,150,151\nC 154,156,157\nC 155,158,159\nM 172,208,104,172\n"
"C 162,164,165\nC 163,166,167\nW 5\nM 168,192,108,168\nT 208,245,512\nT 210,245,768\nM 176,192,124,176\nM 184,192,140,184\nW 6\nM 188,196,140,188\n"
"T 192,246,512\nT 194,246,768\nM 180,196,124,180\nM 172,196,108,172\nW 6\nM 0,200,144,0\nT 196,245,1024\nT 198,245,1280\nE 168\nM 212,240,144,212\n"
"E 169\nE 170\nM 32,200,152,32\nE 171\nC 168,168,169\nM 64,200,160,64\nC 169,170,171\nE 176\nW 6\nM 68,204,160,68\nT 200,246,1024\nT 202,246,1280\n"
"E 177\nM 36,204,152,36\nC 176,176,177\nE 178\nM 216,240,152,216\nE 179\nE 184\nM 4,204,144,4\nC 177,178,179\nE 185\nW 6\nM 8,208,144,8\nT 204,245,1536\n"
"T 206,245,1792\nC 184,184,185\nM 40,208,152,40\nE 186\nE 187\nM 72,208,160,72\nE 172\nC 185,186,187\nM 220,240,160,220\nE 173\nW 6\nM 76,192,160,76\n"
"T 208,246,1536\nT 210,246,1792\nC 170,172,173\nM 44,192,152,44\nE 174\nE 175\nM 12,192,144,12\nC 171,174,175\nE 180\nW 6\nM 16,196,144,16\nE 181\n"
"E 182\nM 48,196,152,48\nE 183\nC 178,180,181\nM 80,196,160,80\nC 179,182,183\ns_lshl_b32 s26, s59, 1\nv_add_u32 v98, 0, v255\n"
"v_mul_u32_u24 v98, 205, v98\nv_lshrrev_b32 v96, 11, v98\nv_add_u32 v247, 0, v255\nv_mul_u32_u24 v98, 10, v96\nv_sub_u32 v97, v247, v98\n"
"v_xor_b32 v98, s26, v97\nv_cmp_gt_u32 vcc, 8, v97\ns_nop 1\nv_cndmask_b32 v97, v97, v98, vcc\nv_mul_u32_u24 v247, 1280, v96\nv_lshlrev_b32 v98, 4, v97\n"
"v_add_u32 v247, v247, v98\nv_subrev_u32 v247, 0, v247\nv_add_u32 v98, 64, v255\nv_mul_u32_u24 v98, 205, v98\nv_lshrrev_b32 v96, 11, v98\n"
"v_add_u32 v248, 64, v255\nv_mul_u32_u24 v98, 10, v96\nv_sub_u32 v97, v248, v98\nv_xor_b32 v98, s26, v97\nv_cmp_gt_u32 vcc, 8, v97\ns_nop 1\n"
"v_cndmask_b32 v97, v97, v98, vcc\nv_mul_u32_u24 v248, 1280, v96\nv_lshlrev_b32 v98, 4, v97\nv_add_u32 v248, v248, v98\nv_subrev_u32 v248, 1024, v248\n"
"v_add_u32 v98, 128, v255\nv_mul_u32_u24 v98, 205, v98\nv_lshrrev_b32 v96, 11, v98\nv_add_u32 v249, 128, v255\nv_mul_u32_u24 v98, 10, v96\n"
"v_sub_u32 v97, v249, v98\nv_xor_b32 v98, s26, v97\nv_cmp_gt_u32 vcc, 8, v97\ns_nop 1\nv_cndmask_b32 v97, v97, v98, vcc\nv_mul_u32_u24 v249, 1280, v96\n"
"v_lshlrev_b32 v98, 4, v97\nv_add_u32 v249, v249, v98\nv_subrev_u32 v249, 2048, v249\nv_add_u32 v98, 192, v255\nv_mul_u32_u24 v98, 205, v98\n"
"v_lshrrev_b32 v96, 11, v98\nv_add_u32 v250, 192, v255\nv_mul_u32_u24 v98, 10, v96\nv_sub_u32 v97, v250, v98\nv_xor_b32 v98, s26, v97\n"
"v_cmp_gt_u32 vcc, 8, v97\ns_nop 1\nv_cndmask_b32 v97, v97, v98, vcc\nv_mul_u32_u24 v250, 1280, v96\nv_lshlrev_b32 v98, 4, v97\n"
"v_add_u32 v250, v250, v98\nv_subrev_u32 v250, 3072, v250\nv_add_u32 v98, 256, v255\nv_mul_u32_u24 v98, 205, v98\nv_lshrrev_b32 v96, 11, v98\n"
"v_add_u32 v251, 256, v255\nv_mul_u32_u24 v98, 10, v96\nv_sub_u32 v97, v251, v98\nv_xor_b32 v98, s26, v97\nv_cmp_gt_u32 vcc, 8, v97\ns_nop 1\n"
"v_cndmask_b32 v97, v97, v98, vcc\nv_mul_u32_u24 v251, 1280, v96\nv_lshlrev_b32 v98, 4, v97\nv_add_u32 v251, v251, v98\nv_subrev_u32 v251, 0, v251\n"
"s_mov_b32 s26, s58\ns_lshl_b32 s26, s26, 1\ns_add_u32 s26, s26, s57\ns_mul_i32 s53, s26, 81920\ns_lshl_b32 s27, s31, 1\ns_add_u32 m0, s27, 36864\n"
"s_nop 0\nbuffer_load_dwordx4 v247, s[44:47], s53 offen lds\nbuffer_load_dwordx4 v248, s[44:47], s53 offen offset:1024 lds\n"
"buffer_load_dwordx4 v249, s[44:47], s53 offen offset:2048 lds\nbuffer_load_dwordx4 v250, s[44:47], s53 offen offset:3072 lds\ns_add_u32 m0, s27, 40960\n"
"s_nop 0\nbuffer_load_dwordx4 v251, s[44:47], s53 offen lds\ns_mov_b32 s26, s58\ns_lshl_b32 s26, s26, 1\ns_add_u32 s26, s26, s57\n"
"s_mul_i32 s53, s26, 81920\ns_lshl_b32 s27, s31, 1\ns_add_u32 m0, s27, 41984\ns_add_u32 s53, s53, 40960\n"
"buffer_load_dwordx4 v247, s[44:47], s53 offen lds\nbuffer_load_dwordx4 v248, s[44:47], s53 offen offset:1024 lds\n"
"buffer_load_dwordx4 v249, s[44:47], s53 offen offset:2048 lds\nbuffer_load_dwordx4 v250, s[44:47], s53 offen offset:3072 lds\ns_add_u32 m0, s27, 46080\n"
"s_nop 0\nbuffer_load_dwordx4 v251, s[44:47], s53 offen lds\ns_add_u32 s26, s71, 0\ns_cmp_ge_u32 s26, 7\ns_cselect_b32 s27, -6, 1\n"
"s_add_u32 s26, s26, s27\ns_add_u32 s26, s26, s58\ns_and_b32 s26, s26, 7\ns_lshl_b32 s26, s26, 1\ns_add_u32 s26, s26, s57\ns_mul_i32 s53, s26, 81920\n"
"s_lshl_b32 s27, s31, 1\ns_add_u32 m0, s27, 57344\ns_nop 0\nbuffer_load_dwordx4 v247, s[44:47], s53 offen lds\n"
"buffer_load_dwordx4 v248, s[44:47], s53 offen offset:1024 lds\nbuffer_load_dwordx4 v249, s[44:47], s53 offen offset:2048 lds\n"
"buffer_load_dwordx4 v250, s[44:47], s53 offen offset:3072 lds\ns_add_u32 m0, s27, 61440\ns_nop 0\nbuffer_load_dwordx4 v251, s[44:47], s53 offen lds\n"
"s_add_u32 s26, s71, 0\ns_cmp_ge_u32 s26, 7\ns_cselect_b32 s27, -6, 1\ns_add_u32 s26, s26, s27\ns_add_u32 s26, s26, s58\ns_and_b32 s26, s26, 7\n"
"s_lshl_b32 s26, s26, 1\ns_add_u32 s26, s26, s57\ns_mul_i32 s53, s26, 81920\ns_lshl_b32 s27, s31, 1\ns_add_u32 m0, s27, 62464\n"
"s_add_u32 s53, s53, 40960\nbuffer_load_dwordx4 v247, s[44:47], s53 offen lds\nbuffer_load_dwordx4 v248, s[44:47], s53 offen offset:1024 lds\n"
"buffer_load_dwordx4 v249, s[44:47], s53 offen offset:2048 lds\nbuffer_load_dwordx4 v250, s[44:47], s53 offen offset:3072 lds\ns_add_u32 m0, s27, 66560\n"
"s_nop 0\nbuffer_load_dwordx4 v251, s[44:47], s53 offen lds\ns_add_u32 s26, s71, 1\ns_cmp_ge_u32 s26, 7\ns_cselect_b32 s27, -6, 1\n"
"s_add_u32 s26, s26, s27\ns_add_u32 s26, s26, s58\ns_and_b32 s26, s26, 7\ns_lshl_b32 s26, s26, 1\ns_add_u32 s26, s26, s57\ns_mul_i32 s53, s26, 81920\n"
"s_lshl_b32 s27, s31, 1\ns_add_u32 m0, s27, 77824\ns_nop 0\nbuffer_load_dwordx4 v247, s[44:47], s53 offen lds\n"
"buffer_load_dwordx4 v248, s[44:47], s53 offen offset:1024 lds\nbuffer_load_dwordx4 v249, s[44:47], s53 offen offset:2048 lds\n"
"buffer_load_dwordx4 v250, s[44:47], s53 offen offset:3072 lds\ns_add_u32 m0, s27, 81920\ns_nop 0\nbuffer_load_dwordx4 v251, s[44:47], s53 offen lds\n"
"s_add_u32 s26, s71, 1\ns_cmp_ge_u32 s26, 7\ns_cselect_b32 s27, -6, 1\ns_add_u32 s26, s26, s27\ns_add_u32 s26, s26, s58\ns_and_b32 s26, s26, 7\n"
"s_lshl_b32 s26, s26, 1\ns_add_u32 s26, s26, s57\ns_mul_i32 s53, s26, 81920\ns_lshl_b32 s27, s31, 1\ns_add_u32 m0, s27, 82944\n"
"s_add_u32 s53, s53, 40960\nbuffer_load_dwordx4 v247, s[44:47], s53 offen lds\nbuffer_load_dwordx4 v248, s[44:47], s53 offen offset:1024 lds\n"
"buffer_load_dwordx4 v249, s[44:47], s53 offen offset:2048 lds\nbuffer_load_dwordx4 v250, s[44:47], s53 offen offset:3072 lds\ns_add_u32 m0, s27, 87040\n"
"s_nop 0\nbuffer_load_dwordx4 v251, s[44:47], s53 offen lds\ns_waitcnt vmcnt(30)\nW 0\ns_barrier\nv_add_u32 v245, 0xfffe8000, v245\n"
"v_add_u32 v246, 0xfffe8000, v246\nT 192,245,0\nT 194,245,256\nM 84,200,160,84\nT 196,246,0\nT 198,246,256\nE 188\nM 52,200,152,52\nE 189\n"
"M 20,200,144,20\nC 186,188,189\nM 24,204,144,24\nT 200,245,512\nT 202,245,768\nM 56,204,152,56\nE 190\nM 88,204,160,88\nM 92,208,160,92\nT 204,246,512\n"
"T 206,246,768\nE 191\nM 60,208,152,60\nC 187,190,191\nM 28,208,144,28\nW 6\nM 0,192,168,0\nT 208,245,1024\nT 210,245,1280\nM 212,240,168,212\n"
"v_add_u32 v244, s72, v252\nv_lshrrev_b32 v151, 2, v252\nM 32,192,176,32\nv_and_b32 v151, 60, v151\nv_add_u32 v150, s73, v151\nM 64,192,184,64\n"
"v_add_u32 v151, s75, v151\nW 6\nM 68,196,184,68\nT 192,246,1024\nT 194,246,1280\nM 36,196,176,36\nv_mov_b32 v144, 0\nv_mov_b32 v145, 0\n"
"M 216,240,176,216\nv_mov_b32 v146, v224\nv_mov_b32 v147, v225\nM 4,196,168,4\nv_mov_b32 v148, 0\nW 6\nM 8,200,168,8\nT 196,245,1536\nT 198,245,1792\n"
"M 40,200,176,40\nv_mov_b32 v149, 0\nv_add_u32 v152, s76, v252\nM 72,200,184,72\nv_mov_b32 v153, 161280\nM 220,240,184,220\nW 6\nM 76,204,184,76\n"
"T 200,246,1536\nT 202,246,1792\nM 44,204,176,44\nC 0,0,1\nC 1,2,3\nM 12,204,168,12\nC 2,4,5\nW 6\nM 16,208,168,16\nC 3,6,7\nds_write_b128 v244, v[0:3]\n"
"M 48,208,176,48\nC 64,64,65\nC 65,66,67\nM 80,208,184,80\nC 66,68,69\nW 5\nM 84,192,184,84\nC 67,70,71\nds_write_b128 v244, v[64:67] offset:17408\n"
"ds_write_b32 v150, v212\nM 52,192,176,52\nds_write_b32 v150, v216 offset:64\nds_write_b32 v150, v220 offset:128\nM 20,192,168,20\nC 8,8,9\nW 7\n"
"M 24,196,168,24\nC 9,10,11\nC 10,12,13\nC 11,14,15\nM 56,196,176,56\nds_write_b128 v244, v[8:11] offset:2048\nC 72,72,73\nM 88,196,184,88\nC 73,74,75\n"
"W 6\nM 92,200,184,92\nC 74,76,77\nC 75,78,79\nds_write_b128 v244, v[72:75] offset:19456\nM 60,200,176,60\nC 16,16,17\nC 17,18,19\nC 18,20,21\n"
"M 28,200,168,28\nC 19,22,23\nds_write_b128 v244, v[16:19] offset:4096\nC 80,80,81\nC 81,82,83\nC 82,84,85\nC 83,86,87\n"
"ds_write_b128 v244, v[80:83] offset:21504\nC 24,24,25\nC 25,26,27\nC 26,28,29\nC 27,30,31\nds_write_b128 v244, v[24:27] offset:6144\nC 88,88,89\n"
"C 89,90,91\nC 90,92,93\nC 91,94,95\nds_write_b128 v244, v[88:91] offset:23552\ns_add_u32 s26, s71, 5\ns_cmp_ge_u32 s26, 7\ns_cselect_b32 s27, -6, 1\n"
"s_add_u32 s26, s26, s27\ns_add_u32 s26, s26, s58\ns_and_b32 s26, s26, 7\ns_lshl_b32 s26, s26, 2\ns_add_u32 s26, s26, s20\ns_mul_i32 s52, s26, 40960\n"
"buffer_load_dwordx4 v[96:99], v247, s[44:47], s52 offen\nbuffer_load_dwordx4 v[100:103], v248, s[44:47], s52 offen offset:1024\n"
"buffer_load_dwordx4 v[104:107], v249, s[44:47], s52 offen offset:2048\nbuffer_load_dwordx4 v[108:111], v250, s[44:47], s52 offen offset:3072\n"
"buffer_load_dwordx4 v[112:115], v251, s[44:47], s52 offen\ns_add_u32 s26, s71, 6\ns_cmp_ge_u32 s26, 7\ns_cselect_b32 s27, -6, 1\n"
"s_add_u32 s26, s26, s27\ns_add_u32 s26, s26, s58\ns_and_b32 s26, s26, 7\ns_lshl_b32 s26, s26, 2\ns_add_u32 s26, s26, s20\ns_mul_i32 s52, s26, 40960\n"
"buffer_load_dwordx4 v[116:119], v247, s[44:47], s52 offen\nbuffer_load_dwordx4 v[120:123], v248, s[44:47], s52 offen offset:1024\n"
"buffer_load_dwordx4 v[124:127], v249, s[44:47], s52 offen offset:2048\nbuffer_load_dwordx4 v[128:131], v250, s[44:47], s52 offen offset:3072\n"
"buffer_load_dwordx4 v[132:135], v251, s[44:47], s52 offen\nv_mov_b32 v240, v216\nv_mov_b32 v168, 0\nv_mov_b32 v169, 0\nds_write_b64 v153, v[168:169]\n"
"W 0\ns_barrier\nds_read_b32 v176, v151 offset:384\nds_read_b32 v177, v151 offset:1152\nR 192,244,8192\nR 196,244,9216\nR 200,244,10240\n"
"R 204,244,11264\nR 208,244,12288\nR 212,244,13312\nR 216,244,14336\nR 220,244,15360\nW 9\nv_add_f32 v240, v240, v176\nW 8\nv_add_f32 v240, v240, v177\n"
"v_cmp_ngt_f32 vcc, 0x53800000, v240\nv_rcp_f32 v170, v240\nv_rcp_f32 v171, v240\ns_cmp_lg_u64 vcc, 0\ns_cbranch_scc0 .Lw_90_%=\n"
"ds_write_b32 v153, v153 offset:4\n.Lw_90_%=:\nW 7\nM 32,146,192,32\nM 36,144,192,36\nW 6\nM 32,146,196,32\nM 36,144,196,36\nW 5\nM 40,146,200,40\n"
"M 44,144,200,44\nW 4\nM 40,146,204,40\nM 44,144,204,44\ns_nop 0\nv_pk_mul_f32 v[32:33], v[32:33], v[170:171]\nW 3\nM 48,146,208,48\n"
"v_pk_mul_f32 v[34:35], v[34:35], v[170:171]\nv_pk_mul_f32 v[36:37], v[36:37], v[170:171]\nM 52,144,208,52\nv_pk_mul_f32 v[38:39], v[38:39], v[170:171]\n"
"C 32,32,33\nW 2\nM 48,146,212,48\nC 33,34,35\nC 34,36,37\nM 52,144,212,52\nC 35,38,39\nbuffer_store_dwordx4 v[32:35], v152, s[48:51], s64 offen sc1\n"
"v_pk_mul_f32 v[40:41], v[40:41], v[170:171]\nW 1\nM 56,146,216,56\nv_pk_mul_f32 v[42:43], v[42:43], v[170:171]\n"
"v_pk_mul_f32 v[44:45], v[44:45], v[170:171]\nM 60,144,216,60\nv_pk_mul_f32 v[46:47], v[46:47], v[170:171]\nC 40,40,41\nW 0\nM 56,146,220,56\n"
"C 41,42,43\nC 42,44,45\nM 60,144,220,60\nC 43,46,47\nbuffer_store_dwordx4 v[40:43], v152, s[48:51], s64 offen offset:1024 sc1\n"
"v_pk_mul_f32 v[48:49], v[48:49], v[170:171]\nv_pk_mul_f32 v[50:51], v[50:51], v[170:171]\nv_pk_mul_f32 v[52:53], v[52:53], v[170:171]\n"
"v_pk_mul_f32 v[54:55], v[54:55], v[170:171]\nC 48,48,49\nC 49,50,51\nC 50,52,53\nC 51,54,55\n"
"buffer_store_dwordx4 v[48:51], v152, s[48:51], s64 offen offset:2048 sc1\nv_pk_mul_f32 v[56:57], v[56:57], v[170:171]\n"
"v_pk_mul_f32 v[58:59], v[58:59], v[170:171]\nv_pk_mul_f32 v[60:61], v[60:61], v[170:171]\nv_pk_mul_f32 v[62:63], v[62:63], v[170:171]\nC 56,56,57\n"
"C 57,58,59\nC 58,60,61\nC 59,62,63\nbuffer_store_dwordx4 v[56:59], v152, s[48:51], s64 offen offset:3072 sc1\nds_write_b128 v152, v[32:35]\n"
"ds_write_b128 v152, v[40:43] offset:1024\nds_write_b128 v152, v[48:51] offset:2048\nds_write_b128 v152, v[56:59] offset:3072\n"
"v_add_u32 v154, s78, v252\nv_xor_b32 v155, 4096, v154\nv_add_u32 v162, s31, v252\ns_waitcnt vmcnt(14)\ns_setprio 0\ns_branch .Lt1_10_%=\n.Lv3_8_%=:\n"
"R 192,244,24576\nR 196,244,24832\nR 200,244,25088\nR 204,244,25344\ns_cmp_eq_u32 s35, 0\ns_cbranch_scc0 .Lw_91_%=\nv_max3_f32 v180, v168, v169, v170\n"
"v_max_f32 v180, v180, v171\nv_max3_f32 v181, v172, v173, v174\nv_max_f32 v181, v181, v175\nv_max3_f32 v182, v176, v177, v178\n"
"v_max_f32 v182, v182, v179\nv_mov_b32 v183, v180\nv_mov_b32 v184, v181\nv_mov_b32 v185, v182\nv_permlane16_swap_b32 v180, v183\n"
"v_permlane16_swap_b32 v181, v184\nv_permlane16_swap_b32 v182, v185\nv_max_f32 v180, v180, v183\nv_max_f32 v181, v181, v184\nv_max_f32 v182, v182, v185\n"
"v_mov_b32 v183, v180\nv_mov_b32 v184, v181\nv_mov_b32 v185, v182\nv_permlane32_swap_b32 v180, v183\nv_permlane32_swap_b32 v181, v184\n"
"v_permlane32_swap_b32 v182, v185\nv_max_f32 v180, v180, v183\nv_max_f32 v181, v181, v184\nv_max_f32 v182, v182, v185\nv_sub_f32 v228, 0, v180\n"
"v_sub_f32 v229, 0, v180\nv_sub_f32 v230, 0, v180\nv_sub_f32 v231, 0, v180\nv_sub_f32 v232, 0, v181\nv_sub_f32 v233, 0, v181\nv_sub_f32 v234, 0, v181\n"
"v_sub_f32 v235, 0, v181\nv_sub_f32 v236, 0, v182\nv_sub_f32 v237, 0, v182\nv_sub_f32 v238, 0, v182\nv_sub_f32 v239, 0, v182\n.Lw_91_%=:\nW 3\n"
"M 144,192,96,228\nM 152,192,112,232\nM 160,192,128,236\nR 208,244,25600\nW 3\nM 164,196,128,236\nM 156,196,112,232\nM 148,196,96,228\nR 192,244,25856\n"
"W 3\nM 144,200,100,144\nM 152,200,116,152\nM 160,200,132,160\nR 196,244,26112\nW 3\nM 164,204,132,164\nM 156,204,116,156\nM 148,204,100,148\n"
"R 200,244,26368\nW 3\nM 144,208,104,144\nM 152,208,120,152\nM 160,208,136,160\ns_waitcnt vmcnt(0)\nW 0\ns_barrier\nv_add_u32 v244, 0x10000, v244\n"
"R 204,244,16384\nM 164,192,136,164\nM 156,192,120,156\nM 148,192,104,148\nR 208,244,16640\nM 144,196,108,144\nM 152,196,124,152\nM 160,196,140,160\n"
"R 192,244,16896\nM 164,200,140,164\nM 156,200,124,156\nM 148,200,108,148\nR 196,244,17152\nW 3\nM 168,204,96,228\nE 144\nE 145\nM 176,204,112,232\n"
"E 146\nC 144,144,145\nM 184,204,128,236\nR 200,244,17408\nE 147\nW 3\nM 188,208,128,236\nE 152\nC 145,146,147\nM 180,208,112,232\nE 153\nE 154\n"
"M 172,208,96,228\nR 204,244,17664\nC 152,152,153\nW 3\nM 168,192,100,168\nE 155\nE 160\nM 176,192,116,176\nE 161\nC 153,154,155\nM 184,192,132,184\n"
"R 208,244,17920\nC 160,160,161\nW 3\nM 188,196,132,188\nE 162\nE 163\nM 180,196,116,180\nE 148\nC 161,162,163\nM 172,196,100,172\nR 192,244,18176\n"
"E 149\nW 3\nM 168,200,104,168\nT 196,245,24576\nT 198,245,24832\nC 146,148,149\nM 176,200,120,176\nE 150\nE 151\nM 184,200,136,184\nC 147,150,151\n"
"E 156\nW 4\nM 188,204,136,188\nT 200,246,24576\nT 202,246,24832\nE 157\nM 180,204,120,180\nE 158\nC 154,156,157\nM 172,204,104,172\nE 159\nW 5\n"
"M 168,208,108,168\nT 204,245,25088\nT 206,245,25344\nC 155,158,159\nM 176,208,124,176\nE 164\nE 165\nM 184,208,140,184\nC 162,164,165\nE 166\nW 6\n"
"M 188,192,140,188\nT 208,246,25088\nT 210,246,25344\nE 167\nM 180,192,124,180\nC 163,166,167\nM 172,192,108,172\nW 6\n"
"v_mfma_f32_16x16x32_bf16 v[0:3], v[196:199], v[144:147], 0\nT 192,245,25600\nT 194,245,25856\nE 168\n"
"v_mfma_f32_16x16x32_bf16 v[212:215], v[240:243], v[144:147], 0\nE 169\nE 170\nv_mfma_f32_16x16x32_bf16 v[32:35], v[196:199], v[152:155], 0\nE 171\n"
"v_mfma_f32_16x16x32_bf16 v[64:67], v[196:199], v[160:163], 0\nC 168,168,169\nW 6\nv_mfma_f32_16x16x32_bf16 v[68:71], v[200:203], v[160:163], 0\n"
"T 196,246,25600\nT 198,246,25856\nv_mfma_f32_16x16x32_bf16 v[36:39], v[200:203], v[152:155], 0\nC 169,170,171\n"
"v_mfma_f32_16x16x32_bf16 v[216:219], v[240:243], v[152:155], 0\nE 176\nv_mfma_f32_16x16x32_bf16 v[4:7], v[200:203], v[144:147], 0\nW 6\n"
"v_mfma_f32_16x16x32_bf16 v[8:11], v[204:207], v[144:147], 0\nT 200,245,26112\nT 202,245,26368\n"
"v_mfma_f32_16x16x32_bf16 v[40:43], v[204:207], v[152:155], 0\nE 177\nv_mfma_f32_16x16x32_bf16 v[72:75], v[204:207], v[160:163], 0\nE 178\n"
"v_mfma_f32_16x16x32_bf16 v[220:223], v[240:243], v[160:163], 0\nC 176,176,177\nW 6\nv_mfma_f32_16x16x32_bf16 v[76:79], v[208:211], v[160:163], 0\n"
"T 204,246,26112\nT 206,246,26368\nv_mfma_f32_16x16x32_bf16 v[44:47], v[208:211], v[152:155], 0\nE 179\n"
"v_mfma_f32_16x16x32_bf16 v[12:15], v[208:211], v[144:147], 0\nC 177,178,179\nW 6\nv_mfma_f32_16x16x32_bf16 v[16:19], v[192:195], v[144:147], 0\nE 184\n"
"v_mfma_f32_16x16x32_bf16 v[48:51], v[192:195], v[152:155], 0\nE 185\nv_mfma_f32_16x16x32_bf16 v[80:83], v[192:195], v[160:163], 0\nR 208,244,24576\n"
"W 5\nv_mfma_f32_16x16x32_bf16 v[84:87], v[196:199], v[160:163], 0\nE 186\nv_mfma_f32_16x16x32_bf16 v[52:55], v[196:199], v[152:155], 0\nE 187\n"
"v_mfma_f32_16x16x32_bf16 v[20:23], v[196:199], v[144:147], 0\nR 192,244,24832\nW 4\nv_mfma_f32_16x16x32_bf16 v[24:27], v[200:203], v[144:147], 0\n"
"E 172\nv_mfma_f32_16x16x32_bf16 v[56:59], v[200:203], v[152:155], 0\nE 173\nv_mfma_f32_16x16x32_bf16 v[88:91], v[200:203], v[160:163], 0\n"
"R 196,244,25088\nW 3\nv_mfma_f32_16x16x32_bf16 v[92:95], v[204:207], v[160:163], 0\nE 174\n"
"v_mfma_f32_16x16x32_bf16 v[60:63], v[204:207], v[152:155], 0\nE 175\nv_mfma_f32_16x16x32_bf16 v[28:31], v[204:207], v[144:147], 0\nR 200,244,25344\n"
"W 3\nM 144,208,96,228\nE 180\nM 152,208,112,232\nE 181\nM 160,208,128,236\nR 204,244,25600\nW 3\nM 164,192,128,236\nE 182\nM 156,192,112,232\nE 183\n"
"M 148,192,96,228\nR 208,244,25856\nW 3\nM 144,196,100,144\nE 188\nM 152,196,116,152\nE 189\nM 160,196,132,160\nR 192,244,26112\nW 3\nM 164,200,132,164\n"
"E 190\nM 156,200,116,156\nE 191\nM 148,200,100,148\nR 196,244,26368\nW 3\nM 144,204,104,144\nC 184,184,185\nC 185,186,187\nM 152,204,120,152\n"
"C 170,172,173\nC 171,174,175\nM 160,204,136,160\nC 178,180,181\nW 0\ns_barrier\nv_add_u32 v245, 0x10000, v245\nv_add_u32 v246, 0x10000, v246\n"
"T 200,245,16384\nT 202,245,16640\nM 164,208,136,164\nT 204,246,16384\nT 206,246,16640\nM 156,208,120,156\nC 179,182,183\nC 186,188,189\n"
"M 148,208,104,148\nC 187,190,191\nM 144,192,108,144\nT 208,245,16896\nT 210,245,17152\nM 152,192,124,152\nM 160,192,140,160\nM 164,196,140,164\n"
"T 192,246,16896\nT 194,246,17152\nM 156,196,124,156\nM 148,196,108,148\nW 6\nM 0,200,168,0\nT 196,245,17408\nT 198,245,17664\nE 144\nM 212,240,168,212\n"
"E 145\nE 146\nM 32,200,176,32\nE 147\nM 64,200,184,64\nC 144,144,145\nW 6\nM 68,204,184,68\nT 200,246,17408\nT 202,246,17664\nM 36,204,176,36\n"
"C 145,146,147\nM 216,240,176,216\nE 152\nM 4,204,168,4\nW 6\nM 8,208,168,8\nT 204,245,17920\nT 206,245,18176\nM 40,208,176,40\nE 153\nM 72,208,184,72\n"
"E 154\nM 220,240,184,220\nC 152,152,153\nW 6\nM 76,192,184,76\nT 208,246,17920\nT 210,246,18176\nM 44,192,176,44\nE 155\nM 12,192,168,12\n"
"C 153,154,155\nW 6\nM 16,196,168,16\nv_add_u32 v244, 0x10000, v244\nR 192,244,24576\nM 48,196,176,48\nE 160\nM 80,196,184,80\nW 5\nM 84,200,184,84\n"
"E 161\nM 52,200,176,52\nE 162\nM 20,200,168,20\nR 196,244,24832\nW 4\nM 24,204,168,24\nE 163\nM 56,204,176,56\nE 148\nM 88,204,184,88\nR 200,244,25088\n"
"W 3\nM 92,208,184,92\nE 149\nM 60,208,176,60\nE 150\nM 28,208,168,28\nR 204,244,25344\nW 3\nM 168,192,96,228\nE 151\nM 176,192,112,232\nE 156\n"
"M 184,192,128,236\nR 208,244,25600\nW 3\nM 188,196,128,236\nE 157\nM 180,196,112,232\nE 158\nM 172,196,96,228\nR 192,244,25856\nW 3\nM 168,200,100,168\n"
"E 159\nM 176,200,116,176\nE 164\nM 184,200,132,184\nR 196,244,26112\nW 3\nM 188,204,132,188\nE 165\nM 180,204,116,180\nE 166\nM 172,204,100,172\n"
"R 200,244,26368\nW 3\nM 168,208,104,168\nT 204,245,24576\nT 206,245,24832\nM 176,208,120,176\nE 167\nM 184,208,136,184\nC 160,160,161\nW 4\n"
"M 188,192,136,188\nT 208,246,24576\nT 210,246,24832\nM 180,192,120,180\nC 161,162,163\nC 146,148,149\nM 172,192,104,172\nC 147,150,151\nW 5\n"
"M 168,196,108,168\nT 192,245,25088\nT 194,245,25344\nC 154,156,157\nM 176,196,124,176\nC 155,158,159\nC 162,164,165\nM 184,196,140,184\nC 163,166,167\n"
"W 6\nM 188,200,140,188\nT 196,246,25088\nT 198,246,25344\nM 180,200,124,180\nM 172,200,108,172\nW 6\nM 0,204,144,0\nT 200,245,25600\nT 202,245,25856\n"
"E 168\nM 212,240,144,212\nE 169\nE 170\nM 32,204,152,32\nE 171\nM 64,204,160,64\nC 168,168,169\nW 6\nM 68,208,160,68\nT 204,246,25600\nT 206,246,25856\n"
"M 36,208,152,36\nC 169,170,171\nM 216,240,152,216\nE 176\nM 4,208,144,4\nW 6\nM 8,192,144,8\nT 208,245,26112\nT 210,245,26368\nM 40,192,152,40\nE 177\n"
"M 72,192,160,72\nE 178\nM 220,240,160,220\nC 176,176,177\nW 6\nM 76,196,160,76\nT 192,246,26112\nT 194,246,26368\nM 44,196,152,44\nE 179\n"
"M 12,196,144,12\nC 177,178,179\nW 6\nM 16,200,144,16\nE 184\nM 48,200,152,48\nE 185\nM 80,200,160,80\nC 184,184,185\nW 0\ns_barrier\n"
"v_add_u32 v244, 0xfffe8000, v244\nR 196,244,16384\nM 84,204,160,84\nE 186\nM 52,204,152,52\nE 187\nM 20,204,144,20\nR 200,244,16640\nM 24,208,144,24\n"
"E 172\nM 56,208,152,56\nE 173\nM 88,208,160,88\nR 204,244,16896\nM 92,192,160,92\nE 174\nM 60,192,152,60\nE 175\nM 28,192,144,28\nR 208,244,17152\nW 3\n"
"M 144,196,96,228\nE 180\nM 152,196,112,232\nE 181\nM 160,196,128,236\nR 192,244,17408\nW 3\nM 164,200,128,236\nE 182\nM 156,200,112,232\nE 183\n"
"M 148,200,96,228\nR 196,244,17664\nW 3\nM 144,204,100,144\nE 188\nM 152,204,116,152\nE 189\nM 160,204,132,160\nR 200,244,17920\nW 3\nM 164,208,132,164\n"
"E 190\nM 156,208,116,156\nE 191\nM 148,208,100,148\nR 204,244,18176\nW 3\nM 144,192,104,144\nv_add_u32 v245, 0xfffe8000, v245\n"
"v_add_u32 v246, 0xfffe8000, v246\nM 152,192,120,152\nT 208,245,24576\nT 210,245,24832\nM 160,192,136,160\nC 185,186,187\nW 4\nM 164,196,136,164\n"
"T 192,246,24576\nT 194,246,24832\nM 156,196,120,156\nC 170,172,173\nC 171,174,175\nM 148,196,104,148\nC 178,180,181\nW 5\nM 144,200,108,144\n"
"T 196,245,25088\nT 198,245,25344\nM 152,200,124,152\nC 179,182,183\nC 186,188,189\nM 160,200,140,160\nC 187,190,191\nW 6\nM 164,204,140,164\n"
"T 200,246,25088\nT 202,246,25344\nM 156,204,124,156\nM 148,204,108,148\nW 6\nM 0,208,168,0\nT 204,245,25600\nT 206,245,25856\nE 144\nM 212,240,168,212\n"
"E 145\nE 146\nM 32,208,176,32\nE 147\nM 64,208,184,64\nC 144,144,145\nW 6\nM 68,192,184,68\nT 208,246,25600\nT 210,246,25856\nM 36,192,176,36\n"
"C 145,146,147\nM 216,240,176,216\nE 152\nM 4,192,168,4\nW 6\nM 8,196,168,8\nT 192,245,26112\nT 194,245,26368\nM 40,196,176,40\nE 153\nM 72,196,184,72\n"
"E 154\nM 220,240,184,220\nC 152,152,153\nW 6\nM 76,200,184,76\nT 196,246,26112\nT 198,246,26368\nM 44,200,176,44\nE 155\nM 12,200,168,12\n"
"C 153,154,155\nW 6\nM 16,204,168,16\nE 160\nM 48,204,176,48\nE 161\nM 80,204,184,80\nR 200,244,24576\nW 5\nM 84,208,184,84\nE 162\nM 52,208,176,52\n"
"E 163\nM 20,208,168,20\nR 204,244,24832\nW 4\nM 24,192,168,24\nE 148\nM 56,192,176,56\nE 149\nM 88,192,184,88\nR 208,244,25088\nW 3\nM 92,196,184,92\n"
"E 150\nM 60,196,176,60\nE 151\nM 28,196,168,28\nR 192,244,25344\nW 3\nM 168,200,96,228\nE 156\nM 176,200,112,232\nE 157\nM 184,200,128,236\n"
"R 196,244,25600\nW 3\nM 188,204,128,236\nE 158\nM 180,204,112,232\nE 159\nM 172,204,96,228\nR 200,244,25856\nW 3\nM 168,208,100,168\nE 164\n"
"M 176,208,116,176\nE 165\nM 184,208,132,184\nR 204,244,26112\nW 3\nM 188,192,132,188\nE 166\nM 180,192,116,180\nE 167\nM 172,192,100,172\n"
"R 208,244,26368\nW 3\nM 168,196,104,168\nC 160,160,161\nC 161,162,163\nM 176,196,120,176\nC 146,148,149\nC 147,150,151\nM 184,196,136,184\n"
"C 154,156,157\ns_and_b32 s71, s21, 3\ns_lshl_b32 s71, s71, 1\ns_mul_i32 s28, s58, 160\ns_mul_i32 s27, s59, 1310720\ns_add_u32 s27, s27, s28\n"
"s_add_u32 s44, s12, s27\ns_addc_u32 s45, s13, 0\ns_and_b32 s45, s45, 65535\ns_mov_b32 s46, 0\ns_mov_b32 s47, 131072\ns_sub_u32 s46, 1310720, s28\n"
"s_mul_i32 s31, s20, 5120\ns_mul_i32 s27, s21, 184320\ns_add_u32 s27, s27, s28\ns_add_u32 s40, s14, s27\ns_addc_u32 s41, s15, 0\n"
"s_and_b32 s41, s41, 65535\ns_mov_b32 s42, 0\ns_mov_b32 s43, 131072\ns_sub_u32 s42, 184320, s28\ns_add_u32 s48, s16, 0\ns_addc_u32 s49, s17, 0\n"
"s_and_b32 s49, s49, 65535\ns_mov_b32 s50, 67108864\ns_mov_b32 s51, 131072\ns_mul_i32 s64, s56, 36864\ns_and_b32 s26, s56, 4294967288\n"
"s_mul_i32 s65, s26, 36864\ns_lshl_b32 s66, s26, 8\ns_add_u32 s66, s66, 33554432\ns_lshl_b32 s26, s21, 3\ns_add_u32 s26, s26, s58\n"
"s_mul_i32 s67, s26, 196608\ns_add_u32 s67, s67, 16777216\ns_mov_b32 s72, 98304\ns_mul_i32 s26, s57, 24576\ns_add_u32 s72, s72, s26\n"
"s_mov_b32 s75, 159872\ns_mul_i32 s26, s57, 192\ns_add_u32 s75, s75, s26\ns_mov_b32 s76, 8192\ns_mul_i32 s26, s57, 12288\ns_add_u32 s76, s76, s26\n"
"s_mov_b32 s73, 159744\ns_mul_i32 s26, s19, 192\ns_add_u32 s73, s73, s26\ns_add_u32 s26, s71, 0\ns_cmp_ge_u32 s26, 7\ns_cselect_b32 s27, -6, 1\n"
"s_add_u32 s26, s26, s27\ns_add_u32 s26, s26, s58\ns_and_b32 s26, s26, 7\ns_mul_i32 s5, s26, 36864\ns_add_u32 s5, s5, s65\ns_add_u32 s26, s71, 1\n"
"s_cmp_ge_u32 s26, 7\ns_cselect_b32 s27, -6, 1\ns_add_u32 s26, s26, s27\ns_add_u32 s26, s26, s58\ns_and_b32 s26, s26, 7\ns_mul_i32 s6, s26, 36864\n"
"s_add_u32 s6, s6, s65\ns_add_u32 s26, s71, 2\ns_cmp_ge_u32 s26, 7\ns_cselect_b32 s27, -6, 1\ns_add_u32 s26, s26, s27\ns_add_u32 s26, s26, s58\n"
"s_and_b32 s26, s26, 7\ns_mul_i32 s7, s26, 36864\ns_add_u32 s7, s7, s65\ns_add_u32 s26, s71, 3\ns_cmp_ge_u32 s26, 7\ns_cselect_b32 s27, -6, 1\n"
"s_add_u32 s26, s26, s27\ns_add_u32 s26, s26, s58\ns_and_b32 s26, s26, 7\ns_mul_i32 s8, s26, 36864\ns_add_u32 s8, s8, s65\ns_add_u32 s26, s71, 4\n"
"s_cmp_ge_u32 s26, 7\ns_cselect_b32 s27, -6, 1\ns_add_u32 s26, s26, s27\ns_add_u32 s26, s26, s58\ns_and_b32 s26, s26, 7\ns_mul_i32 s9, s26, 36864\n"
"s_add_u32 s9, s9, s65\ns_add_u32 s26, s71, 5\ns_cmp_ge_u32 s26, 7\ns_cselect_b32 s27, -6, 1\ns_add_u32 s26, s26, s27\ns_add_u32 s26, s26, s58\n"
"s_and_b32 s26, s26, 7\ns_mul_i32 s10, s26, 36864\ns_add_u32 s10, s10, s65\ns_add_u32 s26, s71, 6\ns_cmp_ge_u32 s26, 7\ns_cselect_b32 s27, -6, 1\n"
"s_add_u32 s26, s26, s27\ns_add_u32 s26, s26, s58\ns_and_b32 s26, s26, 7\ns_mul_i32 s11, s26, 36864\ns_add_u32 s11, s11, s65\ns_mov_b32 s4, 0\n"
"s_mov_b32 s77, 0\ns_mov_b32 s78, 24576\ns_mul_i32 s26, s57, 6144\ns_add_u32 s78, s78, s26\ns_mov_b32 s79, 32768\ns_mul_i32 s26, s57, 2048\n"
"s_add_u32 s79, s79, s26\ns_mov_b32 s80, 0\ns_mul_i32 s26, s57, 10240\ns_add_u32 s80, s80, s26\ns_mul_i32 s28, s59, 12288\ns_lshl_b32 s56, s19, 10\n"
"s_lshl_b32 s29, s28, 2\ns_add_u32 s29, s29, s67\ns_add_u32 s28, s28, s67\ns_xor_b32 s26, s59, 1\ns_mul_i32 s26, s26, 12288\ns_add_u32 s23, s26, s29\n"
"s_lshl_b32 s26, s26, 2\ns_add_u32 s26, s26, s28\ns_add_u32 s12, s26, s56\ns_xor_b32 s26, s59, 2\ns_mul_i32 s26, s26, 12288\ns_add_u32 s24, s26, s29\n"
"s_lshl_b32 s26, s26, 2\ns_add_u32 s26, s26, s28\ns_add_u32 s13, s26, s56\ns_xor_b32 s26, s59, 3\ns_mul_i32 s26, s26, 12288\ns_add_u32 s54, s26, s29\n"
"s_lshl_b32 s26, s26, 2\ns_add_u32 s26, s26, s28\ns_add_u32 s14, s26, s56\ns_lshl_b32 s26, s57, 10\ns_add_u32 s26, s26, 9216\ns_mul_i32 s27, s25, 49152\n"
"s_add_u32 s27, s27, s28\ns_add_u32 s15, s26, s27\ns_cmp_eq_u32 s59, s25\ns_cselect_b32 s69, s26, 268435456\ns_cselect_b32 s99, s34, 0\n"
"s_xor_b32 s26, s59, s20\ns_mul_i32 s26, s26, 49152\ns_add_u32 s26, s26, s28\ns_add_u32 s22, s26, 8192\ns_cmp_eq_u32 s20, 0\n"
"s_cselect_b32 s98, 0, 268435456\ns_cselect_b32 s67, s34, 0\ns_add_u32 s98, s98, 8192\ns_lshl_b32 s26, s59, 1\nv_add_u32 v254, 0, v255\n"
"v_mul_u32_u24 v254, 205, v254\nv_lshrrev_b32 v252, 11, v254\nv_add_u32 v247, 0, v255\nv_mul_u32_u24 v254, 10, v252\nv_sub_u32 v253, v247, v254\n"
"v_xor_b32 v254, s26, v253\nv_cmp_gt_u32 vcc, 8, v253\ns_nop 1\nv_cndmask_b32 v253, v253, v254, vcc\nv_mul_u32_u24 v247, 1280, v252\n"
"v_lshlrev_b32 v254, 4, v253\nv_add_u32 v247, v247, v254\nv_subrev_u32 v247, 0, v247\nv_add_u32 v254, 64, v255\nv_mul_u32_u24 v254, 205, v254\n"
"v_lshrrev_b32 v252, 11, v254\nv_add_u32 v248, 64, v255\nv_mul_u32_u24 v254, 10, v252\nv_sub_u32 v253, v248, v254\nv_xor_b32 v254, s26, v253\n"
"v_cmp_gt_u32 vcc, 8, v253\ns_nop 1\nv_cndmask_b32 v253, v253, v254, vcc\nv_mul_u32_u24 v248, 1280, v252\nv_lshlrev_b32 v254, 4, v253\n"
"v_add_u32 v248, v248, v254\nv_subrev_u32 v248, 1024, v248\nv_add_u32 v254, 128, v255\nv_mul_u32_u24 v254, 205, v254\nv_lshrrev_b32 v252, 11, v254\n"
"v_add_u32 v249, 128, v255\nv_mul_u32_u24 v254, 10, v252\nv_sub_u32 v253, v249, v254\nv_xor_b32 v254, s26, v253\nv_cmp_gt_u32 vcc, 8, v253\ns_nop 1\n"
"v_cndmask_b32 v253, v253, v254, vcc\nv_mul_u32_u24 v249, 1280, v252\nv_lshlrev_b32 v254, 4, v253\nv_add_u32 v249, v249, v254\n"
"v_subrev_u32 v249, 2048, v249\nv_add_u32 v254, 192, v255\nv_mul_u32_u24 v254, 205, v254\nv_lshrrev_b32 v252, 11, v254\nv_add_u32 v250, 192, v255\n"
"v_mul_u32_u24 v254, 10, v252\nv_sub_u32 v253, v250, v254\nv_xor_b32 v254, s26, v253\nv_cmp_gt_u32 vcc, 8, v253\ns_nop 1\n"
"v_cndmask_b32 v253, v253, v254, vcc\nv_mul_u32_u24 v250, 1280, v252\nv_lshlrev_b32 v254, 4, v253\nv_add_u32 v250, v250, v254\n"
"v_subrev_u32 v250, 3072, v250\nv_add_u32 v254, 256, v255\nv_mul_u32_u24 v254, 205, v254\nv_lshrrev_b32 v252, 11, v254\nv_add_u32 v251, 256, v255\n"
"v_mul_u32_u24 v254, 10, v252\nv_sub_u32 v253, v251, v254\nv_xor_b32 v254, s26, v253\nv_cmp_gt_u32 vcc, 8, v253\ns_nop 1\n"
"v_cndmask_b32 v253, v253, v254, vcc\nv_mul_u32_u24 v251, 1280, v252\nv_lshlrev_b32 v254, 4, v253\nv_add_u32 v251, v251, v254\n"
"v_subrev_u32 v251, 0, v251\nv_lshrrev_b32 v254, 4, v255\nv_lshrrev_b32 v253, 2, v255\nv_xor_b32 v254, v254, v253\nv_and_b32 v224, 3, v254\n"
"v_cmp_eq_u32 vcc, 0, v224\nv_and_b32 v254, 1, v255\nv_lshlrev_b32 v254, 4, v254\nv_mov_b32 v253, 16256\nv_lshlrev_b32 v225, v254, v253\n"
"v_cndmask_b32 v225, 0, v225, vcc\nv_and_b32 v224, 2, v255\nv_cmp_eq_u32 vcc, 0, v224\ns_nop 1\nv_cndmask_b32 v224, 0, v225, vcc\n"
"v_xor_b32 v225, v225, v224\nv_lshrrev_b32 v254, 2, v255\nv_mul_u32_u24 v254, 160, v254\nv_and_b32 v252, 3, v255\nv_lshlrev_b32 v252, 3, v252\n"
"v_add_u32 v254, v254, v252\nv_add_u32 v254, s80, v254\nv_add_u32 v253, 36864, v254\nv_add_u32 v227, 5120, v253\nv_and_b32 v254, 15, v255\n"
"v_mul_u32_u24 v254, 1280, v254\nv_lshrrev_b32 v252, 4, v255\nv_lshl_add_u32 v254, v252, 3, v254\nv_lshlrev_b32 v252, 4, v255\nW 0\ns_barrier\n"
"v_add_u32 v245, 0x10000, v245\nv_add_u32 v246, 0x10000, v246\nT 192,245,16384\nT 194,245,16640\nM 188,200,136,188\nT 196,246,16384\nT 198,246,16640\n"
"M 180,200,120,180\nC 155,158,159\nC 162,164,165\nM 172,200,104,172\nC 163,166,167\nM 168,204,108,168\nT 200,245,16896\nT 202,245,17152\n"
"M 176,204,124,176\nM 184,204,140,184\nM 188,208,140,188\nT 204,246,16896\nT 206,246,17152\nM 180,208,124,180\nM 172,208,108,172\nW 6\nM 0,192,144,0\n"
"T 208,245,17408\nT 210,245,17664\nE 168\nM 212,240,144,212\nE 169\nE 170\nM 32,192,152,32\nE 171\nM 64,192,160,64\nC 168,168,169\nW 6\nM 68,196,160,68\n"
"T 192,246,17408\nT 194,246,17664\nM 36,196,152,36\nC 169,170,171\nM 216,240,152,216\nE 176\nM 4,196,144,4\nW 6\nM 8,200,144,8\nT 196,245,17920\n"
"T 198,245,18176\nM 40,200,152,40\nE 177\nM 72,200,160,72\nE 178\nM 220,240,160,220\nC 176,176,177\nW 6\nM 76,204,160,76\nT 200,246,17920\n"
"T 202,246,18176\nM 44,204,152,44\nE 179\nM 12,204,144,12\nC 177,178,179\nW 6\nM 16,208,144,16\nv_add_u32 v244, 0x10000, v244\nR 204,244,24576\n"
"M 48,208,152,48\nE 184\nM 80,208,160,80\nW 5\nM 84,192,160,84\nE 185\nM 52,192,152,52\nE 186\nM 20,192,144,20\nR 208,244,24832\nW 4\nM 24,196,144,24\n"
"E 187\nM 56,196,152,56\nE 172\nM 88,196,160,88\nR 192,244,25088\nW 3\nM 92,200,160,92\nE 173\nM 60,200,152,60\nE 174\nM 28,200,144,28\nR 196,244,25344\n"
"W 3\nM 144,204,96,228\nE 175\nM 152,204,112,232\nE 180\nM 160,204,128,236\nR 200,244,25600\nW 3\nM 164,208,128,236\nE 181\nM 156,208,112,232\nE 182\n"
"M 148,208,96,228\nR 204,244,25856\nW 3\nM 144,192,100,144\nE 183\nM 152,192,116,152\nE 188\nM 160,192,132,160\nR 208,244,26112\nW 3\nM 164,196,132,164\n"
"E 189\nM 156,196,116,156\nE 190\nM 148,196,100,148\nR 192,244,26368\nW 3\nM 144,200,104,144\nT 196,245,24576\nT 198,245,24832\nM 152,200,120,152\n"
"E 191\nM 160,200,136,160\nC 184,184,185\nW 4\nM 164,204,136,164\nT 200,246,24576\nT 202,246,24832\nM 156,204,120,156\nC 185,186,187\nC 170,172,173\n"
"M 148,204,104,148\nC 171,174,175\nW 5\nM 144,208,108,144\nT 204,245,25088\nT 206,245,25344\nC 178,180,181\nM 152,208,124,152\nC 179,182,183\n"
"C 186,188,189\nM 160,208,140,160\nC 187,190,191\nW 6\nM 164,192,140,164\nT 208,246,25088\nT 210,246,25344\nM 156,192,124,156\nM 148,192,108,148\nW 6\n"
"M 0,196,168,0\nT 192,245,25600\nT 194,245,25856\nE 144\nM 212,240,168,212\nE 145\nE 146\nM 32,196,176,32\nE 147\nM 64,196,184,64\nC 144,144,145\nW 6\n"
"M 68,200,184,68\nT 196,246,25600\nT 198,246,25856\nM 36,200,176,36\nC 145,146,147\nM 216,240,176,216\nE 152\nM 4,200,168,4\nW 6\nM 8,204,168,8\n"
"T 200,245,26112\nT 202,245,26368\nM 40,204,176,40\nE 153\nM 72,204,184,72\nE 154\nM 220,240,184,220\nC 152,152,153\nW 6\nM 76,208,184,76\n"
"T 204,246,26112\nT 206,246,26368\nM 44,208,176,44\nE 155\nM 12,208,168,12\nC 153,154,155\nW 6\nM 16,192,168,16\nE 160\nM 48,192,176,48\nE 161\n"
"M 80,192,184,80\nC 160,160,161\nW 0\ns_barrier\nv_add_u32 v244, 0xfffe8000, v244\nR 208,244,16384\nM 84,196,184,84\nE 162\nM 52,196,176,52\nE 163\n"
"M 20,196,168,20\nR 192,244,16640\nM 24,200,168,24\nE 148\nM 56,200,176,56\nE 149\nM 88,200,184,88\nR 196,244,16896\nM 92,204,184,92\nE 150\n"
"M 60,204,176,60\nE 151\nM 28,204,168,28\nR 200,244,17152\nW 3\nM 168,208,96,228\nE 156\nM 176,208,112,232\nE 157\nM 184,208,128,236\nR 204,244,17408\n"
"W 3\nM 188,192,128,236\nE 158\nM 180,192,112,232\nE 159\nM 172,192,96,228\nR 208,244,17664\nW 3\nM 168,196,100,168\nE 164\nM 176,196,116,176\nE 165\n"
"M 184,196,132,184\nR 192,244,17920\nW 3\nM 188,200,132,188\nE 166\nM 180,200,116,180\nE 167\nM 172,200,100,172\nR 196,244,18176\nW 3\n"
"M 168,204,104,168\nv_add_u32 v245, 0x10000, v245\nv_add_u32 v246, 0x10000, v246\nM 176,204,120,176\nT 200,245,24576\nT 202,245,24832\n"
"M 184,204,136,184\nC 161,162,163\nW 4\nM 188,208,136,188\nT 204,246,24576\nT 206,246,24832\nM 180,208,120,180\nC 146,148,149\nC 147,150,151\n"
"M 172,208,104,172\nC 154,156,157\nW 5\nM 168,192,108,168\nT 208,245,25088\nT 210,245,25344\nM 176,192,124,176\nC 155,158,159\nC 162,164,165\n"
"M 184,192,140,184\nC 163,166,167\nW 6\nM 188,196,140,188\nT 192,246,25088\nT 194,246,25344\nM 180,196,124,180\nM 172,196,108,172\nW 6\nM 0,200,144,0\n"
"T 196,245,25600\nT 198,245,25856\nE 168\nM 212,240,144,212\nE 169\nE 170\nM 32,200,152,32\nE 171\nM 64,200,160,64\nC 168,168,169\nW 6\nM 68,204,160,68\n"
"T 200,246,25600\nT 202,246,25856\nM 36,204,152,36\nC 169,170,171\nM 216,240,152,216\nE 176\nM 4,204,144,4\nW 6\nM 8,208,144,8\nT 204,245,26112\n"
"T 206,245,26368\nM 40,208,152,40\nE 177\nM 72,208,160,72\nE 178\nM 220,240,160,220\nC 176,176,177\nW 6\nM 76,192,160,76\nT 208,246,26112\n"
"T 210,246,26368\nM 44,192,152,44\nE 179\nM 12,192,144,12\nC 177,178,179\nW 6\nM 16,196,144,16\nE 184\nM 48,196,152,48\nE 185\nM 80,196,160,80\n"
"R 192,244,24576\nW 5\nM 84,200,160,84\nE 186\nM 52,200,152,52\nE 187\nM 20,200,144,20\nR 196,244,24832\nW 4\nM 24,204,144,24\nE 172\nM 56,204,152,56\n"
"E 173\nM 88,204,160,88\nR 200,244,25088\nW 3\nM 92,208,160,92\nE 174\nM 60,208,152,60\nE 175\nM 28,208,144,28\nR 204,244,25344\nW 3\nM 144,192,96,228\n"
"E 180\nM 152,192,112,232\nE 181\nM 160,192,128,236\nR 208,244,25600\nW 3\nM 164,196,128,236\nE 182\nM 156,196,112,232\nE 183\nM 148,196,96,228\n"
"R 192,244,25856\nW 3\nM 144,200,100,144\nE 188\nM 152,200,116,152\nE 189\nM 160,200,132,160\nR 196,244,26112\nW 3\nM 164,204,132,164\nE 190\n"
"M 156,204,116,156\nE 191\nM 148,204,100,148\nR 200,244,26368\nW 3\nM 144,208,104,144\nC 184,184,185\nC 185,186,187\nM 152,208,120,152\nC 170,172,173\n"
"C 171,174,175\nM 160,208,136,160\nC 178,180,181\nW 0\ns_barrier\nv_add_u32 v245, 0xfffe8000, v245\nv_add_u32 v246, 0xfffe8000, v246\nT 204,245,16384\n"
"T 206,245,16640\nM 164,192,136,164\nT 208,246,16384\nT 210,246,16640\nM 156,192,120,156\nC 179,182,183\nC 186,188,189\nM 148,192,104,148\n"
"C 187,190,191\nM 144,196,108,144\nT 192,245,16896\nT 194,245,17152\nM 152,196,124,152\nM 160,196,140,160\nM 164,200,140,164\nT 196,246,16896\n"
"T 198,246,17152\nM 156,200,124,156\nM 148,200,108,148\nW 6\nM 0,204,168,0\nT 200,245,17408\nT 202,245,17664\nE 144\nM 212,240,168,212\nE 145\nE 146\n"
"M 32,204,176,32\nE 147\nM 64,204,184,64\nC 144,144,145\nW 6\nM 68,208,184,68\nT 204,246,17408\nT 206,246,17664\nM 36,208,176,36\nC 145,146,147\n"
"M 216,240,176,216\nE 152\nM 4,208,168,4\nW 6\nM 8,192,168,8\nT 208,245,17920\nT 210,245,18176\nM 40,192,176,40\nE 153\nM 72,192,184,72\nE 154\n"
"M 220,240,184,220\nC 152,152,153\nW 6\nM 76,196,184,76\nT 192,246,17920\nT 194,246,18176\nM 44,196,176,44\nE 155\nM 12,196,168,12\nC 153,154,155\nW 6\n"
"M 16,200,168,16\nv_add_u32 v244, 0x10000, v244\nR 196,244,24576\nM 48,200,176,48\nE 160\nM 80,200,184,80\nW 5\nM 84,204,184,84\nE 161\nM 52,204,176,52\n"
"E 162\nM 20,204,168,20\nR 200,244,24832\nW 4\nM 24,208,168,24\nE 163\nM 56,208,176,56\nE 148\nM 88,208,184,88\nR 204,244,25088\nW 3\nM 92,192,184,92\n"
"E 149\nM 60,192,176,60\nE 150\nM 28,192,168,28\nR 208,244,25344\nW 3\nM 168,196,96,228\nE 151\nM 176,196,112,232\nE 156\nM 184,196,128,236\n"
"R 192,244,25600\nW 3\nM 188,200,128,236\nE 157\nM 180,200,112,232\nE 158\nM 172,200,96,228\nR 196,244,25856\nW 3\nM 168,204,100,168\nE 159\n"
"M 176,204,116,176\nE 164\nM 184,204,132,184\nR 200,244,26112\nW 3\nM 188,208,132,188\nE 165\nM 180,208,116,180\nE 166\nM 172,208,100,172\n"
"R 204,244,26368\nW 3\nM 168,192,104,168\nT 208,245,24576\nT 210,245,24832\nM 176,192,120,176\nE 167\nM 184,192,136,184\nC 160,160,161\nW 4\n"
"M 188,196,136,188\nT 192,246,24576\nT 194,246,24832\nM 180,196,120,180\nC 161,162,163\nC 146,148,149\nM 172,196,104,172\nC 147,150,151\nW 5\n"
"M 168,200,108,168\nT 196,245,25088\nT 198,245,25344\nC 154,156,157\nM 176,200,124,176\nC 155,158,159\nC 162,164,165\nM 184,200,140,184\nC 163,166,167\n"
"W 6\nM 188,204,140,188\nT 200,246,25088\nT 202,246,25344\nM 180,204,124,180\nM 172,204,108,172\nW 6\nM 0,208,144,0\nT 204,245,25600\nT 206,245,25856\n"
"E 168\nM 212,240,144,212\nE 169\nE 170\nM 32,208,152,32\nE 171\nM 64,208,160,64\nC 168,168,169\nW 6\nM 68,192,160,68\nT 208,246,25600\nT 210,246,25856\n"
"M 36,192,152,36\nC 169,170,171\nM 216,240,152,216\nE 176\nM 4,192,144,4\nW 6\nM 8,196,144,8\nT 192,245,26112\nT 194,245,26368\nM 40,196,152,40\nE 177\n"
"M 72,196,160,72\nE 178\nM 220,240,160,220\nC 176,176,177\nW 6\nM 76,200,160,76\nT 196,246,26112\nT 198,246,26368\nM 44,200,152,44\nE 179\n"
"M 12,200,144,12\nC 177,178,179\nW 6\nM 16,204,144,16\nE 184\nM 48,204,152,48\nE 185\nM 80,204,160,80\nC 184,184,185\nW 0\ns_barrier\n"
"v_add_u32 v244, 0x10000, v244\nR 200,244,16384\nM 84,208,160,84\nE 186\nM 52,208,152,52\nE 187\nM 20,208,144,20\nR 204,244,16640\nM 24,192,144,24\n"
"E 172\nM 56,192,152,56\nE 173\nM 88,192,160,88\nR 208,244,16896\nM 92,196,160,92\nE 174\nM 60,196,152,60\nE 175\nM 28,196,144,28\nR 192,244,17152\nW 3\n"
"M 144,200,96,228\nE 180\nM 152,200,112,232\nE 181\nM 160,200,128,236\nR 196,244,17408\nW 3\nM 164,204,128,236\nE 182\nM 156,204,112,232\nE 183\n"
"M 148,204,96,228\nR 200,244,17664\nW 3\nM 144,208,100,144\nE 188\nM 152,208,116,152\nE 189\nM 160,208,132,160\nR 204,244,17920\nW 3\nM 164,192,132,164\n"
"E 190\nM 156,192,116,156\nE 191\nM 148,192,100,148\nR 208,244,18176\nW 3\nM 144,196,104,144\nv_add_u32 v245, 0x10000, v245\n"
"v_add_u32 v246, 0x10000, v246\nM 152,196,120,152\nT 192,245,24576\nT 194,245,24832\nM 160,196,136,160\nC 185,186,187\nW 4\nM 164,200,136,164\n"
"T 196,246,24576\nT 198,246,24832\nM 156,200,120,156\nC 170,172,173\nC 171,174,175\nM 148,200,104,148\nC 178,180,181\nW 5\nM 144,204,108,144\n"
"T 200,245,25088\nT 202,245,25344\nM 152,204,124,152\nC 179,182,183\nC 186,188,189\nM 160,204,140,160\nC 187,190,191\nW 6\nM 164,208,140,164\n"
"T 204,246,25088\nT 206,246,25344\nM 156,208,124,156\nM 148,208,108,148\nW 6\nM 0,192,168,0\nT 208,245,25600\nT 210,245,25856\nE 144\nM 212,240,168,212\n"
"E 145\nE 146\nM 32,192,176,32\nE 147\nM 64,192,184,64\nC 144,144,145\nW 6\nM 68,196,184,68\nT 192,246,25600\nT 194,246,25856\nM 36,196,176,36\n"
"C 145,146,147\nM 216,240,176,216\nE 152\nM 4,196,168,4\nW 6\nM 8,200,168,8\nT 196,245,26112\nT 198,245,26368\nM 40,200,176,40\nE 153\nM 72,200,184,72\n"
"E 154\nM 220,240,184,220\nC 152,152,153\nW 6\nM 76,204,184,76\nT 200,246,26112\nT 202,246,26368\nM 44,204,176,44\nE 155\nM 12,204,168,12\n"
"C 153,154,155\nW 6\nM 16,208,168,16\nE 160\nM 48,208,176,48\nE 161\nM 80,208,184,80\nR 204,244,24576\nW 5\nM 84,192,184,84\nE 162\nM 52,192,176,52\n"
"E 163\nM 20,192,168,20\nR 208,244,24832\nW 4\nM 24,196,168,24\nE 148\nM 56,196,176,56\nE 149\nM 88,196,184,88\nR 192,244,25088\nW 3\nM 92,200,184,92\n"
"E 150\nM 60,200,176,60\nE 151\nM 28,200,168,28\nR 196,244,25344\nW 3\nM 168,204,96,228\nE 156\nM 176,204,112,232\nE 157\nM 184,204,128,236\n"
"R 200,244,25600\nW 3\nM 188,208,128,236\nE 158\nM 180,208,112,232\nE 159\nM 172,208,96,228\nR 204,244,25856\nW 3\nM 168,192,100,168\nE 164\n"
"M 176,192,116,176\nE 165\nM 184,192,132,184\nR 208,244,26112\nW 3\nM 188,196,132,188\nE 166\nM 180,196,116,180\nE 167\nM 172,196,100,172\n"
"R 192,244,26368\nW 3\nM 168,200,104,168\nC 160,160,161\nC 161,162,163\nM 176,200,120,176\nC 146,148,149\nC 147,150,151\nM 184,200,136,184\n"
"C 154,156,157\nW 0\ns_barrier\nv_add_u32 v245, 0xfffe8000, v245\nv_add_u32 v246, 0xfffe8000, v246\nT 196,245,16384\nT 198,245,16640\nM 188,204,136,188\n"
"T 200,246,16384\nT 202,246,16640\nC 155,158,159\nM 180,204,120,180\nC 162,164,165\nC 163,166,167\nM 172,204,104,172\nM 168,208,108,168\n"
"T 204,245,16896\nT 206,245,17152\nM 176,208,124,176\nM 184,208,140,184\nM 188,192,140,188\nT 208,246,16896\nT 210,246,17152\nM 180,192,124,180\n"
"M 172,192,108,172\nW 6\nM 0,196,144,0\nT 192,245,17408\nT 194,245,17664\nE 168\nM 212,240,144,212\nE 169\nE 170\nM 32,196,152,32\nE 171\nC 168,168,169\n"
"M 64,196,160,64\nC 169,170,171\nE 176\nW 6\nM 68,200,160,68\nT 196,246,17408\nT 198,246,17664\nE 177\nM 36,200,152,36\nC 176,176,177\nE 178\n"
"M 216,240,152,216\nE 179\nE 184\nM 4,200,144,4\nC 177,178,179\nE 185\nW 6\nM 8,204,144,8\nT 200,245,17920\nT 202,245,18176\nC 184,184,185\n"
"M 40,204,152,40\nE 186\nE 187\nM 72,204,160,72\nE 172\nC 185,186,187\nM 220,240,160,220\nE 173\nW 6\nM 76,208,160,76\nT 204,246,17920\nT 206,246,18176\n"
"C 170,172,173\nM 44,208,152,44\nE 174\nE 175\nM 12,208,144,12\nC 171,174,175\nE 180\nW 6\nM 16,192,144,16\nT 208,245,24576\nT 210,245,24832\nE 181\n"
"M 48,192,152,48\nE 182\nC 178,180,181\nM 80,192,160,80\nE 183\nW 6\nM 84,196,160,84\nT 192,246,24576\nT 194,246,24832\nC 179,182,183\nM 52,196,152,52\n"
"E 188\nM 20,196,144,20\nE 189\nW 6\nM 24,200,144,24\nT 196,245,25088\nT 198,245,25344\nC 186,188,189\nM 56,200,152,56\nE 190\nM 88,200,160,88\nW 6\n"
"M 92,204,160,92\nT 200,246,25088\nT 202,246,25344\nE 191\nM 60,204,152,60\nC 187,190,191\nM 28,204,144,28\nW 6\nM 0,208,168,0\nT 204,245,25600\n"
"T 206,245,25856\nM 212,240,168,212\nv_add_u32 v244, s72, v252\nv_lshrrev_b32 v151, 2, v252\nM 32,208,176,32\nv_and_b32 v151, 60, v151\n"
"v_add_u32 v150, s73, v151\nM 64,208,184,64\nv_add_u32 v151, s75, v151\nW 6\nM 68,192,184,68\nT 208,246,25600\nT 210,246,25856\nM 36,192,176,36\n"
"v_mov_b32 v144, 0\nv_mov_b32 v145, 0\nM 216,240,176,216\nv_mov_b32 v146, v224\nv_mov_b32 v147, v225\nM 4,192,168,4\nv_mov_b32 v148, 0\nW 6\n"
"M 8,196,168,8\nT 192,245,26112\nT 194,245,26368\nM 40,196,176,40\nv_mov_b32 v149, 0\nv_add_u32 v152, s76, v252\nM 72,196,184,72\n"
"v_mov_b32 v153, 161280\nM 220,240,184,220\nW 6\nM 76,200,184,76\nT 196,246,26112\nT 198,246,26368\nM 44,200,176,44\nC 0,0,1\nC 1,2,3\nM 12,200,168,12\n"
"C 2,4,5\nW 6\nM 16,204,168,16\nC 3,6,7\nds_write_b128 v244, v[0:3] offset:1024\nM 48,204,176,48\nC 32,32,33\nC 33,34,35\nM 80,204,184,80\nC 34,36,37\n"
"W 5\nM 84,208,184,84\nC 35,38,39\nds_write_b128 v244, v[32:35] offset:9216\nds_write_b32 v150, v212\nM 52,208,176,52\n"
"ds_write_b32 v150, v216 offset:64\nds_write_b32 v150, v220 offset:128\nM 20,208,168,20\nC 8,8,9\nW 7\nM 24,192,168,24\nC 9,10,11\nC 10,12,13\n"
"C 11,14,15\nM 56,192,176,56\nds_write_b128 v244, v[8:11] offset:3072\nC 40,40,41\nM 88,192,184,88\nC 41,42,43\nW 6\nM 92,196,184,92\nC 42,44,45\n"
"C 43,46,47\nds_write_b128 v244, v[40:43] offset:11264\nM 60,196,176,60\nC 16,16,17\nC 17,18,19\nC 18,20,21\nM 28,196,168,28\nC 19,22,23\n"
"ds_write_b128 v244, v[16:19] offset:5120\nC 48,48,49\nC 49,50,51\nC 50,52,53\nC 51,54,55\nds_write_b128 v244, v[48:51] offset:13312\nC 24,24,25\n"
"C 25,26,27\nC 26,28,29\nC 27,30,31\nds_write_b128 v244, v[24:27] offset:7168\nC 56,56,57\nC 57,58,59\nC 58,60,61\nC 59,62,63\n"
"ds_write_b128 v244, v[56:59] offset:15360\nv_mov_b32 v240, v220\nW 0\ns_barrier\nds_read_b32 v176, v151 offset:384\nds_read_b32 v177, v151 offset:768\n"
"R 192,244,16384\nR 196,244,17408\nR 200,244,18432\nR 204,244,19456\nR 208,244,20480\nR 212,244,21504\nR 216,244,22528\nR 220,244,23552\nW 9\n"
"v_add_f32 v240, v240, v176\nW 8\nv_add_f32 v240, v240, v177\nv_cmp_ngt_f32 vcc, 0x53800000, v240\nv_rcp_f32 v170, v240\nv_rcp_f32 v171, v240\n"
"s_cmp_lg_u64 vcc, 0\ns_cbranch_scc0 .Lw_92_%=\nds_write_b32 v153, v153 offset:4\n.Lw_92_%=:\nW 7\nM 64,146,192,64\nM 68,144,192,68\nW 6\n"
"M 64,146,196,64\nM 68,144,196,68\nW 5\nM 72,146,200,72\nM 76,144,200,76\nW 4\nM 72,146,204,72\nM 76,144,204,76\ns_nop 0\n"
"v_pk_mul_f32 v[64:65], v[64:65], v[170:171]\nW 3\nM 80,146,208,80\nv_pk_mul_f32 v[66:67], v[66:67], v[170:171]\n"
"v_pk_mul_f32 v[68:69], v[68:69], v[170:171]\nM 84,144,208,84\nv_pk_mul_f32 v[70:71], v[70:71], v[170:171]\nC 64,64,65\nW 2\nM 80,146,212,80\n"
"C 65,66,67\nC 66,68,69\nM 84,144,212,84\nC 67,70,71\nbuffer_store_dwordx4 v[64:67], v152, s[48:51], s64 offen sc1\n"
"v_pk_mul_f32 v[72:73], v[72:73], v[170:171]\nW 1\nM 88,146,216,88\nv_pk_mul_f32 v[74:75], v[74:75], v[170:171]\n"
"v_pk_mul_f32 v[76:77], v[76:77], v[170:171]\nM 92,144,216,92\nv_pk_mul_f32 v[78:79], v[78:79], v[170:171]\nC 72,72,73\nW 0\nM 88,146,220,88\n"
"C 73,74,75\nC 74,76,77\nM 92,144,220,92\nC 75,78,79\nbuffer_store_dwordx4 v[72:75], v152, s[48:51], s64 offen offset:1024 sc1\n"
"v_pk_mul_f32 v[80:81], v[80:81], v[170:171]\nv_pk_mul_f32 v[82:83], v[82:83], v[170:171]\nv_pk_mul_f32 v[84:85], v[84:85], v[170:171]\n"
"v_pk_mul_f32 v[86:87], v[86:87], v[170:171]\nC 80,80,81\nC 81,82,83\nC 82,84,85\nC 83,86,87\n"
"buffer_store_dwordx4 v[80:83], v152, s[48:51], s64 offen offset:2048 sc1\nv_pk_mul_f32 v[88:89], v[88:89], v[170:171]\n"
"v_pk_mul_f32 v[90:91], v[90:91], v[170:171]\nv_pk_mul_f32 v[92:93], v[92:93], v[170:171]\nv_pk_mul_f32 v[94:95], v[94:95], v[170:171]\nC 88,88,89\n"
"C 89,90,91\nC 90,92,93\nC 91,94,95\nbuffer_store_dwordx4 v[88:91], v152, s[48:51], s64 offen offset:3072 sc1\nds_write_b128 v152, v[64:67]\n"
"ds_write_b128 v152, v[72:75] offset:1024\nds_write_b128 v152, v[80:83] offset:2048\nds_write_b128 v152, v[88:91] offset:3072\n"
"v_add_u32 v154, s78, v252\nv_xor_b32 v155, 4096, v154\n.Lt1_10_%=:\ns_nop 1\nW 0\ns_barrier\nds_read_b32 v244, v153 offset:4\nR 184,154,0\nR 188,155,0\n"
"R 192,154,1024\nR 196,155,1024\nT 60,253,0\nT 62,253,2560\nT 64,253,32\nT 66,253,2592\nT 68,253,64\nT 70,253,2624\nT 72,253,96\nT 74,253,2656\n"
"T 76,253,128\nT 78,253,2688\nW 14\nv_readfirstlane_b32 s28, v244\ns_cmp_eq_u32 s35, 0\ns_cselect_b32 s28, s28, 0\ns_cmp_lg_u32 s28, 0\n"
"s_cbranch_scc1 .Lfb1_12_%=\ns_add_u32 s26, s71, 2\ns_cmp_ge_u32 s26, 7\ns_cselect_b32 s27, -6, 1\ns_add_u32 s26, s26, s27\ns_add_u32 s26, s26, s58\n"
"s_and_b32 s26, s26, 7\ns_lshl_b32 s26, s26, 2\ns_add_u32 s26, s26, s20\ns_mul_i32 s52, s26, 40960\ns_add_u32 m0, s31, 98304\ns_nop 0\n"
"buffer_load_dwordx4 v247, s[44:47], s52 offen lds\nbuffer_load_dwordx4 v248, s[44:47], s52 offen offset:1024 lds\n"
"buffer_load_dwordx4 v249, s[44:47], s52 offen offset:2048 lds\nbuffer_load_dwordx4 v250, s[44:47], s52 offen offset:3072 lds\n"
"s_add_u32 m0, s31, 102400\ns_nop 0\nbuffer_load_dwordx4 v251, s[44:47], s52 offen lds\nW 8\nv_mfma_f32_16x16x32_bf16 v[20:23], v[60:63], v[188:191], 0\n"
"T 80,227,0\nT 82,227,2560\nv_mfma_f32_16x16x32_bf16 v[0:3], v[60:63], v[184:187], 0\nv_add_u32 v84, 61440, v253\nW 8\n"
"v_mfma_f32_16x16x32_bf16 v[4:7], v[64:67], v[184:187], 0\nT 60,227,32\nT 62,227,2592\nv_mfma_f32_16x16x32_bf16 v[24:27], v[64:67], v[188:191], 0\n"
"v_add_u32 v85, 61440, v227\nW 8\nv_mfma_f32_16x16x32_bf16 v[28:31], v[68:71], v[188:191], 0\nT 64,227,64\nT 66,227,2624\n"
"v_mfma_f32_16x16x32_bf16 v[8:11], v[68:71], v[184:187], 0\nv_add_u32 v86, 4294930432, v253\nds_read_b32 v243, v153\nW 0\nv_readfirstlane_b32 s4, v243\n"
"s_cmp_eq_u32 s77, 0\ns_cbranch_scc0 .Lw_93_%=\ns_cmp_ge_u32 s4, 1\ns_cbranch_scc0 .Lw_94_%=\ns_mov_b32 s77, 1\n"
"buffer_load_dwordx4 v[164:167], v154, s[48:51], s5 offen sc1\nbuffer_load_dwordx4 v[168:171], v155, s[48:51], s5 offen sc1\n"
"buffer_load_dwordx4 v[172:175], v154, s[48:51], s5 offen offset:1024 sc1\nbuffer_load_dwordx4 v[176:179], v155, s[48:51], s5 offen offset:1024 sc1\n"
".Lw_94_%=:\n.Lw_93_%=:\ns_cmp_eq_u32 s77, 1\ns_cbranch_scc0 .Lw_95_%=\ns_cmp_ge_u32 s4, 2\ns_cbranch_scc0 .Lw_96_%=\ns_mov_b32 s77, 2\n"
"buffer_load_dwordx4 v[204:207], v154, s[48:51], s6 offen sc1\nbuffer_load_dwordx4 v[208:211], v155, s[48:51], s6 offen sc1\n"
"buffer_load_dwordx4 v[212:215], v154, s[48:51], s6 offen offset:1024 sc1\nbuffer_load_dwordx4 v[216:219], v155, s[48:51], s6 offen offset:1024 sc1\n"
".Lw_96_%=:\n.Lw_95_%=:\nv_mfma_f32_16x16x32_bf16 v[12:15], v[72:75], v[184:187], 0\nT 68,227,96\nT 70,227,2656\n"
"v_mfma_f32_16x16x32_bf16 v[32:35], v[72:75], v[188:191], 0\nv_add_u32 v87, 4294930432, v227\n"
"v_mfma_f32_16x16x32_bf16 v[36:39], v[76:79], v[188:191], 0\nT 72,227,128\nT 74,227,2688\nv_mfma_f32_16x16x32_bf16 v[16:19], v[76:79], v[184:187], 0\n"
"s_mov_b64 s[60:61], s[48:49]\ns_mov_b32 s63, 131072\nM 20,80,196,20\nT 76,253,20480\nT 78,253,23040\nM 0,80,192,0\nM 4,60,192,4\nT 80,253,20512\n"
"T 82,253,23072\nM 24,60,196,24\nM 28,64,196,28\nT 60,253,20544\nT 62,253,23104\nM 8,64,192,8\nW 8\nM 12,68,192,12\nT 64,253,20576\nT 66,253,23136\n"
"M 32,68,196,32\nW 8\nM 36,72,196,36\nT 68,253,20608\nT 70,253,23168\nM 16,72,192,16\ns_cmp_eq_u32 s77, 0\ns_cbranch_scc0 .Lw_97_%=\ns_waitcnt vmcnt(0)\n"
".Lw_97_%=:\ns_cmp_eq_u32 s77, 1\ns_cbranch_scc0 .Lw_98_%=\ns_waitcnt vmcnt(4)\n.Lw_98_%=:\ns_waitcnt vmcnt(8)\nW 0\ns_barrier\nds_read_b32 v243, v153\n"
"W 0\nv_readfirstlane_b32 s4, v243\ns_cmp_eq_u32 s77, 0\ns_cbranch_scc0 .Lw_99_%=\ns_cmp_ge_u32 s4, 1\ns_cbranch_scc0 .Lw_100_%=\ns_mov_b32 s77, 1\n"
"buffer_load_dwordx4 v[164:167], v154, s[48:51], s5 offen sc1\nbuffer_load_dwordx4 v[168:171], v155, s[48:51], s5 offen sc1\n"
"buffer_load_dwordx4 v[172:175], v154, s[48:51], s5 offen offset:1024 sc1\nbuffer_load_dwordx4 v[176:179], v155, s[48:51], s5 offen offset:1024 sc1\n"
".Lw_100_%=:\n.Lw_99_%=:\ns_cmp_eq_u32 s77, 1\ns_cbranch_scc0 .Lw_101_%=\ns_cmp_ge_u32 s4, 2\ns_cbranch_scc0 .Lw_102_%=\ns_mov_b32 s77, 2\n"
"buffer_load_dwordx4 v[204:207], v154, s[48:51], s6 offen sc1\nbuffer_load_dwordx4 v[208:211], v155, s[48:51], s6 offen sc1\n"
"buffer_load_dwordx4 v[212:215], v154, s[48:51], s6 offen offset:1024 sc1\nbuffer_load_dwordx4 v[216:219], v155, s[48:51], s6 offen offset:1024 sc1\n"
".Lw_102_%=:\n.Lw_101_%=:\ns_add_u32 s26, s71, 3\ns_cmp_ge_u32 s26, 7\ns_cselect_b32 s27, -6, 1\ns_add_u32 s26, s26, s27\ns_add_u32 s26, s26, s58\n"
"s_and_b32 s26, s26, 7\ns_lshl_b32 s26, s26, 2\ns_add_u32 s26, s26, s20\ns_mul_i32 s52, s26, 40960\ns_add_u32 m0, s31, 118784\ns_nop 0\n"
"buffer_load_dwordx4 v247, s[44:47], s52 offen lds\nbuffer_load_dwordx4 v248, s[44:47], s52 offen offset:1024 lds\n"
"buffer_load_dwordx4 v249, s[44:47], s52 offen offset:2048 lds\nbuffer_load_dwordx4 v250, s[44:47], s52 offen offset:3072 lds\n"
"s_add_u32 m0, s31, 122880\ns_nop 0\nbuffer_load_dwordx4 v251, s[44:47], s52 offen lds\ns_add_u32 s26, s71, 4\ns_cmp_ge_u32 s26, 7\n"
"s_cselect_b32 s27, -6, 1\ns_add_u32 s26, s26, s27\ns_add_u32 s26, s26, s58\ns_and_b32 s26, s26, 7\ns_lshl_b32 s26, s26, 2\ns_add_u32 s26, s26, s20\n"
"s_mul_i32 s52, s26, 40960\ns_add_u32 m0, s31, 139264\ns_nop 0\nbuffer_load_dwordx4 v247, s[44:47], s52 offen lds\n"
"buffer_load_dwordx4 v248, s[44:47], s52 offen offset:1024 lds\nbuffer_load_dwordx4 v249, s[44:47], s52 offen offset:2048 lds\n"
"buffer_load_dwordx4 v250, s[44:47], s52 offen offset:3072 lds\ns_add_u32 m0, s31, 143360\ns_nop 0\nbuffer_load_dwordx4 v251, s[44:47], s52 offen lds\n"
"s_cmp_eq_u32 s25, 2\ns_cbranch_scc0 .Lw_103_%=\nds_write_b128 v162, v[96:99]\nds_write_b128 v162, v[100:103] offset:1024\n"
"ds_write_b128 v162, v[104:107] offset:2048\nds_write_b128 v162, v[108:111] offset:3072\nds_write_b128 v162, v[112:115] offset:4096\n"
"ds_write_b128 v162, v[116:119] offset:20480\nds_write_b128 v162, v[120:123] offset:21504\nds_write_b128 v162, v[124:127] offset:22528\n"
"ds_write_b128 v162, v[128:131] offset:23552\nds_write_b128 v162, v[132:135] offset:24576\n.Lw_103_%=:\ns_cmp_lg_u32 s4, 7\ns_cbranch_scc1 .Lss_104_%=\n"
"s_waitcnt vmcnt(14)\n.Lsb_105_%=:\nM 20,76,168,20\nT 72,227,20480\nT 74,227,23040\nM 0,76,164,0\ns_add_u32 s26, s77, 1\ns_min_u32 s26, s26, s4\n"
"M 4,80,164,4\nT 76,227,20512\nT 78,227,23072\nM 24,80,168,24\ns_cmp_eq_u32 s26, 3\ns_cselect_b32 s62, 67108864, 0\ns_cselect_b32 s77, 3, s77\n"
"M 28,60,168,28\nT 80,227,20544\nT 82,227,23104\nM 8,60,164,8\nbuffer_load_dwordx4 v[184:187], v154, s[60:63], s7 offen sc1\nM 12,64,164,12\n"
"T 60,227,20576\nT 62,227,23136\nM 32,64,168,32\nbuffer_load_dwordx4 v[188:191], v155, s[60:63], s7 offen sc1\nM 36,68,168,36\nT 64,227,20608\n"
"T 66,227,23168\nM 16,68,164,16\nbuffer_load_dwordx4 v[192:195], v154, s[60:63], s7 offen offset:1024 sc1\nW 8\nM 20,72,176,20\nT 68,253,40960\n"
"T 70,253,43520\nM 0,72,172,0\nbuffer_load_dwordx4 v[196:199], v155, s[60:63], s7 offen offset:1024 sc1\nW 8\nM 4,76,172,4\nT 72,253,40992\n"
"T 74,253,43552\nM 24,76,176,24\nv_add_u32 v163, s56, v252\nW 8\nM 28,80,176,28\nT 76,253,41024\nT 78,253,43584\nM 8,80,172,8\n"
"v_add_u32 v163, 40960, v163\nW 8\nM 12,60,172,12\nT 80,253,41056\nT 82,253,43616\nM 32,60,176,32\nv_xor_b32 v244, 1024, v163\nW 8\nM 36,64,176,36\n"
"T 60,253,41088\nT 62,253,43648\nM 16,64,172,16\nv_mov_b32 v229, s34\ns_waitcnt vmcnt(4)\ns_cmp_lg_u32 s4, 7\ns_cbranch_scc1 .Lss_116_%=\n.Lsb_117_%=:\n"
"W 8\nM 20,68,208,20\nT 64,227,40960\nT 66,227,43520\nM 0,68,204,0\ns_add_u32 s26, s77, 1\ns_min_u32 s26, s26, s4\nW 8\nM 4,72,204,4\nT 68,227,40992\n"
"T 70,227,43552\nM 24,72,208,24\ns_cmp_eq_u32 s26, 4\ns_cselect_b32 s62, 67108864, 0\ns_cselect_b32 s77, 4, s77\nW 8\nM 28,76,208,28\nT 72,227,41024\n"
"T 74,227,43584\nM 8,76,204,8\nbuffer_load_dwordx4 v[164:167], v154, s[60:63], s8 offen sc1\nW 8\nM 12,80,204,12\nT 76,227,41056\nT 78,227,43616\n"
"M 32,80,208,32\nbuffer_load_dwordx4 v[168:171], v155, s[60:63], s8 offen sc1\nW 8\nM 36,60,208,36\nT 80,227,41088\nT 82,227,43648\nM 16,60,204,16\n"
"buffer_load_dwordx4 v[172:175], v154, s[60:63], s8 offen offset:1024 sc1\nW 8\nM 20,64,216,20\nT 60,84,0\nT 62,84,2560\nM 0,64,212,0\n"
"buffer_load_dwordx4 v[176:179], v155, s[60:63], s8 offen offset:1024 sc1\nW 8\nM 4,68,212,4\nT 64,84,32\nT 66,84,2592\nM 24,68,216,24\n"
"v_mov_b32 v231, s34\nW 8\nM 28,72,216,28\nT 68,84,64\nT 70,84,2624\nM 8,72,212,8\nv_mov_b32 v233, s34\nW 8\nM 12,76,212,12\nT 72,84,96\nT 74,84,2656\n"
"M 32,76,216,32\nv_mov_b32 v235, s34\nW 8\nM 36,80,216,36\nT 76,84,128\nT 78,84,2688\nM 16,80,212,16\nv_mov_b32 v237, s34\ns_waitcnt vmcnt(4)\n"
"s_cmp_lg_u32 s4, 7\ns_cbranch_scc1 .Lss_128_%=\n.Lsb_129_%=:\nW 8\nM 20,60,188,20\nT 80,85,0\nT 82,85,2560\nM 0,60,184,0\ns_add_u32 s26, s77, 1\n"
"s_min_u32 s26, s26, s4\nW 8\nM 4,64,184,4\nT 60,85,32\nT 62,85,2592\nM 24,64,188,24\ns_cmp_eq_u32 s26, 5\ns_cselect_b32 s62, 67108864, 0\n"
"s_cselect_b32 s77, 5, s77\nW 8\nM 28,68,188,28\nT 64,85,64\nT 66,85,2624\nM 8,68,184,8\nbuffer_load_dwordx4 v[204:207], v154, s[60:63], s9 offen sc1\n"
"W 8\nM 12,72,184,12\nT 68,85,96\nT 70,85,2656\nM 32,72,188,32\nbuffer_load_dwordx4 v[208:211], v155, s[60:63], s9 offen sc1\nW 8\nM 36,76,188,36\n"
"T 72,85,128\nT 74,85,2688\nM 16,76,184,16\nbuffer_load_dwordx4 v[212:215], v154, s[60:63], s9 offen offset:1024 sc1\nW 0\ns_barrier\nM 20,80,196,20\n"
"T 76,84,20512\nT 78,84,23072\nM 0,80,192,0\nbuffer_load_dwordx4 v[216:219], v155, s[60:63], s9 offen offset:1024 sc1\nM 4,60,192,4\nT 80,84,20544\n"
"T 82,84,23104\nM 24,60,196,24\nv_mov_b32 v239, s34\nM 28,64,196,28\nT 60,84,20576\nT 62,84,23136\nM 8,64,192,8\nv_mov_b32 v144, v224\nM 12,68,192,12\n"
"T 64,84,20608\nT 66,84,23168\nM 32,68,196,32\nv_mov_b32 v145, 0\nM 36,72,196,36\nT 68,85,20512\nT 70,85,23072\nM 16,72,192,16\nv_mov_b32 v146, v225\n"
"s_waitcnt vmcnt(4)\ns_cmp_lg_u32 s4, 7\ns_cbranch_scc1 .Lss_140_%=\n.Lsb_141_%=:\nW 8\nM 4,76,164,4\nT 72,85,20544\nT 74,85,23104\nM 24,76,168,24\n"
"s_add_u32 s26, s77, 1\ns_min_u32 s26, s26, s4\ns_cmp_eq_u32 s26, 6\ns_cselect_b32 s62, 67108864, 0\ns_cselect_b32 s77, 6, s77\nW 8\nM 28,80,168,28\n"
"T 76,85,20576\nT 78,85,23136\nM 8,80,164,8\nbuffer_load_dwordx4 v[184:187], v154, s[60:63], s10 offen sc1\n"
"buffer_load_dwordx4 v[188:191], v155, s[60:63], s10 offen sc1\nW 8\nM 12,60,164,12\nT 80,85,20608\nT 82,85,23168\nM 32,60,168,32\n"
"buffer_load_dwordx4 v[192:195], v154, s[60:63], s10 offen offset:1024 sc1\nbuffer_load_dwordx4 v[196:199], v155, s[60:63], s10 offen offset:1024 sc1\n"
"W 8\nM 36,64,168,36\nT 60,84,40992\nT 62,84,43552\nM 16,64,164,16\nv_mov_b32 v147, 0\nv_add_u32 v92, s56, v252\nW 8\nM 4,68,172,4\nT 64,84,41024\n"
"T 66,84,43584\nM 24,68,176,24\nv_add_u32 v93, s69, v252\nv_add_u32 v94, s98, v252\nW 8\nM 28,72,176,28\nT 68,84,41056\nT 70,84,43616\nM 8,72,172,8\n"
"s_mul_i32 s72, s19, 20480\ns_lshl_b32 s73, s59, 5\ns_and_b32 s74, s69, 268435456\ns_add_u32 s74, s74, s72\nW 8\nM 12,76,172,12\nT 72,84,41088\n"
"T 74,84,43648\nM 32,76,176,32\nv_add_u32 v90, s74, v254\ns_add_u32 s72, s72, s73\nv_add_u32 v89, s72, v254\nW 8\nM 36,80,176,36\nT 76,85,40992\n"
"T 78,85,43552\nM 16,80,172,16\ns_and_b32 s74, s98, 268435456\ns_add_u32 s74, s74, 163840\ns_add_u32 s74, s74, s73\ns_waitcnt vmcnt(4)\n"
"s_cmp_lg_u32 s4, 7\ns_cbranch_scc1 .Lss_152_%=\n.Lsb_153_%=:\nW 8\nM 4,60,204,4\nT 80,85,41024\nT 82,85,43584\nM 24,60,208,24\ns_add_u32 s26, s77, 1\n"
"s_min_u32 s26, s26, s4\ns_cmp_eq_u32 s26, 7\ns_cselect_b32 s62, 67108864, 0\ns_cselect_b32 s77, 7, s77\nW 8\nM 28,64,208,28\nT 60,85,41056\n"
"T 62,85,43616\nM 8,64,204,8\nbuffer_load_dwordx4 v[96:99], v154, s[60:63], s11 offen sc1\n"
"buffer_load_dwordx4 v[100:103], v155, s[60:63], s11 offen sc1\nW 8\nM 12,68,204,12\nT 64,85,41088\nT 66,85,43648\nM 32,68,208,32\n"
"buffer_load_dwordx4 v[104:107], v154, s[60:63], s11 offen offset:1024 sc1\nbuffer_load_dwordx4 v[108:111], v155, s[60:63], s11 offen offset:1024 sc1\n"
"W 8\nM 36,72,208,36\nT 68,86,32\nT 70,86,2592\nM 16,72,204,16\nv_add_u32 v91, s74, v254\ns_andn2_b32 s74, 268435456, s69\nv_add_u32 v95, s74, v252\n"
"W 8\nM 4,76,212,4\nT 72,87,32\nT 74,87,2592\nM 24,76,216,24\ns_andn2_b32 s74, 268435456, s98\nv_add_u32 v88, s74, v252\nW 8\nM 28,80,216,28\n"
"T 76,86,20512\nT 78,86,23072\nM 8,80,212,8\nW 8\nM 12,60,212,12\nT 80,87,20512\nT 82,87,23072\nM 32,60,216,32\nW 8\nM 36,64,216,36\nT 60,86,64\n"
"T 62,86,2624\nM 16,64,212,16\ns_cmp_lg_u32 s4, 7\ns_cbranch_scc1 .Lss_164_%=\n.Lsb_165_%=:\ns_waitcnt vmcnt(4)\nW 8\nM 24,68,188,24\nT 64,87,64\n"
"T 66,87,2624\nM 4,68,184,4\nW 8\nM 24,72,196,24\nT 68,86,20544\nT 70,86,23104\nM 4,72,192,4\ns_waitcnt vmcnt(0)\nW 8\nM 24,76,100,24\nT 72,87,20544\n"
"T 74,87,23104\nM 4,76,96,4\nW 8\nM 24,80,108,24\nT 76,86,96\nT 78,86,2656\nM 4,80,104,4\ns_mul_i32 s26, s20, 12288\ns_add_u32 s26, s26, 36864\n"
"v_add_u32 v243, s26, v252\nW 8\nM 28,60,188,28\nT 80,87,96\nT 82,87,2656\nM 8,60,184,8\nW 8\nM 28,64,196,28\nT 60,86,20576\nT 62,86,23136\n"
"M 8,64,192,8\nds_write_b128 v163, v[24:27]\nW 9\nM 28,68,100,28\nT 64,87,20576\nT 66,87,23136\nM 8,68,96,8\nW 2\ns_barrier\nR 24,244,0\nM 28,72,108,28\n"
"T 68,86,128\nT 70,86,2688\nM 8,72,104,8\nM 32,76,188,32\nT 72,87,128\nT 74,87,2688\nM 12,76,184,12\nW 4\nv_pk_add_f32 v[4:5], v[4:5], v[24:25]\n"
"v_pk_add_f32 v[6:7], v[6:7], v[26:27]\nM 32,80,196,32\nT 76,86,20608\nT 78,86,23168\nM 12,80,192,12\nC 228,4,5\nC 230,6,7\nM 32,60,100,32\n"
"T 80,87,20608\nT 82,87,23168\nM 12,60,96,12\nbuffer_store_dwordx4 v[228:231], v252, s[48:51], s12 offen sc1\nds_write_b128 v163, v[28:31] offset:12288\n"
"M 32,64,108,32\nT 60,84,20480\nT 62,84,23040\nM 12,64,104,12\nW 2\ns_barrier\nR 28,244,12288\nM 36,68,188,36\nT 64,85,20480\nT 66,85,23040\n"
"M 16,68,184,16\nW 2\nv_pk_add_f32 v[8:9], v[8:9], v[28:29]\nv_pk_add_f32 v[10:11], v[10:11], v[30:31]\nM 36,72,196,36\nT 68,84,40960\nT 70,84,43520\n"
"M 16,72,192,16\nC 232,8,9\nC 234,10,11\nM 36,76,100,36\nT 72,85,40960\nT 74,85,43520\nM 16,76,96,16\n"
"buffer_store_dwordx4 v[232:235], v252, s[48:51], s13 offen sc1\nds_write_b128 v163, v[32:35] offset:24576\nM 36,80,108,36\nT 76,86,0\nT 78,86,2560\n"
"M 16,80,104,16\nW 2\ns_barrier\nR 32,244,24576\nR 116,243,0\nM 20,60,168,20\nT 80,87,0\nT 82,87,2560\nM 0,60,164,0\nR 120,243,1024\nR 124,243,2048\n"
"M 20,64,176,20\nT 60,86,20480\nT 62,86,23040\nM 0,64,172,0\nR 128,243,3072\nW 8\nv_pk_add_f32 v[12:13], v[12:13], v[32:33]\nM 20,68,208,20\n"
"T 64,87,20480\nT 66,87,23040\nM 0,68,204,0\nv_pk_add_f32 v[14:15], v[14:15], v[34:35]\nC 228,12,13\nM 20,72,216,20\nC 230,14,15\n"
"buffer_store_dwordx4 v[228:231], v252, s[48:51], s14 offen sc1\nM 0,72,212,0\nW 6\nv_pk_add_f32 v[132:133], v[116:117], v[120:121]\nW 2\n"
"v_pk_add_f32 v[124:125], v[124:125], v[128:129]\nM 20,76,188,20\nv_pk_add_f32 v[134:135], v[118:119], v[122:123]\n"
"v_pk_add_f32 v[126:127], v[126:127], v[130:131]\nM 0,76,184,0\nv_pk_add_f32 v[132:133], v[132:133], v[124:125]\n"
"v_pk_add_f32 v[134:135], v[134:135], v[126:127]\nM 20,80,196,20\nC 236,132,133\nC 238,134,135\nM 0,80,192,0\n"
"buffer_store_dwordx4 v[236:239], v88, s[48:51], s22 offen sc1\nds_write_b128 v163, v[36:39] offset:36864\nM 20,60,100,20\nW 0\ns_barrier\n"
"R 36,244,36864\nW 0\nv_pk_add_f32 v[16:17], v[16:17], v[36:37]\nM 0,60,96,0\nv_pk_add_f32 v[18:19], v[18:19], v[38:39]\nC 232,16,17\nM 20,64,108,20\n"
"C 234,18,19\nbuffer_store_dwordx4 v[232:235], v95, s[48:51], s15 offen sc1\nM 0,64,104,0\ns_nop 4\nds_write_b128 v163, v[20:23] offset:49152\nW 0\n"
"s_barrier\nR 20,244,49152\nW 0\nv_pk_add_f32 v[0:1], v[0:1], v[20:21]\nv_pk_add_f32 v[2:3], v[2:3], v[22:23]\ns_cmp_eq_u32 s20, 0\n"
"s_cbranch_scc0 .Lw_174_%=\nv_add_u32 v243, 98304, v252\nR 116,243,0\nR 120,243,1024\nR 124,243,2048\nR 128,243,3072\nW 2\n"
"v_pk_add_f32 v[132:133], v[116:117], v[120:121]\nW 0\nv_pk_add_f32 v[124:125], v[124:125], v[128:129]\nv_pk_add_f32 v[134:135], v[118:119], v[122:123]\n"
"v_pk_add_f32 v[126:127], v[126:127], v[130:131]\nv_pk_add_f32 v[132:133], v[132:133], v[124:125]\nv_pk_add_f32 v[134:135], v[134:135], v[126:127]\n"
".Lw_174_%=:\ns_mov_b32 s68, 0\ns_mov_b32 s77, 7\n.Lgp_175_%=:\ns_bitcmp1_b32 s77, 0\ns_cbranch_scc0 .Lw_177_%=\n"
"buffer_load_dwordx4 v[164:167], v92, s[48:51], s23 offen sc1\nbuffer_load_dwordx4 v[168:171], v93, s[48:51], s23 offen sc1\n"
"buffer_load_dwordx4 v[172:175], v94, s[48:51], s23 offen sc1\n.Lw_177_%=:\ns_bitcmp1_b32 s77, 1\ns_cbranch_scc0 .Lw_178_%=\n"
"buffer_load_dwordx4 v[176:179], v92, s[48:51], s24 offen sc1\nbuffer_load_dwordx4 v[180:183], v93, s[48:51], s24 offen sc1\n"
"buffer_load_dwordx4 v[136:139], v94, s[48:51], s24 offen sc1\n.Lw_178_%=:\ns_bitcmp1_b32 s77, 2\ns_cbranch_scc0 .Lw_179_%=\n"
"buffer_load_dwordx4 v[140:143], v92, s[48:51], s54 offen sc1\nbuffer_load_dwordx4 v[148:151], v93, s[48:51], s54 offen sc1\n"
"buffer_load_dwordx4 v[156:159], v94, s[48:51], s54 offen sc1\n.Lw_179_%=:\ns_waitcnt vmcnt(0)\n.Lgc_176_%=:\ns_bitcmp1_b32 s77, 0\n"
"s_cbranch_scc0 .Lw_180_%=\nv_xor_b32 v60, s34, v165\nv_xor_b32 v61, s34, v167\nv_xor_b32 v62, s99, v169\nv_xor_b32 v63, s99, v171\n"
"v_xor_b32 v64, s67, v173\nv_xor_b32 v65, s67, v175\nv_or3_b32 v60, v60, v61, v62\nv_or3_b32 v63, v63, v64, v65\nv_or_b32 v60, v60, v63\n"
"v_cmp_ne_u32 vcc, 0, v60\ns_nop 0\ns_cmp_eq_u64 vcc, 0\ns_cbranch_scc0 .Lw_181_%=\ns_bitset0_b32 s77, 0\n.Lw_181_%=:\n.Lw_180_%=:\n"
"s_bitcmp1_b32 s77, 1\ns_cbranch_scc0 .Lw_182_%=\nv_xor_b32 v60, s34, v177\nv_xor_b32 v61, s34, v179\nv_xor_b32 v62, s99, v181\n"
"v_xor_b32 v63, s99, v183\nv_xor_b32 v64, s67, v137\nv_xor_b32 v65, s67, v139\nv_or3_b32 v60, v60, v61, v62\nv_or3_b32 v63, v63, v64, v65\n"
"v_or_b32 v60, v60, v63\nv_cmp_ne_u32 vcc, 0, v60\ns_nop 0\ns_cmp_eq_u64 vcc, 0\ns_cbranch_scc0 .Lw_183_%=\ns_bitset0_b32 s77, 1\n.Lw_183_%=:\n"
".Lw_182_%=:\ns_bitcmp1_b32 s77, 2\ns_cbranch_scc0 .Lw_184_%=\nv_xor_b32 v60, s34, v141\nv_xor_b32 v61, s34, v143\nv_xor_b32 v62, s99, v149\n"
"v_xor_b32 v63, s99, v151\nv_xor_b32 v64, s67, v157\nv_xor_b32 v65, s67, v159\nv_or3_b32 v60, v60, v61, v62\nv_or3_b32 v63, v63, v64, v65\n"
"v_or_b32 v60, v60, v63\nv_cmp_ne_u32 vcc, 0, v60\ns_nop 0\ns_cmp_eq_u64 vcc, 0\ns_cbranch_scc0 .Lw_185_%=\ns_bitset0_b32 s77, 2\n.Lw_185_%=:\n"
".Lw_184_%=:\ns_add_u32 s68, s68, 1\ns_cmp_lt_u32 s68, 2097152\ns_cselect_b32 s77, s77, 0\ns_cmp_lg_u32 s77, 0\ns_cbranch_scc0 .Lw_186_%=\ns_sleep 2\n"
"s_branch .Lgp_175_%=\n.Lw_186_%=:\nM 0,144,164,0\nM 16,144,168,16\nM 132,144,172,132\nM 0,144,176,0\nM 16,144,180,16\nM 132,144,136,132\nM 0,144,140,0\n"
"M 16,144,148,16\nM 132,144,156,132\ns_nop 5\nC 0,0,1\nC 1,2,3\nbuffer_store_dwordx2 v[0:1], v89, s[40:43], 0 offen sc1\nC 16,16,17\nC 17,18,19\n"
"buffer_store_dwordx2 v[16:17], v90, s[40:43], 0 offen offset:128 sc1\nC 132,132,133\nC 133,134,135\n"
"buffer_store_dwordx2 v[132:133], v91, s[40:43], 0 offen sc1\ns_branch .Lend_2_%=\n.Lss_104_%=:\ns_cmp_lt_u32 s77, 1\ns_cbranch_scc0 .Lw_107_%=\n"
"s_mov_b32 s68, 0\n.Lpoll_108_%=:\ns_cmp_ge_u32 s4, 1\ns_cbranch_scc1 .Lpolld_109_%=\ns_sleep 1\ns_add_u32 s68, s68, 1\nds_read_b32 v243, v153\nW 0\n"
"v_readfirstlane_b32 s4, v243\ns_branch .Lpoll_108_%=\n.Lpolld_109_%=:\ns_mov_b32 s77, 1\nbuffer_load_dwordx4 v[164:167], v154, s[48:51], s5 offen sc1\n"
"buffer_load_dwordx4 v[168:171], v155, s[48:51], s5 offen sc1\nbuffer_load_dwordx4 v[172:175], v154, s[48:51], s5 offen offset:1024 sc1\n"
"buffer_load_dwordx4 v[176:179], v155, s[48:51], s5 offen offset:1024 sc1\ns_cmp_eq_u32 s77, 1\ns_cbranch_scc0 .Lw_110_%=\ns_cmp_ge_u32 s4, 2\n"
"s_cbranch_scc0 .Lw_111_%=\ns_mov_b32 s77, 2\nbuffer_load_dwordx4 v[204:207], v154, s[48:51], s6 offen sc1\n"
"buffer_load_dwordx4 v[208:211], v155, s[48:51], s6 offen sc1\nbuffer_load_dwordx4 v[212:215], v154, s[48:51], s6 offen offset:1024 sc1\n"
"buffer_load_dwordx4 v[216:219], v155, s[48:51], s6 offen offset:1024 sc1\n.Lw_111_%=:\n.Lw_110_%=:\ns_cmp_eq_u32 s77, 1\ns_cbranch_scc0 .Lw_112_%=\n"
"s_waitcnt vmcnt(0)\n.Lw_112_%=:\ns_cmp_eq_u32 s77, 2\ns_cbranch_scc0 .Lw_113_%=\ns_waitcnt vmcnt(4)\n.Lw_113_%=:\ns_branch .Ln_106_%=\n.Lw_107_%=:\n"
"s_cmp_eq_u32 s77, 1\ns_cbranch_scc0 .Lw_114_%=\ns_waitcnt vmcnt(10)\n.Lw_114_%=:\ns_cmp_eq_u32 s77, 2\ns_cbranch_scc0 .Lw_115_%=\ns_waitcnt vmcnt(14)\n"
".Lw_115_%=:\n.Ln_106_%=:\ns_branch .Lsb_105_%=\n.Lss_116_%=:\nds_read_b32 v243, v153\nW 0\nv_readfirstlane_b32 s4, v243\ns_cmp_lt_u32 s77, 2\n"
"s_cbranch_scc0 .Lw_119_%=\ns_mov_b32 s68, 0\n.Lpoll_120_%=:\ns_cmp_ge_u32 s4, 2\ns_cbranch_scc1 .Lpolld_121_%=\ns_sleep 1\ns_add_u32 s68, s68, 1\n"
"ds_read_b32 v243, v153\nW 0\nv_readfirstlane_b32 s4, v243\ns_branch .Lpoll_120_%=\n.Lpolld_121_%=:\ns_mov_b32 s77, 2\n"
"buffer_load_dwordx4 v[204:207], v154, s[48:51], s6 offen sc1\nbuffer_load_dwordx4 v[208:211], v155, s[48:51], s6 offen sc1\n"
"buffer_load_dwordx4 v[212:215], v154, s[48:51], s6 offen offset:1024 sc1\nbuffer_load_dwordx4 v[216:219], v155, s[48:51], s6 offen offset:1024 sc1\n"
"s_cmp_eq_u32 s77, 2\ns_cbranch_scc0 .Lw_122_%=\ns_cmp_ge_u32 s4, 3\ns_cbranch_scc0 .Lw_123_%=\ns_mov_b32 s77, 3\n"
"buffer_load_dwordx4 v[184:187], v154, s[48:51], s7 offen sc1\nbuffer_load_dwordx4 v[188:191], v155, s[48:51], s7 offen sc1\n"
"buffer_load_dwordx4 v[192:195], v154, s[48:51], s7 offen offset:1024 sc1\nbuffer_load_dwordx4 v[196:199], v155, s[48:51], s7 offen offset:1024 sc1\n"
".Lw_123_%=:\n.Lw_122_%=:\ns_cmp_eq_u32 s77, 2\ns_cbranch_scc0 .Lw_124_%=\ns_waitcnt vmcnt(0)\n.Lw_124_%=:\ns_cmp_eq_u32 s77, 3\n"
"s_cbranch_scc0 .Lw_125_%=\ns_waitcnt vmcnt(4)\n.Lw_125_%=:\ns_branch .Ln_118_%=\n.Lw_119_%=:\ns_cmp_eq_u32 s77, 2\ns_cbranch_scc0 .Lw_126_%=\n"
"s_cmp_ge_u32 s4, 3\ns_cbranch_scc0 .Lw_127_%=\ns_mov_b32 s77, 3\nbuffer_load_dwordx4 v[184:187], v154, s[48:51], s7 offen sc1\n"
"buffer_load_dwordx4 v[188:191], v155, s[48:51], s7 offen sc1\nbuffer_load_dwordx4 v[192:195], v154, s[48:51], s7 offen offset:1024 sc1\n"
"buffer_load_dwordx4 v[196:199], v155, s[48:51], s7 offen offset:1024 sc1\n.Lw_127_%=:\n.Lw_126_%=:\n.Ln_118_%=:\ns_branch .Lsb_117_%=\n.Lss_128_%=:\n"
"ds_read_b32 v243, v153\nW 0\nv_readfirstlane_b32 s4, v243\ns_cmp_lt_u32 s77, 3\ns_cbranch_scc0 .Lw_131_%=\ns_mov_b32 s68, 0\n.Lpoll_132_%=:\n"
"s_cmp_ge_u32 s4, 3\ns_cbranch_scc1 .Lpolld_133_%=\ns_sleep 1\ns_add_u32 s68, s68, 1\nds_read_b32 v243, v153\nW 0\nv_readfirstlane_b32 s4, v243\n"
"s_branch .Lpoll_132_%=\n.Lpolld_133_%=:\ns_mov_b32 s77, 3\nbuffer_load_dwordx4 v[184:187], v154, s[48:51], s7 offen sc1\n"
"buffer_load_dwordx4 v[188:191], v155, s[48:51], s7 offen sc1\nbuffer_load_dwordx4 v[192:195], v154, s[48:51], s7 offen offset:1024 sc1\n"
"buffer_load_dwordx4 v[196:199], v155, s[48:51], s7 offen offset:1024 sc1\ns_cmp_eq_u32 s77, 3\ns_cbranch_scc0 .Lw_134_%=\ns_cmp_ge_u32 s4, 4\n"
"s_cbranch_scc0 .Lw_135_%=\ns_mov_b32 s77, 4\nbuffer_load_dwordx4 v[164:167], v154, s[48:51], s8 offen sc1\n"
"buffer_load_dwordx4 v[168:171], v155, s[48:51], s8 offen sc1\nbuffer_load_dwordx4 v[172:175], v154, s[48:51], s8 offen offset:1024 sc1\n"
"buffer_load_dwordx4 v[176:179], v155, s[48:51], s8 offen offset:1024 sc1\n.Lw_135_%=:\n.Lw_134_%=:\ns_cmp_eq_u32 s77, 3\ns_cbranch_scc0 .Lw_136_%=\n"
"s_waitcnt vmcnt(0)\n.Lw_136_%=:\ns_cmp_eq_u32 s77, 4\ns_cbranch_scc0 .Lw_137_%=\ns_waitcnt vmcnt(4)\n.Lw_137_%=:\ns_branch .Ln_130_%=\n.Lw_131_%=:\n"
"s_cmp_eq_u32 s77, 3\ns_cbranch_scc0 .Lw_138_%=\ns_cmp_ge_u32 s4, 4\ns_cbranch_scc0 .Lw_139_%=\ns_mov_b32 s77, 4\n"
"buffer_load_dwordx4 v[164:167], v154, s[48:51], s8 offen sc1\nbuffer_load_dwordx4 v[168:171], v155, s[48:51], s8 offen sc1\n"
"buffer_load_dwordx4 v[172:175], v154, s[48:51], s8 offen offset:1024 sc1\nbuffer_load_dwordx4 v[176:179], v155, s[48:51], s8 offen offset:1024 sc1\n"
".Lw_139_%=:\n.Lw_138_%=:\n.Ln_130_%=:\ns_branch .Lsb_129_%=\n.Lss_140_%=:\nds_read_b32 v243, v153\nW 0\nv_readfirstlane_b32 s4, v243\n"
"s_cmp_lt_u32 s77, 4\ns_cbranch_scc0 .Lw_143_%=\ns_mov_b32 s68, 0\n.Lpoll_144_%=:\ns_cmp_ge_u32 s4, 4\ns_cbranch_scc1 .Lpolld_145_%=\ns_sleep 1\n"
"s_add_u32 s68, s68, 1\nds_read_b32 v243, v153\nW 0\nv_readfirstlane_b32 s4, v243\ns_branch .Lpoll_144_%=\n.Lpolld_145_%=:\ns_mov_b32 s77, 4\n"
"buffer_load_dwordx4 v[164:167], v154, s[48:51], s8 offen sc1\nbuffer_load_dwordx4 v[168:171], v155, s[48:51], s8 offen sc1\n"
"buffer_load_dwordx4 v[172:175], v154, s[48:51], s8 offen offset:1024 sc1\nbuffer_load_dwordx4 v[176:179], v155, s[48:51], s8 offen offset:1024 sc1\n"
"s_cmp_eq_u32 s77, 4\ns_cbranch_scc0 .Lw_146_%=\ns_cmp_ge_u32 s4, 5\ns_cbranch_scc0 .Lw_147_%=\ns_mov_b32 s77, 5\n"
"buffer_load_dwordx4 v[204:207], v154, s[48:51], s9 offen sc1\nbuffer_load_dwordx4 v[208:211], v155, s[48:51], s9 offen sc1\n"
"buffer_load_dwordx4 v[212:215], v154, s[48:51], s9 offen offset:1024 sc1\nbuffer_load_dwordx4 v[216:219], v155, s[48:51], s9 offen offset:1024 sc1\n"
".Lw_147_%=:\n.Lw_146_%=:\ns_cmp_eq_u32 s77, 4\ns_cbranch_scc0 .Lw_148_%=\ns_waitcnt vmcnt(0)\n.Lw_148_%=:\ns_cmp_eq_u32 s77, 5\n"
"s_cbranch_scc0 .Lw_149_%=\ns_waitcnt vmcnt(4)\n.Lw_149_%=:\ns_branch .Ln_142_%=\n.Lw_143_%=:\ns_cmp_eq_u32 s77, 4\ns_cbranch_scc0 .Lw_150_%=\n"
"s_cmp_ge_u32 s4, 5\ns_cbranch_scc0 .Lw_151_%=\ns_mov_b32 s77, 5\nbuffer_load_dwordx4 v[204:207], v154, s[48:51], s9 offen sc1\n"
"buffer_load_dwordx4 v[208:211], v155, s[48:51], s9 offen sc1\nbuffer_load_dwordx4 v[212:215], v154, s[48:51], s9 offen offset:1024 sc1\n"
"buffer_load_dwordx4 v[216:219], v155, s[48:51], s9 offen offset:1024 sc1\n.Lw_151_%=:\n.Lw_150_%=:\n.Ln_142_%=:\ns_branch .Lsb_141_%=\n.Lss_152_%=:\n"
"ds_read_b32 v243, v153\nW 0\nv_readfirstlane_b32 s4, v243\ns_cmp_lt_u32 s77, 5\ns_cbranch_scc0 .Lw_155_%=\ns_mov_b32 s68, 0\n.Lpoll_156_%=:\n"
"s_cmp_ge_u32 s4, 5\ns_cbranch_scc1 .Lpolld_157_%=\ns_sleep 1\ns_add_u32 s68, s68, 1\nds_read_b32 v243, v153\nW 0\nv_readfirstlane_b32 s4, v243\n"
"s_branch .Lpoll_156_%=\n.Lpolld_157_%=:\ns_mov_b32 s77, 5\nbuffer_load_dwordx4 v[204:207], v154, s[48:51], s9 offen sc1\n"
"buffer_load_dwordx4 v[208:211], v155, s[48:51], s9 offen sc1\nbuffer_load_dwordx4 v[212:215], v154, s[48:51], s9 offen offset:1024 sc1\n"
"buffer_load_dwordx4 v[216:219], v155, s[48:51], s9 offen offset:1024 sc1\ns_cmp_eq_u32 s77, 5\ns_cbranch_scc0 .Lw_158_%=\ns_cmp_ge_u32 s4, 6\n"
"s_cbranch_scc0 .Lw_159_%=\ns_mov_b32 s77, 6\nbuffer_load_dwordx4 v[184:187], v154, s[48:51], s10 offen sc1\n"
"buffer_load_dwordx4 v[188:191], v155, s[48:51], s10 offen sc1\nbuffer_load_dwordx4 v[192:195], v154, s[48:51], s10 offen offset:1024 sc1\n"
"buffer_load_dwordx4 v[196:199], v155, s[48:51], s10 offen offset:1024 sc1\n.Lw_159_%=:\n.Lw_158_%=:\ns_cmp_eq_u32 s77, 5\ns_cbranch_scc0 .Lw_160_%=\n"
"s_waitcnt vmcnt(0)\n.Lw_160_%=:\ns_cmp_eq_u32 s77, 6\ns_cbranch_scc0 .Lw_161_%=\ns_waitcnt vmcnt(4)\n.Lw_161_%=:\ns_branch .Ln_154_%=\n.Lw_155_%=:\n"
"s_cmp_eq_u32 s77, 5\ns_cbranch_scc0 .Lw_162_%=\ns_cmp_ge_u32 s4, 6\ns_cbranch_scc0 .Lw_163_%=\ns_mov_b32 s77, 6\n"
"buffer_load_dwordx4 v[184:187], v154, s[48:51], s10 offen sc1\nbuffer_load_dwordx4 v[188:191], v155, s[48:51], s10 offen sc1\n"
"buffer_load_dwordx4 v[192:195], v154, s[48:51], s10 offen offset:1024 sc1\nbuffer_load_dwordx4 v[196:199], v155, s[48:51], s10 offen offset:1024 sc1\n"
".Lw_163_%=:\n.Lw_162_%=:\n.Ln_154_%=:\ns_branch .Lsb_153_%=\n.Lss_164_%=:\ns_waitcnt vmcnt(4)\nds_read_b32 v243, v153\nW 0\n"
"v_readfirstlane_b32 s4, v243\ns_cmp_eq_u32 s77, 5\ns_cbranch_scc0 .Lw_166_%=\ns_cmp_ge_u32 s4, 6\ns_cbranch_scc0 .Lw_167_%=\ns_mov_b32 s77, 6\n"
"buffer_load_dwordx4 v[184:187], v154, s[48:51], s10 offen sc1\nbuffer_load_dwordx4 v[188:191], v155, s[48:51], s10 offen sc1\n"
"buffer_load_dwordx4 v[192:195], v154, s[48:51], s10 offen offset:1024 sc1\nbuffer_load_dwordx4 v[196:199], v155, s[48:51], s10 offen offset:1024 sc1\n"
".Lw_167_%=:\n.Lw_166_%=:\ns_mov_b32 s68, 0\n.Lpoll_168_%=:\ns_cmp_ge_u32 s4, 7\ns_cbranch_scc1 .Lpolld_169_%=\ns_sleep 1\ns_add_u32 s68, s68, 1\n"
"ds_read_b32 v243, v153\nW 0\nv_readfirstlane_b32 s4, v243\ns_branch .Lpoll_168_%=\n.Lpolld_169_%=:\ns_cmp_eq_u32 s77, 5\ns_cbranch_scc0 .Lw_170_%=\n"
"s_cmp_ge_u32 s4, 6\ns_cbranch_scc0 .Lw_171_%=\ns_mov_b32 s77, 6\nbuffer_load_dwordx4 v[184:187], v154, s[48:51], s10 offen sc1\n"
"buffer_load_dwordx4 v[188:191], v155, s[48:51], s10 offen sc1\nbuffer_load_dwordx4 v[192:195], v154, s[48:51], s10 offen offset:1024 sc1\n"
"buffer_load_dwordx4 v[196:199], v155, s[48:51], s10 offen offset:1024 sc1\n.Lw_171_%=:\n.Lw_170_%=:\ns_cmp_eq_u32 s77, 6\ns_cbranch_scc0 .Lw_172_%=\n"
"s_cmp_ge_u32 s4, 7\ns_cbranch_scc0 .Lw_173_%=\ns_mov_b32 s77, 7\nbuffer_load_dwordx4 v[96:99], v154, s[48:51], s11 offen sc1\n"
"buffer_load_dwordx4 v[100:103], v155, s[48:51], s11 offen sc1\nbuffer_load_dwordx4 v[104:107], v154, s[48:51], s11 offen offset:1024 sc1\n"
"buffer_load_dwordx4 v[108:111], v155, s[48:51], s11 offen offset:1024 sc1\n.Lw_173_%=:\n.Lw_172_%=:\ns_branch .Lsb_165_%=\n.Lfb1_12_%=:\n"
"s_waitcnt vmcnt(0) lgkmcnt(0)\ns_nop 15\ns_mov_b32 s35, 1\ns_barrier\ns_branch .Lrs_16_%=\n.Lq0_3_%=:\nW 0\ns_lshl_b32 s26, s21, 8\n"
"s_add_u32 s26, s26, 33619968\ns_load_dword s69, s[16:17], s26\nv_and_b32 v254, 15, v255\nv_lshlrev_b32 v254, 7, v254\nv_add_u32 v254, 32768, v254\n"
"v_lshrrev_b32 v253, 4, v255\nv_and_b32 v252, 15, v255\nv_lshrrev_b32 v252, 1, v252\nv_xor_b32 v253, v253, v252\nv_lshlrev_b32 v253, 4, v253\n"
"v_add_u32 v230, v254, v253\nv_xor_b32 v231, 64, v230\nv_add_u32 v220, 75776, v230\nv_add_u32 v221, 75776, v231\nv_lshrrev_b32 v254, 4, v255\n"
"v_lshlrev_b32 v254, 11, v254\nv_add_u32 v254, 51200, v254\nv_mov_b32 v252, s20\nv_lshlrev_b32 v253, 9, v252\nv_add_u32 v254, v254, v253\n"
"v_lshrrev_b32 v253, 2, v255\nv_and_b32 v253, 3, v253\nv_lshlrev_b32 v253, 6, v253\nv_add_u32 v254, v254, v253\nv_and_b32 v253, 1, v255\n"
"v_lshlrev_b32 v253, 3, v253\nv_add_u32 v254, v254, v253\nv_lshrrev_b32 v253, 1, v255\nv_and_b32 v253, 1, v253\nv_lshrrev_b32 v252, 3, v255\n"
"v_and_b32 v252, 2, v252\nv_xor_b32 v253, v253, v252\nv_lshlrev_b32 v253, 4, v253\nv_add_u32 v222, v254, v253\nv_xor_b32 v254, 16, v222\n"
"v_add_u32 v223, 256, v254\nv_xor_b32 v224, 32, v222\nv_xor_b32 v225, 32, v223\ns_lshl_b32 s27, s56, 18\ns_add_u32 s60, s6, s27\ns_addc_u32 s61, s7, 0\n"
"s_sub_u32 s60, s60, 1024\ns_subb_u32 s61, s61, 0\ns_and_b32 s61, s61, 65535\ns_mov_b32 s62, 263168\ns_mov_b32 s63, 131072\ns_add_u32 s36, s8, s27\n"
"s_addc_u32 s37, s9, 0\ns_sub_u32 s36, s36, 1024\ns_subb_u32 s37, s37, 0\ns_and_b32 s37, s37, 65535\ns_mov_b32 s38, 263168\ns_mov_b32 s39, 131072\n"
"v_mov_b32 v253, s20\nv_lshlrev_b32 v254, 13, v253\nv_add_u32 v254, 65536, v254\nv_lshlrev_b32 v253, 4, v255\nv_add_u32 v251, v254, v253\n"
"s_lshl_b32 s30, s20, 14\ns_lshl_b32 s55, s20, 13\ns_mul_i32 s26, s18, 2654435761\ns_lshr_b32 s26, s26, 11\ns_and_b32 s95, s26, 258048\n"
"s_and_b32 s26, s26, 0\ns_xor_b32 s30, s30, s26\ns_and_b32 s68, s21, 1\ns_mul_i32 s68, s68, -16\ns_add_u32 s68, s68, 480\nv_lshrrev_b32 v254, 2, v255\n"
"v_and_b32 v254, 7, v254\nv_lshlrev_b32 v254, 8, v254\nv_lshrrev_b32 v253, 5, v255\nv_lshlrev_b32 v253, 6, v253\nv_add_u32 v254, v254, v253\n"
"v_and_b32 v253, 3, v255\nv_lshlrev_b32 v253, 4, v253\nv_add_u32 v254, v254, v253\nv_add_u32 v247, 1024, v254\nv_subrev_u32 v248, 896, v247\n"
"v_xor_b32 v249, 32, v247\nv_xor_b32 v250, 32, v248\nv_and_b32 v254, 15, v255\nv_lshlrev_b32 v254, 8, v254\nv_add_u32 v254, 126976, v254\n"
"v_mov_b32 v252, s20\nv_lshlrev_b32 v253, 2, v252\nv_lshrrev_b32 v252, 5, v255\nv_add_u32 v253, v253, v252\nv_and_b32 v252, 15, v255\n"
"v_xor_b32 v253, v253, v252\nv_lshlrev_b32 v253, 4, v253\nv_add_u32 v254, v254, v253\nv_lshrrev_b32 v253, 4, v255\nv_and_b32 v253, 1, v253\n"
"v_lshlrev_b32 v253, 3, v253\nv_add_u32 v233, v254, v253\nv_xor_b32 v234, 32, v233\ns_mul_i32 s27, s57, 12288\ns_mov_b32 s28, 0\n"
"s_lshl_b32 s29, s57, 14\ns_cmp_lt_u32 s19, 2\ns_cselect_b32 s27, 24576, s27\ns_cselect_b32 s28, s29, 0\nv_and_b32 v254, 15, v255\n"
"v_lshlrev_b32 v254, 8, v254\nv_add_u32 v254, 126976, v254\nv_lshrrev_b32 v253, 4, v255\nv_and_b32 v252, 15, v255\nv_xor_b32 v253, v253, v252\n"
"v_lshlrev_b32 v253, 4, v253\nv_add_u32 v254, v254, v253\nv_add_u32 v235, s27, v254\nv_xor_b32 v236, 64, v235\nv_xor_b32 v237, 128, v235\n"
"v_xor_b32 v238, 192, v235\nv_lshrrev_b32 v254, 2, v255\nv_and_b32 v254, 3, v254\nv_lshlrev_b32 v254, 11, v254\nv_and_b32 v253, 3, v255\n"
"v_lshlrev_b32 v253, 6, v253\nv_add_u32 v254, v254, v253\nv_lshrrev_b32 v253, 4, v255\nv_lshrrev_b32 v252, 1, v255\nv_and_b32 v252, 2, v252\n"
"v_xor_b32 v253, v253, v252\nv_lshlrev_b32 v253, 4, v253\nv_add_u32 v239, v254, v253\nv_add_u32 v244, s28, v239\nv_lshrrev_b32 v254, 4, v255\n"
"v_lshlrev_b32 v254, 11, v254\nv_add_u32 v254, 32768, v254\nv_lshrrev_b32 v253, 2, v255\nv_and_b32 v253, 3, v253\nv_lshlrev_b32 v253, 6, v253\n"
"v_add_u32 v254, v254, v253\nv_and_b32 v253, 1, v255\nv_lshlrev_b32 v253, 3, v253\nv_add_u32 v254, v254, v253\nv_lshrrev_b32 v253, 1, v255\n"
"v_and_b32 v253, 1, v253\nv_lshrrev_b32 v252, 3, v255\nv_and_b32 v252, 2, v252\nv_xor_b32 v253, v253, v252\nv_lshlrev_b32 v253, 4, v253\n"
"v_add_u32 v254, v254, v253\nv_add_u32 v245, s28, v254\nv_xor_b32 v246, 32, v245\nv_mov_b32 v240, 1065369472\nv_mov_b32 v241, 1065369472\n"
"v_mov_b32 v242, 1065369472\nv_mov_b32 v243, 1065369472\nv_mov_b32 v252, 1040356334\nv_mov_b32 v253, 1040356334\nW 0\ns_or_b32 s34, s69, 1073758208\n"
"s_add_u32 s34, s34, 1\ns_and_b32 s34, s34, 3221209087\ns_waitcnt vmcnt(0)\nW 0\ns_barrier\nT 136,222,8192\nT 138,223,8192\nR 144,220,36864\n"
"T 140,224,8192\nT 142,225,8192\nR 148,220,38912\nR 152,220,40960\nR 156,220,43008\nR 160,220,45056\nT 164,222,16384\nT 166,223,16384\nR 172,221,36864\n"
"T 168,224,16384\nT 170,225,16384\nR 176,221,38912\nR 180,221,40960\nR 184,221,43008\nR 188,221,45056\nW 15\n"
"v_mfma_f32_16x16x32_bf16 v[96:99], v[136:139], v[144:147], 0\nW 13\nv_mfma_f32_16x16x32_bf16 v[116:119], v[140:143], v[144:147], 0\nW 12\n"
"v_mfma_f32_16x16x32_bf16 v[120:123], v[140:143], v[148:151], 0\nv_mfma_f32_16x16x32_bf16 v[100:103], v[136:139], v[148:151], 0\nW 11\n"
"v_mfma_f32_16x16x32_bf16 v[104:107], v[136:139], v[152:155], 0\nv_mfma_f32_16x16x32_bf16 v[124:127], v[140:143], v[152:155], 0\nW 10\n"
"v_mfma_f32_16x16x32_bf16 v[128:131], v[140:143], v[156:159], 0\nv_mfma_f32_16x16x32_bf16 v[108:111], v[136:139], v[156:159], 0\nW 9\n"
"v_mfma_f32_16x16x32_bf16 v[112:115], v[136:139], v[160:163], 0\nv_mfma_f32_16x16x32_bf16 v[132:135], v[140:143], v[160:163], 0\nW 0\ns_barrier\n"
"M 96,164,172,96\nT 192,222,24576\nT 194,223,24576\nM 116,168,172,116\nR 200,220,0\nT 196,224,24576\nM 120,168,176,120\nT 198,225,24576\nR 204,220,2048\n"
"M 100,164,176,100\nR 208,220,4096\nR 212,220,6144\nM 104,164,180,104\nR 216,220,8192\nT 136,222,32768\nM 124,168,180,124\nT 138,223,32768\nR 144,221,0\n"
"M 128,168,184,128\nT 140,224,32768\nT 142,225,32768\nM 108,164,184,108\nR 148,221,2048\nR 152,221,4096\nM 112,164,188,112\nR 156,221,6144\n"
"R 160,221,8192\nM 132,168,188,132\nW 0\ns_barrier\nM 96,192,200,96\nT 164,222,40960\nT 166,223,40960\nM 116,196,200,116\nR 172,230,0\nT 168,224,40960\n"
"M 120,196,204,120\nT 170,225,40960\nR 176,230,2048\nM 100,192,204,100\nR 180,230,4096\nR 184,230,6144\nM 104,192,208,104\nR 188,230,8192\n"
"M 124,196,208,124\nM 128,196,212,128\nM 108,192,212,108\nM 112,192,216,112\nM 132,196,216,132\nM 96,136,144,96\nT 192,222,49152\nT 194,223,49152\n"
"M 116,140,144,116\nR 200,231,0\nT 196,224,49152\nM 120,140,148,120\nT 198,225,49152\nR 204,231,2048\nM 100,136,148,100\nR 208,231,4096\nR 212,231,6144\n"
"M 104,136,152,104\nR 216,231,8192\nM 124,140,152,124\nM 128,140,156,128\nM 108,136,156,108\nM 112,136,160,112\nM 132,140,160,132\nW 0\ns_barrier\n"
"s_bitcmp1_b32 s21, 0\ns_cbranch_scc0 .Lw_187_%=\ns_add_u32 s53, s55, 32768\ns_xor_b32 s53, s53, s95\n"
"buffer_load_dwordx4 v[0:3], v247, s[60:63], s53 offen nt\nbuffer_load_dwordx4 v[4:7], v248, s[60:63], s53 offen offset:1024 nt\n"
"buffer_load_dwordx4 v[8:11], v249, s[60:63], s53 offen offset:2048 nt\nbuffer_load_dwordx4 v[12:15], v250, s[60:63], s53 offen offset:3072 nt\n"
"s_add_u32 s53, s55, 36864\ns_xor_b32 s53, s53, s95\nbuffer_load_dwordx4 v[16:19], v247, s[60:63], s53 offen nt\n"
"buffer_load_dwordx4 v[20:23], v248, s[60:63], s53 offen offset:1024 nt\nbuffer_load_dwordx4 v[24:27], v249, s[60:63], s53 offen offset:2048 nt\n"
"buffer_load_dwordx4 v[28:31], v250, s[60:63], s53 offen offset:3072 nt\ns_add_u32 s53, s55, 32768\ns_xor_b32 s53, s53, s95\n"
"buffer_load_dwordx4 v[32:35], v247, s[36:39], s53 offen nt\nbuffer_load_dwordx4 v[36:39], v248, s[36:39], s53 offen offset:1024 nt\n"
"buffer_load_dwordx4 v[40:43], v249, s[36:39], s53 offen offset:2048 nt\nbuffer_load_dwordx4 v[44:47], v250, s[36:39], s53 offen offset:3072 nt\n"
"s_add_u32 s53, s55, 36864\ns_xor_b32 s53, s53, s95\nbuffer_load_dwordx4 v[48:51], v247, s[36:39], s53 offen nt\n"
"buffer_load_dwordx4 v[52:55], v248, s[36:39], s53 offen offset:1024 nt\nbuffer_load_dwordx4 v[56:59], v249, s[36:39], s53 offen offset:2048 nt\n"
"buffer_load_dwordx4 v[60:63], v250, s[36:39], s53 offen offset:3072 nt\n.Lw_187_%=:\nM 96,164,172,96\nT 136,222,0\nT 138,223,0\nM 116,168,172,116\n"
"R 144,220,18432\nT 140,224,0\nM 120,168,176,120\nT 142,225,0\nR 148,220,20480\nM 100,164,176,100\nR 152,220,22528\nR 156,220,24576\nM 104,164,180,104\n"
"R 160,220,26624\nM 124,168,180,124\nM 128,168,184,128\nM 108,164,184,108\nM 112,164,188,112\nM 132,168,188,132\nM 96,192,200,96\nT 164,222,8192\n"
"T 166,223,8192\nM 116,196,200,116\nR 172,221,18432\nT 168,224,8192\nM 120,196,204,120\nT 170,225,8192\nR 176,221,20480\nM 100,192,204,100\n"
"R 180,221,22528\nR 184,221,24576\nM 104,192,208,104\nR 188,221,26624\nM 124,196,208,124\nM 128,196,212,128\nM 108,192,212,108\nM 112,192,216,112\n"
"M 132,196,216,132\nW 0\ns_barrier\nM 96,136,144,96\nT 192,222,16384\nT 194,223,16384\nM 116,140,144,116\nR 200,220,36864\nT 196,224,16384\n"
"M 120,140,148,120\nT 198,225,16384\nR 204,220,38912\nM 100,136,148,100\nR 208,220,40960\nR 212,220,43008\nM 104,136,152,104\nR 216,220,45056\n"
"M 124,140,152,124\nM 128,140,156,128\nM 108,136,156,108\nM 112,136,160,112\nM 132,140,160,132\nM 96,164,172,96\nT 136,222,24576\nT 138,223,24576\n"
"M 116,168,172,116\nR 144,221,36864\nT 140,224,24576\nM 120,168,176,120\nT 142,225,24576\nR 148,221,38912\nM 100,164,176,100\nR 152,221,40960\n"
"R 156,221,43008\nM 104,164,180,104\nR 160,221,45056\nM 124,168,180,124\nM 128,168,184,128\nM 108,164,184,108\nM 112,164,188,112\nM 132,168,188,132\n"
"W 0\ns_barrier\nM 96,192,200,96\nT 164,222,32768\nT 166,223,32768\nM 116,196,200,116\nR 172,220,0\nT 168,224,32768\nM 120,196,204,120\nT 170,225,32768\n"
"R 176,220,2048\nM 100,192,204,100\nR 180,220,4096\nR 184,220,6144\nM 104,192,208,104\nR 188,220,8192\nM 124,196,208,124\nM 128,196,212,128\n"
"M 108,192,212,108\nM 112,192,216,112\nM 132,196,216,132\nM 96,136,144,96\nT 192,222,40960\nT 194,223,40960\nM 116,140,144,116\nR 200,221,0\n"
"T 196,224,40960\nM 120,140,148,120\nT 198,225,40960\nR 204,221,2048\nM 100,136,148,100\nR 208,221,4096\nR 212,221,6144\nM 104,136,152,104\n"
"R 216,221,8192\nM 124,140,152,124\nM 128,140,156,128\nM 108,136,156,108\nM 112,136,160,112\nM 132,140,160,132\nW 0\ns_barrier\nM 96,164,172,96\n"
"T 136,222,49152\nT 138,223,49152\nM 116,168,172,116\nR 144,230,0\nT 140,224,49152\nM 120,168,176,120\nT 142,225,49152\nR 148,230,2048\n"
"M 100,164,176,100\nR 152,230,4096\nR 156,230,6144\nM 104,164,180,104\nR 160,230,8192\nM 124,168,180,124\nM 128,168,184,128\nM 108,164,184,108\n"
"M 112,164,188,112\nM 132,168,188,132\nM 96,192,200,96\nT 164,222,0\nT 166,223,0\nM 116,196,200,116\nR 172,231,0\nT 168,224,0\nM 120,196,204,120\n"
"T 170,225,0\nR 176,231,2048\nM 100,192,204,100\nR 180,231,4096\nR 184,231,6144\nM 104,192,208,104\nR 188,231,8192\nM 124,196,208,124\n"
"M 128,196,212,128\nM 108,192,212,108\nM 112,192,216,112\nM 132,196,216,132\nW 0\ns_barrier\nM 96,136,144,96\nT 192,222,8192\nT 194,223,8192\n"
"M 116,140,144,116\nR 200,220,18432\nT 196,224,8192\nM 120,140,148,120\nT 198,225,8192\nR 204,220,20480\nM 100,136,148,100\nR 208,220,22528\n"
"R 212,220,24576\nM 104,136,152,104\nR 216,220,26624\nM 124,140,152,124\nM 128,140,156,128\nM 108,136,156,108\nM 112,136,160,112\nM 132,140,160,132\n"
"M 96,164,172,96\nT 136,222,16384\nT 138,223,16384\nM 116,168,172,116\nR 144,221,18432\nT 140,224,16384\nM 120,168,176,120\nT 142,225,16384\n"
"R 148,221,20480\nM 100,164,176,100\nR 152,221,22528\nR 156,221,24576\nM 104,164,180,104\nR 160,221,26624\nM 124,168,180,124\nM 128,168,184,128\n"
"M 108,164,184,108\nM 112,164,188,112\nM 132,168,188,132\nW 0\ns_barrier\nM 96,192,200,96\nT 164,222,24576\nT 166,223,24576\nM 116,196,200,116\n"
"R 172,220,36864\nT 168,224,24576\nM 120,196,204,120\nT 170,225,24576\nR 176,220,38912\nM 100,192,204,100\nR 180,220,40960\nR 184,220,43008\n"
"M 104,192,208,104\nR 188,220,45056\nM 124,196,208,124\nM 128,196,212,128\nM 108,192,212,108\nM 112,192,216,112\nM 132,196,216,132\nM 96,136,144,96\n"
"T 192,222,32768\nT 194,223,32768\nM 116,140,144,116\nR 200,221,36864\nT 196,224,32768\nM 120,140,148,120\nT 198,225,32768\nR 204,221,38912\n"
"M 100,136,148,100\nR 208,221,40960\nR 212,221,43008\nM 104,136,152,104\nR 216,221,45056\nM 124,140,152,124\nM 128,140,156,128\nM 108,136,156,108\n"
"M 112,136,160,112\nM 132,140,160,132\nW 0\ns_barrier\nM 96,164,172,96\nT 136,222,40960\nT 138,223,40960\nM 116,168,172,116\nR 144,220,0\n"
"T 140,224,40960\nM 120,168,176,120\nT 142,225,40960\nR 148,220,2048\nM 100,164,176,100\nR 152,220,4096\nR 156,220,6144\nM 104,164,180,104\n"
"R 160,220,8192\nM 124,168,180,124\nM 128,168,184,128\nM 108,164,184,108\nM 112,164,188,112\nM 132,168,188,132\nM 96,192,200,96\nT 164,222,49152\n"
"T 166,223,49152\nM 116,196,200,116\nR 172,221,0\nT 168,224,49152\nM 120,196,204,120\nT 170,225,49152\nR 176,221,2048\nM 100,192,204,100\n"
"R 180,221,4096\nR 184,221,6144\nM 104,192,208,104\nR 188,221,8192\nM 124,196,208,124\nM 128,196,212,128\nM 108,192,212,108\nM 112,192,216,112\n"
"M 132,196,216,132\nW 15\nM 96,136,144,96\nW 13\nM 116,140,144,116\nW 12\nM 120,140,148,120\nM 100,136,148,100\nW 11\nM 104,136,152,104\n"
"M 124,140,152,124\nW 10\nM 128,140,156,128\nM 108,136,156,108\nW 9\nM 112,136,160,112\nM 132,140,160,132\nW 6\nM 96,164,172,96\nW 4\nM 116,168,172,116\n"
"W 3\nM 120,168,176,120\nM 100,164,176,100\nW 2\nM 104,164,180,104\nM 124,168,180,124\nv_pk_mul_f32 v[96:97], v[96:97], v[252:253]\n"
"v_pk_mul_f32 v[98:99], v[98:99], v[252:253]\nC 96,96,97\nC 97,98,99\nds_write_b64 v233, v[96:97]\nv_pk_mul_f32 v[116:117], v[116:117], v[252:253]\n"
"v_pk_mul_f32 v[118:119], v[118:119], v[252:253]\nC 116,116,117\nC 117,118,119\nds_write_b64 v234, v[116:117]\nW 3\nM 128,168,184,128\n"
"M 108,164,184,108\nv_pk_mul_f32 v[100:101], v[100:101], v[252:253]\nv_pk_mul_f32 v[102:103], v[102:103], v[252:253]\nC 100,100,101\nC 101,102,103\n"
"ds_write_b64 v233, v[100:101] offset:4096\nv_pk_mul_f32 v[120:121], v[120:121], v[252:253]\nv_pk_mul_f32 v[122:123], v[122:123], v[252:253]\n"
"C 120,120,121\nC 121,122,123\nds_write_b64 v234, v[120:121] offset:4096\nW 4\nM 112,164,188,112\nM 132,168,188,132\n"
"v_pk_mul_f32 v[104:105], v[104:105], v[252:253]\nv_pk_mul_f32 v[106:107], v[106:107], v[252:253]\nC 104,104,105\nC 105,106,107\n"
"ds_write_b64 v233, v[104:105] offset:8192\nv_pk_mul_f32 v[124:125], v[124:125], v[252:253]\nv_pk_mul_f32 v[126:127], v[126:127], v[252:253]\n"
"C 124,124,125\nC 125,126,127\nds_write_b64 v234, v[124:125] offset:8192\nv_pk_mul_f32 v[108:109], v[108:109], v[252:253]\n"
"v_pk_mul_f32 v[110:111], v[110:111], v[252:253]\nC 108,108,109\nC 109,110,111\nds_write_b64 v233, v[108:109] offset:12288\n"
"v_pk_mul_f32 v[128:129], v[128:129], v[252:253]\nv_pk_mul_f32 v[130:131], v[130:131], v[252:253]\nC 128,128,129\nC 129,130,131\n"
"ds_write_b64 v234, v[128:129] offset:12288\nv_pk_mul_f32 v[112:113], v[112:113], v[252:253]\nv_pk_mul_f32 v[114:115], v[114:115], v[252:253]\n"
"C 112,112,113\nC 113,114,115\nds_write_b64 v233, v[112:113] offset:16384\nv_pk_mul_f32 v[132:133], v[132:133], v[252:253]\n"
"v_pk_mul_f32 v[134:135], v[134:135], v[252:253]\nC 132,132,133\nC 133,134,135\nds_write_b64 v234, v[132:133] offset:16384\nW 0\ns_barrier\nR 144,239,0\n"
"R 96,235,0\nR 112,235,4096\nR 128,235,8192\nR 148,239,512\nR 100,236,0\nR 116,236,4096\nR 132,236,8192\nR 152,239,1024\nR 104,237,0\nR 120,237,4096\n"
"R 136,237,8192\nW 10\nv_mfma_f32_16x16x32_bf16 v[168:171], v[144:147], v[96:99], 0\nW 9\n"
"v_mfma_f32_16x16x32_bf16 v[172:175], v[144:147], v[112:115], 0\nW 8\nv_mfma_f32_16x16x32_bf16 v[176:179], v[144:147], v[128:131], 0\nR 156,239,1536\n"
"R 108,238,0\nR 124,238,4096\nR 140,238,8192\nW 10\nM 168,148,100,168\nW 9\nM 172,148,116,172\nW 8\nM 176,148,132,176\nW 6\nM 168,152,104,168\nW 5\n"
"M 172,152,120,172\nW 4\nM 176,152,136,176\ns_waitcnt vmcnt(0)\nW 0\ns_barrier\nM 168,156,108,168\nM 172,156,124,172\nM 176,156,140,176\n"
"s_cmp_lg_u32 s35, 0\ns_cbranch_scc1 .Lmx_13_%=\n.Lmr0_14_%=:\ns_bitcmp1_b32 s19, 1\ns_cbranch_scc1 .Lv1_6_%=\nR 192,244,0\nR 196,244,256\n"
"R 200,244,512\nR 204,244,768\ns_cmp_eq_u32 s35, 0\ns_cbranch_scc0 .Lw_188_%=\nv_max3_f32 v180, v168, v169, v170\nv_max_f32 v180, v180, v171\n"
"v_max3_f32 v181, v172, v173, v174\nv_max_f32 v181, v181, v175\nv_max3_f32 v182, v176, v177, v178\nv_max_f32 v182, v182, v179\nv_mov_b32 v183, v180\n"
"v_mov_b32 v184, v181\nv_mov_b32 v185, v182\nv_permlane16_swap_b32 v180, v183\nv_permlane16_swap_b32 v181, v184\nv_permlane16_swap_b32 v182, v185\n"
"v_max_f32 v180, v180, v183\nv_max_f32 v181, v181, v184\nv_max_f32 v182, v182, v185\nv_mov_b32 v183, v180\nv_mov_b32 v184, v181\nv_mov_b32 v185, v182\n"
"v_permlane32_swap_b32 v180, v183\nv_permlane32_swap_b32 v181, v184\nv_permlane32_swap_b32 v182, v185\nv_max_f32 v180, v180, v183\n"
"v_max_f32 v181, v181, v184\nv_max_f32 v182, v182, v185\nv_sub_f32 v228, 0, v180\nv_sub_f32 v229, 0, v180\nv_sub_f32 v230, 0, v180\n"
"v_sub_f32 v231, 0, v180\nv_sub_f32 v232, 0, v181\nv_sub_f32 v233, 0, v181\nv_sub_f32 v234, 0, v181\nv_sub_f32 v235, 0, v181\nv_sub_f32 v236, 0, v182\n"
"v_sub_f32 v237, 0, v182\nv_sub_f32 v238, 0, v182\nv_sub_f32 v239, 0, v182\n.Lw_188_%=:\nW 3\nM 144,192,96,228\nM 152,192,112,232\nM 160,192,128,236\n"
"R 208,244,1024\ns_bitcmp1_b32 s21, 0\ns_cbranch_scc0 .Lw_189_%=\nds_write_b128 v251, v[0:3]\nds_write_b128 v251, v[4:7] offset:1024\n"
"ds_write_b128 v251, v[8:11] offset:2048\n.Lw_189_%=:\nW 3\nM 164,196,128,236\nM 156,196,112,232\nM 148,196,96,228\nR 192,244,1280\n"
"s_bitcmp1_b32 s21, 0\ns_cbranch_scc0 .Lw_190_%=\nds_write_b128 v251, v[12:15] offset:3072\nds_write_b128 v251, v[16:19] offset:4096\n"
"ds_write_b128 v251, v[20:23] offset:5120\n.Lw_190_%=:\nW 3\nM 144,200,100,144\nM 152,200,116,152\nM 160,200,132,160\nR 196,244,1536\n"
"s_bitcmp1_b32 s21, 0\ns_cbranch_scc0 .Lw_191_%=\nds_write_b128 v251, v[24:27] offset:6144\nds_write_b128 v251, v[28:31] offset:7168\n"
"ds_write_b128 v251, v[32:35] offset:32768\n.Lw_191_%=:\nW 3\nM 164,204,132,164\nM 156,204,116,156\nM 148,204,100,148\nR 200,244,1792\n"
"s_bitcmp1_b32 s21, 0\ns_cbranch_scc0 .Lw_192_%=\nds_write_b128 v251, v[36:39] offset:33792\nds_write_b128 v251, v[40:43] offset:34816\n"
"ds_write_b128 v251, v[44:47] offset:35840\n.Lw_192_%=:\nW 3\nM 144,208,104,144\nM 152,208,120,152\nM 160,208,136,160\nR 204,244,8192\n"
"s_bitcmp1_b32 s21, 0\ns_cbranch_scc0 .Lw_193_%=\nds_write_b128 v251, v[48:51] offset:36864\nds_write_b128 v251, v[52:55] offset:37888\n"
"ds_write_b128 v251, v[56:59] offset:38912\n.Lw_193_%=:\nW 3\nM 164,192,136,164\nM 156,192,120,156\nM 148,192,104,148\nR 208,244,8448\n"
"s_bitcmp1_b32 s21, 0\ns_cbranch_scc0 .Lw_194_%=\nds_write_b128 v251, v[60:63] offset:39936\n.Lw_194_%=:\nW 3\nM 144,196,108,144\nM 152,196,124,152\n"
"M 160,196,140,160\nR 192,244,8704\nW 3\nM 164,200,140,164\nM 156,200,124,156\nM 148,200,108,148\nR 196,244,8960\nW 3\nM 168,204,96,228\nE 144\nE 145\n"
"M 176,204,112,232\nE 146\nC 144,144,145\nM 184,204,128,236\nR 200,244,9216\nE 147\nW 3\nM 188,208,128,236\nE 152\nC 145,146,147\nM 180,208,112,232\n"
"E 153\nE 154\nM 172,208,96,228\nR 204,244,9472\nC 152,152,153\nW 3\nM 168,192,100,168\nE 155\nE 160\nM 176,192,116,176\nE 161\nC 153,154,155\n"
"M 184,192,132,184\nR 208,244,9728\nC 160,160,161\nW 3\nM 188,196,132,188\nE 162\nE 163\nM 180,196,116,180\nE 148\nC 161,162,163\nM 172,196,100,172\n"
"R 192,244,9984\nE 149\nW 3\nM 168,200,104,168\nE 150\nC 146,148,149\nM 176,200,120,176\nE 151\nE 156\nM 184,200,136,184\nC 147,150,151\nE 157\nW 0\n"
"s_barrier\nT 196,245,0\nT 198,245,256\nM 188,204,136,188\nT 200,246,0\nT 202,246,256\nC 154,156,157\nM 180,204,120,180\nE 158\nE 159\n"
"M 172,204,104,172\nC 155,158,159\nE 164\nM 168,208,108,168\nT 204,245,512\nT 206,245,768\nE 165\nM 176,208,124,176\nE 166\nC 162,164,165\n"
"M 184,208,140,184\nE 167\nM 188,192,140,188\nT 208,246,512\nT 210,246,768\nC 163,166,167\nM 180,192,124,180\nM 172,192,108,172\nW 6\n"
"v_mfma_f32_16x16x32_bf16 v[0:3], v[196:199], v[144:147], 0\nT 192,245,1024\nT 194,245,1280\nE 168\n"
"v_mfma_f32_16x16x32_bf16 v[212:215], v[240:243], v[144:147], 0\nE 169\nE 170\nv_mfma_f32_16x16x32_bf16 v[32:35], v[196:199], v[152:155], 0\nE 171\n"
"v_mfma_f32_16x16x32_bf16 v[64:67], v[196:199], v[160:163], 0\nC 168,168,169\nW 6\nv_mfma_f32_16x16x32_bf16 v[68:71], v[200:203], v[160:163], 0\n"
"T 196,246,1024\nT 198,246,1280\nv_mfma_f32_16x16x32_bf16 v[36:39], v[200:203], v[152:155], 0\nC 169,170,171\n"
"v_mfma_f32_16x16x32_bf16 v[216:219], v[240:243], v[152:155], 0\nE 176\nv_mfma_f32_16x16x32_bf16 v[4:7], v[200:203], v[144:147], 0\nW 6\n"
"v_mfma_f32_16x16x32_bf16 v[8:11], v[204:207], v[144:147], 0\nT 200,245,1536\nT 202,245,1792\n"
"v_mfma_f32_16x16x32_bf16 v[40:43], v[204:207], v[152:155], 0\nE 177\nv_mfma_f32_16x16x32_bf16 v[72:75], v[204:207], v[160:163], 0\nE 178\n"
"v_mfma_f32_16x16x32_bf16 v[220:223], v[240:243], v[160:163], 0\nC 176,176,177\nW 6\nv_mfma_f32_16x16x32_bf16 v[76:79], v[208:211], v[160:163], 0\n"
"T 204,246,1536\nT 206,246,1792\nv_mfma_f32_16x16x32_bf16 v[44:47], v[208:211], v[152:155], 0\nE 179\n"
"v_mfma_f32_16x16x32_bf16 v[12:15], v[208:211], v[144:147], 0\nC 177,178,179\nW 6\nv_mfma_f32_16x16x32_bf16 v[16:19], v[192:195], v[144:147], 0\n"
"v_add_u32 v244, 0x10000, v244\nR 208,244,0\nv_mfma_f32_16x16x32_bf16 v[48:51], v[192:195], v[152:155], 0\nE 184\n"
"v_mfma_f32_16x16x32_bf16 v[80:83], v[192:195], v[160:163], 0\nW 5\nv_mfma_f32_16x16x32_bf16 v[84:87], v[196:199], v[160:163], 0\nE 185\n"
"v_mfma_f32_16x16x32_bf16 v[52:55], v[196:199], v[152:155], 0\nE 186\nv_mfma_f32_16x16x32_bf16 v[20:23], v[196:199], v[144:147], 0\nR 192,244,256\nW 4\n"
"v_mfma_f32_16x16x32_bf16 v[24:27], v[200:203], v[144:147], 0\nE 187\nv_mfma_f32_16x16x32_bf16 v[56:59], v[200:203], v[152:155], 0\nE 172\n"
"v_mfma_f32_16x16x32_bf16 v[88:91], v[200:203], v[160:163], 0\nR 196,244,512\nW 3\nv_mfma_f32_16x16x32_bf16 v[92:95], v[204:207], v[160:163], 0\nE 173\n"
"v_mfma_f32_16x16x32_bf16 v[60:63], v[204:207], v[152:155], 0\nE 174\nv_mfma_f32_16x16x32_bf16 v[28:31], v[204:207], v[144:147], 0\nR 200,244,768\nW 3\n"
"M 144,208,96,228\nE 175\nM 152,208,112,232\nE 180\nM 160,208,128,236\nR 204,244,1024\nW 3\nM 164,192,128,236\nE 181\nM 156,192,112,232\nE 182\n"
"M 148,192,96,228\nR 208,244,1280\nW 3\nM 144,196,100,144\nE 183\nM 152,196,116,152\nE 188\nM 160,196,132,160\nR 192,244,1536\nW 3\nM 164,200,132,164\n"
"E 189\nM 156,200,116,156\nE 190\nM 148,200,100,148\nR 196,244,1792\nW 3\nM 144,204,104,144\nT 200,245,8192\nT 202,245,8448\nM 152,204,120,152\nE 191\n"
"M 160,204,136,160\nC 184,184,185\nW 4\nM 164,208,136,164\nT 204,246,8192\nT 206,246,8448\nM 156,208,120,156\nC 185,186,187\nC 170,172,173\n"
"M 148,208,104,148\nC 171,174,175\nW 5\nM 144,192,108,144\nT 208,245,8704\nT 210,245,8960\nC 178,180,181\nM 152,192,124,152\nC 179,182,183\n"
"C 186,188,189\nM 160,192,140,160\nC 187,190,191\nW 6\nM 164,196,140,164\nT 192,246,8704\nT 194,246,8960\nM 156,196,124,156\nM 148,196,108,148\nW 6\n"
"M 0,200,168,0\nT 196,245,9216\nT 198,245,9472\nE 144\nM 212,240,168,212\nE 145\nE 146\nM 32,200,176,32\nE 147\nM 64,200,184,64\nC 144,144,145\nW 6\n"
"M 68,204,184,68\nT 200,246,9216\nT 202,246,9472\nM 36,204,176,36\nC 145,146,147\nM 216,240,176,216\nE 152\nM 4,204,168,4\nW 6\nM 8,208,168,8\n"
"T 204,245,9728\nT 206,245,9984\nM 40,208,176,40\nE 153\nM 72,208,184,72\nE 154\nM 220,240,184,220\nC 152,152,153\nW 6\nM 76,192,184,76\nT 208,246,9728\n"
"T 210,246,9984\nM 44,192,176,44\nE 155\nM 12,192,168,12\nC 153,154,155\nW 6\nM 16,196,168,16\nE 160\nM 48,196,176,48\nE 161\nM 80,196,184,80\n"
"R 192,244,8192\nW 5\nM 84,200,184,84\nE 162\nM 52,200,176,52\nE 163\nM 20,200,168,20\nR 196,244,8448\nW 4\nM 24,204,168,24\nE 148\nM 56,204,176,56\n"
"E 149\nM 88,204,184,88\nR 200,244,8704\nW 3\nM 92,208,184,92\nE 150\nM 60,208,176,60\nE 151\nM 28,208,168,28\nR 204,244,8960\nW 3\nM 168,192,96,228\n"
"E 156\nM 176,192,112,232\nE 157\nM 184,192,128,236\nR 208,244,9216\nW 3\nM 188,196,128,236\nE 158\nM 180,196,112,232\nE 159\nM 172,196,96,228\n"
"R 192,244,9472\nW 3\nM 168,200,100,168\nE 164\nM 176,200,116,176\nE 165\nM 184,200,132,184\nR 196,244,9728\nW 3\nM 188,204,132,188\nE 166\n"
"M 180,204,116,180\nE 167\nM 172,204,100,172\nR 200,244,9984\nW 3\nM 168,208,104,168\nC 160,160,161\nC 161,162,163\nM 176,208,120,176\nC 146,148,149\n"
"C 147,150,151\nM 184,208,136,184\nC 154,156,157\nW 0\ns_barrier\nv_add_u32 v245, 0x10000, v245\nv_add_u32 v246, 0x10000, v246\nT 204,245,0\n"
"T 206,245,256\nM 188,192,136,188\nT 208,246,0\nT 210,246,256\nM 180,192,120,180\nC 155,158,159\nC 162,164,165\nM 172,192,104,172\nC 163,166,167\n"
"M 168,196,108,168\nT 192,245,512\nT 194,245,768\nM 176,196,124,176\nM 184,196,140,184\nM 188,200,140,188\nT 196,246,512\nT 198,246,768\n"
"M 180,200,124,180\nM 172,200,108,172\nW 6\nM 0,204,144,0\nT 200,245,1024\nT 202,245,1280\nE 168\nM 212,240,144,212\nE 169\nE 170\nM 32,204,152,32\n"
"E 171\nM 64,204,160,64\nC 168,168,169\nW 6\nM 68,208,160,68\nT 204,246,1024\nT 206,246,1280\nM 36,208,152,36\nC 169,170,171\nM 216,240,152,216\nE 176\n"
"M 4,208,144,4\nW 6\nM 8,192,144,8\nT 208,245,1536\nT 210,245,1792\nM 40,192,152,40\nE 177\nM 72,192,160,72\nE 178\nM 220,240,160,220\nC 176,176,177\n"
"W 6\nM 76,196,160,76\nT 192,246,1536\nT 194,246,1792\nM 44,196,152,44\nE 179\nM 12,196,144,12\nC 177,178,179\nW 6\nM 16,200,144,16\n"
"v_add_u32 v244, 0x10000, v244\nR 196,244,0\nM 48,200,152,48\nE 184\nM 80,200,160,80\nW 5\nM 84,204,160,84\nE 185\nM 52,204,152,52\nE 186\n"
"M 20,204,144,20\nR 200,244,256\nW 4\nM 24,208,144,24\nE 187\nM 56,208,152,56\nE 172\nM 88,208,160,88\nR 204,244,512\nW 3\nM 92,192,160,92\nE 173\n"
"M 60,192,152,60\nE 174\nM 28,192,144,28\nR 208,244,768\nW 3\nM 144,196,96,228\nE 175\nM 152,196,112,232\nE 180\nM 160,196,128,236\nR 192,244,1024\nW 3\n"
"M 164,200,128,236\nE 181\nM 156,200,112,232\nE 182\nM 148,200,96,228\nR 196,244,1280\nW 3\nM 144,204,100,144\nE 183\nM 152,204,116,152\nE 188\n"
"M 160,204,132,160\nR 200,244,1536\nW 3\nM 164,208,132,164\nE 189\nM 156,208,116,156\nE 190\nM 148,208,100,148\nR 204,244,1792\nW 3\nM 144,192,104,144\n"
"T 208,245,8192\nT 210,245,8448\nM 152,192,120,152\nE 191\nM 160,192,136,160\nC 184,184,185\nW 4\nM 164,196,136,164\nT 192,246,8192\nT 194,246,8448\n"
"M 156,196,120,156\nC 185,186,187\nC 170,172,173\nM 148,196,104,148\nC 171,174,175\nW 5\nM 144,200,108,144\nT 196,245,8704\nT 198,245,8960\n"
"C 178,180,181\nM 152,200,124,152\nC 179,182,183\nC 186,188,189\nM 160,200,140,160\nC 187,190,191\nW 6\nM 164,204,140,164\nT 200,246,8704\n"
"T 202,246,8960\nM 156,204,124,156\nM 148,204,108,148\nW 6\nM 0,208,168,0\nT 204,245,9216\nT 206,245,9472\nE 144\nM 212,240,168,212\nE 145\nE 146\n"
"M 32,208,176,32\nE 147\nM 64,208,184,64\nC 144,144,145\nW 6\nM 68,192,184,68\nT 208,246,9216\nT 210,246,9472\nM 36,192,176,36\nC 145,146,147\n"
"M 216,240,176,216\nE 152\nM 4,192,168,4\nW 6\nM 8,196,168,8\nT 192,245,9728\nT 194,245,9984\nM 40,196,176,40\nE 153\nM 72,196,184,72\nE 154\n"
"M 220,240,184,220\nC 152,152,153\nW 6\nM 76,200,184,76\nT 196,246,9728\nT 198,246,9984\nM 44,200,176,44\nE 155\nM 12,200,168,12\nC 153,154,155\nW 6\n"
"M 16,204,168,16\nE 160\nM 48,204,176,48\nE 161\nM 80,204,184,80\nR 200,244,8192\nW 5\nM 84,208,184,84\nE 162\nM 52,208,176,52\nE 163\nM 20,208,168,20\n"
"R 204,244,8448\nW 4\nM 24,192,168,24\nE 148\nM 56,192,176,56\nE 149\nM 88,192,184,88\nR 208,244,8704\nW 3\nM 92,196,184,92\nE 150\nM 60,196,176,60\n"
"E 151\nM 28,196,168,28\nR 192,244,8960\nW 3\nM 168,200,96,228\nE 156\nM 176,200,112,232\nE 157\nM 184,200,128,236\nR 196,244,9216\nW 3\n"
"M 188,204,128,236\nE 158\nM 180,204,112,232\nE 159\nM 172,204,96,228\nR 200,244,9472\nW 3\nM 168,208,100,168\nE 164\nM 176,208,116,176\nE 165\n"
"M 184,208,132,184\nR 204,244,9728\nW 3\nM 188,192,132,188\nE 166\nM 180,192,116,180\nE 167\nM 172,192,100,172\nR 208,244,9984\nW 3\nM 168,196,104,168\n"
"C 160,160,161\nC 161,162,163\nM 176,196,120,176\nC 146,148,149\nC 147,150,151\nM 184,196,136,184\nC 154,156,157\nW 0\ns_barrier\n"
"v_add_u32 v245, 0xfffe8000, v245\nv_add_u32 v246, 0xfffe8000, v246\nT 192,245,0\nT 194,245,256\nM 188,200,136,188\nT 196,246,0\nT 198,246,256\n"
"M 180,200,120,180\nC 155,158,159\nC 162,164,165\nM 172,200,104,172\nC 163,166,167\nM 168,204,108,168\nT 200,245,512\nT 202,245,768\nM 176,204,124,176\n"
"M 184,204,140,184\nM 188,208,140,188\nT 204,246,512\nT 206,246,768\nM 180,208,124,180\nM 172,208,108,172\nW 6\nM 0,192,144,0\nT 208,245,1024\n"
"T 210,245,1280\nE 168\nM 212,240,144,212\nE 169\nE 170\nM 32,192,152,32\nE 171\nM 64,192,160,64\nC 168,168,169\nW 6\nM 68,196,160,68\nT 192,246,1024\n"
"T 194,246,1280\nM 36,196,152,36\nC 169,170,171\nM 216,240,152,216\nE 176\nM 4,196,144,4\nW 6\nM 8,200,144,8\nT 196,245,1536\nT 198,245,1792\n"
"M 40,200,152,40\nE 177\nM 72,200,160,72\nE 178\nM 220,240,160,220\nC 176,176,177\nW 6\nM 76,204,160,76\nT 200,246,1536\nT 202,246,1792\n"
"M 44,204,152,44\nE 179\nM 12,204,144,12\nC 177,178,179\nW 6\nM 16,208,144,16\nv_add_u32 v244, 0xfffe8000, v244\nR 204,244,0\nM 48,208,152,48\nE 184\n"
"M 80,208,160,80\nW 5\nM 84,192,160,84\nE 185\nM 52,192,152,52\nE 186\nM 20,192,144,20\nR 208,244,256\nW 4\nM 24,196,144,24\nE 187\nM 56,196,152,56\n"
"E 172\nM 88,196,160,88\nR 192,244,512\nW 3\nM 92,200,160,92\nE 173\nM 60,200,152,60\nE 174\nM 28,200,144,28\nR 196,244,768\nW 3\nM 144,204,96,228\n"
"E 175\nM 152,204,112,232\nE 180\nM 160,204,128,236\nR 200,244,1024\nW 3\nM 164,208,128,236\nE 181\nM 156,208,112,232\nE 182\nM 148,208,96,228\n"
"R 204,244,1280\nW 3\nM 144,192,100,144\nE 183\nM 152,192,116,152\nE 188\nM 160,192,132,160\nR 208,244,1536\nW 3\nM 164,196,132,164\nE 189\n"
"M 156,196,116,156\nE 190\nM 148,196,100,148\nR 192,244,1792\nW 3\nM 144,200,104,144\nT 196,245,8192\nT 198,245,8448\nM 152,200,120,152\nE 191\n"
"M 160,200,136,160\nC 184,184,185\nW 4\nM 164,204,136,164\nT 200,246,8192\nT 202,246,8448\nM 156,204,120,156\nC 185,186,187\nC 170,172,173\n"
"M 148,204,104,148\nC 171,174,175\nW 5\nM 144,208,108,144\nT 204,245,8704\nT 206,245,8960\nC 178,180,181\nM 152,208,124,152\nC 179,182,183\n"
"C 186,188,189\nM 160,208,140,160\nC 187,190,191\nW 6\nM 164,192,140,164\nT 208,246,8704\nT 210,246,8960\nM 156,192,124,156\nM 148,192,108,148\nW 6\n"
"M 0,196,168,0\nT 192,245,9216\nT 194,245,9472\nE 144\nM 212,240,168,212\nE 145\nE 146\nM 32,196,176,32\nE 147\nM 64,196,184,64\nC 144,144,145\nW 6\n"
"M 68,200,184,68\nT 196,246,9216\nT 198,246,9472\nM 36,200,176,36\nC 145,146,147\nM 216,240,176,216\nE 152\nM 4,200,168,4\nW 6\nM 8,204,168,8\n"
"T 200,245,9728\nT 202,245,9984\nM 40,204,176,40\nE 153\nM 72,204,184,72\nE 154\nM 220,240,184,220\nC 152,152,153\nW 6\nM 76,208,184,76\nT 204,246,9728\n"
"T 206,246,9984\nM 44,208,176,44\nE 155\nM 12,208,168,12\nC 153,154,155\nW 6\nM 16,192,168,16\nE 160\nM 48,192,176,48\nE 161\nM 80,192,184,80\n"
"R 208,244,8192\nW 5\nM 84,196,184,84\nE 162\nM 52,196,176,52\nE 163\nM 20,196,168,20\nR 192,244,8448\nW 4\nM 24,200,168,24\nE 148\nM 56,200,176,56\n"
"E 149\nM 88,200,184,88\nR 196,244,8704\nW 3\nM 92,204,184,92\nE 150\nM 60,204,176,60\nE 151\nM 28,204,168,28\nR 200,244,8960\nW 3\nM 168,208,96,228\n"
"E 156\nM 176,208,112,232\nE 157\nM 184,208,128,236\nR 204,244,9216\nW 3\nM 188,192,128,236\nE 158\nM 180,192,112,232\nE 159\nM 172,192,96,228\n"
"R 208,244,9472\nW 3\nM 168,196,100,168\nE 164\nM 176,196,116,176\nE 165\nM 184,196,132,184\nR 192,244,9728\nW 3\nM 188,200,132,188\nE 166\n"
"M 180,200,116,180\nE 167\nM 172,200,100,172\nR 196,244,9984\nW 3\nM 168,204,104,168\nC 160,160,161\nC 161,162,163\nM 176,204,120,176\nC 146,148,149\n"
"C 147,150,151\nM 184,204,136,184\nC 154,156,157\ns_and_b32 s71, s21, 3\ns_lshl_b32 s71, s71, 1\ns_mul_i32 s28, s58, 160\ns_mul_i32 s27, s59, 1310720\n"
"s_add_u32 s27, s27, s28\ns_add_u32 s44, s12, s27\ns_addc_u32 s45, s13, 0\ns_and_b32 s45, s45, 65535\ns_mov_b32 s46, 0\ns_mov_b32 s47, 131072\n"
"s_sub_u32 s46, 1310720, s28\ns_mul_i32 s31, s20, 5120\ns_mul_i32 s27, s21, 184320\ns_add_u32 s27, s27, s28\ns_add_u32 s40, s14, s27\n"
"s_addc_u32 s41, s15, 0\ns_and_b32 s41, s41, 65535\ns_mov_b32 s42, 0\ns_mov_b32 s43, 131072\ns_sub_u32 s42, 184320, s28\ns_add_u32 s48, s16, 0\n"
"s_addc_u32 s49, s17, 0\ns_and_b32 s49, s49, 65535\ns_mov_b32 s50, 67108864\ns_mov_b32 s51, 131072\ns_mul_i32 s64, s56, 36864\n"
"s_and_b32 s26, s56, 4294967288\ns_mul_i32 s65, s26, 36864\ns_lshl_b32 s66, s26, 8\ns_add_u32 s66, s66, 33554432\ns_lshl_b32 s26, s21, 3\n"
"s_add_u32 s26, s26, s58\ns_mul_i32 s67, s26, 196608\ns_add_u32 s67, s67, 16777216\ns_mov_b32 s72, 153600\ns_mul_i32 s26, s57, -6144\n"
"s_add_u32 s72, s72, s26\ns_mov_b32 s74, 147456\ns_mul_i32 s26, s57, 6144\ns_add_u32 s74, s74, s26\ns_mov_b32 s75, 159936\ns_mul_i32 s26, s57, -192\n"
"s_add_u32 s75, s75, s26\ns_mov_b32 s76, 24576\ns_mul_i32 s26, s57, 2048\ns_add_u32 s76, s76, s26\ns_mov_b32 s73, 159744\ns_mul_i32 s26, s19, 192\n"
"s_add_u32 s73, s73, s26\ns_add_u32 s26, s71, 0\ns_cmp_ge_u32 s26, 7\ns_cselect_b32 s27, -6, 1\ns_add_u32 s26, s26, s27\ns_add_u32 s26, s26, s58\n"
"s_and_b32 s26, s26, 7\ns_mul_i32 s5, s26, 36864\ns_add_u32 s5, s5, s65\ns_add_u32 s26, s71, 1\ns_cmp_ge_u32 s26, 7\ns_cselect_b32 s27, -6, 1\n"
"s_add_u32 s26, s26, s27\ns_add_u32 s26, s26, s58\ns_and_b32 s26, s26, 7\ns_mul_i32 s6, s26, 36864\ns_add_u32 s6, s6, s65\ns_add_u32 s26, s71, 2\n"
"s_cmp_ge_u32 s26, 7\ns_cselect_b32 s27, -6, 1\ns_add_u32 s26, s26, s27\ns_add_u32 s26, s26, s58\ns_and_b32 s26, s26, 7\ns_mul_i32 s7, s26, 36864\n"
"s_add_u32 s7, s7, s65\ns_add_u32 s26, s71, 3\ns_cmp_ge_u32 s26, 7\ns_cselect_b32 s27, -6, 1\ns_add_u32 s26, s26, s27\ns_add_u32 s26, s26, s58\n"
"s_and_b32 s26, s26, 7\ns_mul_i32 s8, s26, 36864\ns_add_u32 s8, s8, s65\ns_add_u32 s26, s71, 4\ns_cmp_ge_u32 s26, 7\ns_cselect_b32 s27, -6, 1\n"
"s_add_u32 s26, s26, s27\ns_add_u32 s26, s26, s58\ns_and_b32 s26, s26, 7\ns_mul_i32 s9, s26, 36864\ns_add_u32 s9, s9, s65\ns_add_u32 s26, s71, 5\n"
"s_cmp_ge_u32 s26, 7\ns_cselect_b32 s27, -6, 1\ns_add_u32 s26, s26, s27\ns_add_u32 s26, s26, s58\ns_and_b32 s26, s26, 7\ns_mul_i32 s10, s26, 36864\n"
"s_add_u32 s10, s10, s65\ns_add_u32 s26, s71, 6\ns_cmp_ge_u32 s26, 7\ns_cselect_b32 s27, -6, 1\ns_add_u32 s26, s26, s27\ns_add_u32 s26, s26, s58\n"
"s_and_b32 s26, s26, 7\ns_mul_i32 s11, s26, 36864\ns_add_u32 s11, s11, s65\ns_mov_b32 s4, 0\ns_mov_b32 s77, 0\ns_mov_b32 s78, 0\n"
"s_mul_i32 s26, s57, 6144\ns_add_u32 s78, s78, s26\ns_mov_b32 s79, 32768\ns_mul_i32 s26, s57, 2048\ns_add_u32 s79, s79, s26\ns_mov_b32 s80, 0\n"
"s_mul_i32 s26, s57, 10240\ns_add_u32 s80, s80, s26\ns_mul_i32 s28, s59, 12288\ns_lshl_b32 s56, s19, 10\ns_lshl_b32 s29, s28, 2\n"
"s_add_u32 s29, s29, s67\ns_add_u32 s28, s28, s67\ns_xor_b32 s26, s59, 1\ns_mul_i32 s26, s26, 12288\ns_add_u32 s23, s26, s29\ns_lshl_b32 s26, s26, 2\n"
"s_add_u32 s26, s26, s28\ns_add_u32 s12, s26, s56\ns_xor_b32 s26, s59, 2\ns_mul_i32 s26, s26, 12288\ns_add_u32 s24, s26, s29\ns_lshl_b32 s26, s26, 2\n"
"s_add_u32 s26, s26, s28\ns_add_u32 s13, s26, s56\ns_xor_b32 s26, s59, 3\ns_mul_i32 s26, s26, 12288\ns_add_u32 s54, s26, s29\ns_lshl_b32 s26, s26, 2\n"
"s_add_u32 s26, s26, s28\ns_add_u32 s14, s26, s56\ns_lshl_b32 s26, s57, 10\ns_add_u32 s26, s26, 9216\ns_mul_i32 s27, s25, 49152\n"
"s_add_u32 s27, s27, s28\ns_add_u32 s15, s26, s27\ns_cmp_eq_u32 s59, s25\ns_cselect_b32 s69, s26, 268435456\ns_cselect_b32 s99, s34, 0\n"
"s_add_u32 s22, s28, 11264\ns_or_b32 s26, s59, s19\ns_cselect_b32 s98, 268435456, 0\ns_cselect_b32 s67, 0, s34\ns_add_u32 s98, s98, 11264\n"
"v_lshrrev_b32 v254, 4, v255\nv_lshrrev_b32 v253, 2, v255\nv_xor_b32 v254, v254, v253\nv_and_b32 v224, 3, v254\nv_cmp_eq_u32 vcc, 0, v224\n"
"v_and_b32 v254, 1, v255\nv_lshlrev_b32 v254, 4, v254\nv_mov_b32 v253, 16256\nv_lshlrev_b32 v225, v254, v253\nv_cndmask_b32 v225, 0, v225, vcc\n"
"v_and_b32 v224, 2, v255\nv_cmp_eq_u32 vcc, 0, v224\ns_nop 1\nv_cndmask_b32 v224, 0, v225, vcc\nv_xor_b32 v225, v225, v224\nv_min_u32 v226, 6, v255\n"
"v_add_u32 v226, s71, v226\nv_subrev_u32 v252, 7, v226\nv_min_u32 v226, v226, v252\nv_add_u32 v252, s58, v226\nv_add_u32 v252, 1, v252\n"
"v_and_b32 v226, 7, v252\nv_lshlrev_b32 v254, 8, v226\nv_mov_b32 v252, s57\nv_xor_b32 v253, 1, v252\nv_lshlrev_b32 v253, 28, v253\n"
"v_add_u32 v226, v254, v253\nv_lshrrev_b32 v254, 2, v255\nv_mul_u32_u24 v254, 160, v254\nv_and_b32 v252, 3, v255\nv_lshlrev_b32 v252, 3, v252\n"
"v_add_u32 v254, v254, v252\nv_add_u32 v254, s80, v254\nv_add_u32 v253, 36864, v254\nv_add_u32 v227, 5120, v253\nv_and_b32 v254, 15, v255\n"
"v_mul_u32_u24 v254, 1280, v254\nv_lshrrev_b32 v252, 4, v255\nv_lshl_add_u32 v254, v252, 3, v254\nv_lshlrev_b32 v252, 4, v255\nW 0\ns_barrier\n"
"v_add_u32 v245, 0x10000, v245\nv_add_u32 v246, 0x10000, v246\nT 200,245,0\nT 202,245,256\nM 188,208,136,188\nT 204,246,0\nT 206,246,256\n"
"M 180,208,120,180\nC 155,158,159\nC 162,164,165\nM 172,208,104,172\nC 163,166,167\nM 168,192,108,168\nT 208,245,512\nT 210,245,768\nM 176,192,124,176\n"
"M 184,192,140,184\nM 188,196,140,188\nT 192,246,512\nT 194,246,768\nM 180,196,124,180\nM 172,196,108,172\nW 6\nM 0,200,144,0\nT 196,245,1024\n"
"T 198,245,1280\nE 168\nM 212,240,144,212\nE 169\nE 170\nM 32,200,152,32\nE 171\nM 64,200,160,64\nC 168,168,169\nW 6\nM 68,204,160,68\nT 200,246,1024\n"
"T 202,246,1280\nM 36,204,152,36\nC 169,170,171\nM 216,240,152,216\nE 176\nM 4,204,144,4\nW 6\nM 8,208,144,8\nT 204,245,1536\nT 206,245,1792\n"
"M 40,208,152,40\nE 177\nM 72,208,160,72\nE 178\nM 220,240,160,220\nC 176,176,177\nW 6\nM 76,192,160,76\nT 208,246,1536\nT 210,246,1792\n"
"M 44,192,152,44\nE 179\nM 12,192,144,12\nC 177,178,179\nW 6\nM 16,196,144,16\nv_add_u32 v244, 0x10000, v244\nR 192,244,0\nM 48,196,152,48\nE 184\n"
"M 80,196,160,80\nW 5\nM 84,200,160,84\nE 185\nM 52,200,152,52\nE 186\nM 20,200,144,20\nR 196,244,256\nW 4\nM 24,204,144,24\nE 187\nM 56,204,152,56\n"
"E 172\nM 88,204,160,88\nR 200,244,512\nW 3\nM 92,208,160,92\nE 173\nM 60,208,152,60\nE 174\nM 28,208,144,28\nR 204,244,768\nW 3\nM 144,192,96,228\n"
"E 175\nM 152,192,112,232\nE 180\nM 160,192,128,236\nR 208,244,1024\nW 3\nM 164,196,128,236\nE 181\nM 156,196,112,232\nE 182\nM 148,196,96,228\n"
"R 192,244,1280\nW 3\nM 144,200,100,144\nE 183\nM 152,200,116,152\nE 188\nM 160,200,132,160\nR 196,244,1536\nW 3\nM 164,204,132,164\nE 189\n"
"M 156,204,116,156\nE 190\nM 148,204,100,148\nR 200,244,1792\nW 3\nM 144,208,104,144\nT 204,245,8192\nT 206,245,8448\nM 152,208,120,152\nE 191\n"
"M 160,208,136,160\nC 184,184,185\nW 4\nM 164,192,136,164\nT 208,246,8192\nT 210,246,8448\nM 156,192,120,156\nC 185,186,187\nC 170,172,173\n"
"M 148,192,104,148\nC 171,174,175\nW 5\nM 144,196,108,144\nT 192,245,8704\nT 194,245,8960\nC 178,180,181\nM 152,196,124,152\nC 179,182,183\n"
"C 186,188,189\nM 160,196,140,160\nC 187,190,191\nW 6\nM 164,200,140,164\nT 196,246,8704\nT 198,246,8960\nM 156,200,124,156\nM 148,200,108,148\nW 6\n"
"M 0,204,168,0\nT 200,245,9216\nT 202,245,9472\nE 144\nM 212,240,168,212\nE 145\nE 146\nM 32,204,176,32\nE 147\nM 64,204,184,64\nC 144,144,145\nW 6\n"
"M 68,208,184,68\nT 204,246,9216\nT 206,246,9472\nM 36,208,176,36\nC 145,146,147\nM 216,240,176,216\nE 152\nM 4,208,168,4\nW 6\nM 8,192,168,8\n"
"T 208,245,9728\nT 210,245,9984\nM 40,192,176,40\nE 153\nM 72,192,184,72\nE 154\nM 220,240,184,220\nC 152,152,153\nW 6\nM 76,196,184,76\nT 192,246,9728\n"
"T 194,246,9984\nM 44,196,176,44\nE 155\nM 12,196,168,12\nC 153,154,155\nW 6\nM 16,200,168,16\nE 160\nM 48,200,176,48\nE 161\nM 80,200,184,80\n"
"R 196,244,8192\nW 5\nM 84,204,184,84\nE 162\nM 52,204,176,52\nE 163\nM 20,204,168,20\nR 200,244,8448\nW 4\nM 24,208,168,24\nE 148\nM 56,208,176,56\n"
"E 149\nM 88,208,184,88\nR 204,244,8704\nW 3\nM 92,192,184,92\nE 150\nM 60,192,176,60\nE 151\nM 28,192,168,28\nR 208,244,8960\nW 3\nM 168,196,96,228\n"
"E 156\nM 176,196,112,232\nE 157\nM 184,196,128,236\nR 192,244,9216\nW 3\nM 188,200,128,236\nE 158\nM 180,200,112,232\nE 159\nM 172,200,96,228\n"
"R 196,244,9472\nW 3\nM 168,204,100,168\nE 164\nM 176,204,116,176\nE 165\nM 184,204,132,184\nR 200,244,9728\nW 3\nM 188,208,132,188\nE 166\n"
"M 180,208,116,180\nE 167\nM 172,208,100,172\nR 204,244,9984\nW 3\nM 168,192,104,168\nC 160,160,161\nC 161,162,163\nM 176,192,120,176\nC 146,148,149\n"
"C 147,150,151\nM 184,192,136,184\nC 154,156,157\nW 0\ns_barrier\nv_add_u32 v245, 0x10000, v245\nv_add_u32 v246, 0x10000, v246\nT 208,245,0\n"
"T 210,245,256\nM 188,196,136,188\nT 192,246,0\nT 194,246,256\nM 180,196,120,180\nC 155,158,159\nC 162,164,165\nM 172,196,104,172\nC 163,166,167\n"
"M 168,200,108,168\nT 196,245,512\nT 198,245,768\nM 176,200,124,176\nM 184,200,140,184\nM 188,204,140,188\nT 200,246,512\nT 202,246,768\n"
"M 180,204,124,180\nM 172,204,108,172\nW 6\nM 0,208,144,0\nT 204,245,1024\nT 206,245,1280\nE 168\nM 212,240,144,212\nE 169\nE 170\nM 32,208,152,32\n"
"E 171\nM 64,208,160,64\nC 168,168,169\nW 6\nM 68,192,160,68\nT 208,246,1024\nT 210,246,1280\nM 36,192,152,36\nC 169,170,171\nM 216,240,152,216\nE 176\n"
"M 4,192,144,4\nW 6\nM 8,196,144,8\nT 192,245,1536\nT 194,245,1792\nM 40,196,152,40\nE 177\nM 72,196,160,72\nE 178\nM 220,240,160,220\nC 176,176,177\n"
"W 6\nM 76,200,160,76\nT 196,246,1536\nT 198,246,1792\nM 44,200,152,44\nE 179\nM 12,200,144,12\nC 177,178,179\nW 6\nM 16,204,144,16\n"
"v_add_u32 v244, 0xfffe8000, v244\nR 200,244,0\nM 48,204,152,48\nE 184\nM 80,204,160,80\nW 5\nM 84,208,160,84\nE 185\nM 52,208,152,52\nE 186\n"
"M 20,208,144,20\nR 204,244,256\nW 4\nM 24,192,144,24\nE 187\nM 56,192,152,56\nE 172\nM 88,192,160,88\nR 208,244,512\nW 3\nM 92,196,160,92\nE 173\n"
"M 60,196,152,60\nE 174\nM 28,196,144,28\nR 192,244,768\nW 3\nM 144,200,96,228\nE 175\nM 152,200,112,232\nE 180\nM 160,200,128,236\nR 196,244,1024\nW 3\n"
"M 164,204,128,236\nE 181\nM 156,204,112,232\nE 182\nM 148,204,96,228\nR 200,244,1280\nW 3\nM 144,208,100,144\nE 183\nM 152,208,116,152\nE 188\n"
"M 160,208,132,160\nR 204,244,1536\nW 3\nM 164,192,132,164\nE 189\nM 156,192,116,156\nE 190\nM 148,192,100,148\nR 208,244,1792\nW 3\nM 144,196,104,144\n"
"T 192,245,8192\nT 194,245,8448\nM 152,196,120,152\nE 191\nM 160,196,136,160\nC 184,184,185\nW 4\nM 164,200,136,164\nT 196,246,8192\nT 198,246,8448\n"
"M 156,200,120,156\nC 185,186,187\nC 170,172,173\nM 148,200,104,148\nC 171,174,175\nW 5\nM 144,204,108,144\nT 200,245,8704\nT 202,245,8960\n"
"C 178,180,181\nM 152,204,124,152\nC 179,182,183\nC 186,188,189\nM 160,204,140,160\nC 187,190,191\nW 6\nM 164,208,140,164\nT 204,246,8704\n"
"T 206,246,8960\nM 156,208,124,156\nM 148,208,108,148\nW 6\nM 0,192,168,0\nT 208,245,9216\nT 210,245,9472\nE 144\nM 212,240,168,212\nE 145\nE 146\n"
"M 32,192,176,32\nE 147\nM 64,192,184,64\nC 144,144,145\nW 6\nM 68,196,184,68\nT 192,246,9216\nT 194,246,9472\nM 36,196,176,36\nC 145,146,147\n"
"M 216,240,176,216\nE 152\nM 4,196,168,4\nW 6\nM 8,200,168,8\nT 196,245,9728\nT 198,245,9984\nM 40,200,176,40\nE 153\nM 72,200,184,72\nE 154\n"
"M 220,240,184,220\nC 152,152,153\nW 6\nM 76,204,184,76\nT 200,246,9728\nT 202,246,9984\nM 44,204,176,44\nE 155\nM 12,204,168,12\nC 153,154,155\nW 6\n"
"M 16,208,168,16\nE 160\nM 48,208,176,48\nE 161\nM 80,208,184,80\nR 204,244,8192\nW 5\nM 84,192,184,84\nE 162\nM 52,192,176,52\nE 163\nM 20,192,168,20\n"
"R 208,244,8448\nW 4\nM 24,196,168,24\nE 148\nM 56,196,176,56\nE 149\nM 88,196,184,88\nR 192,244,8704\nW 3\nM 92,200,184,92\nE 150\nM 60,200,176,60\n"
"E 151\nM 28,200,168,28\nR 196,244,8960\nW 3\nM 168,204,96,228\nE 156\nM 176,204,112,232\nE 157\nM 184,204,128,236\nR 200,244,9216\nW 3\n"
"M 188,208,128,236\nE 158\nM 180,208,112,232\nE 159\nM 172,208,96,228\nR 204,244,9472\nW 3\nM 168,192,100,168\nE 164\nM 176,192,116,176\nE 165\n"
"M 184,192,132,184\nR 208,244,9728\nW 3\nM 188,196,132,188\nE 166\nM 180,196,116,180\nE 167\nM 172,196,100,172\nR 192,244,9984\nW 3\nM 168,200,104,168\n"
"C 160,160,161\nC 161,162,163\nM 176,200,120,176\nC 146,148,149\nC 147,150,151\nM 184,200,136,184\nC 154,156,157\nW 0\ns_barrier\n"
"v_add_u32 v245, 0xfffe8000, v245\nv_add_u32 v246, 0xfffe8000, v246\nT 196,245,0\nT 198,245,256\nM 188,204,136,188\nT 200,246,0\nT 202,246,256\n"
"M 180,204,120,180\nC 155,158,159\nC 162,164,165\nM 172,204,104,172\nC 163,166,167\nM 168,208,108,168\nT 204,245,512\nT 206,245,768\nM 176,208,124,176\n"
"M 184,208,140,184\nM 188,192,140,188\nT 208,246,512\nT 210,246,768\nM 180,192,124,180\nM 172,192,108,172\nW 6\nM 0,196,144,0\nT 192,245,1024\n"
"T 194,245,1280\nE 168\nM 212,240,144,212\nE 169\nE 170\nM 32,196,152,32\nE 171\nM 64,196,160,64\nC 168,168,169\nW 6\nM 68,200,160,68\nT 196,246,1024\n"
"T 198,246,1280\nM 36,200,152,36\nC 169,170,171\nM 216,240,152,216\nE 176\nM 4,200,144,4\nW 6\nM 8,204,144,8\nT 200,245,1536\nT 202,245,1792\n"
"M 40,204,152,40\nE 177\nM 72,204,160,72\nE 178\nM 220,240,160,220\nC 176,176,177\nW 6\nM 76,208,160,76\nT 204,246,1536\nT 206,246,1792\n"
"M 44,208,152,44\nE 179\nM 12,208,144,12\nC 177,178,179\nW 6\nM 16,192,144,16\nv_add_u32 v244, 0x10000, v244\nR 208,244,0\nM 48,192,152,48\nE 184\n"
"M 80,192,160,80\nW 5\nM 84,196,160,84\nE 185\nM 52,196,152,52\nE 186\nM 20,196,144,20\nR 192,244,256\nW 4\nM 24,200,144,24\nE 187\nM 56,200,152,56\n"
"E 172\nM 88,200,160,88\nR 196,244,512\nW 3\nM 92,204,160,92\nE 173\nM 60,204,152,60\nE 174\nM 28,204,144,28\nR 200,244,768\nW 3\nM 144,208,96,228\n"
"E 175\nM 152,208,112,232\nE 180\nM 160,208,128,236\nR 204,244,1024\nW 3\nM 164,192,128,236\nE 181\nM 156,192,112,232\nE 182\nM 148,192,96,228\n"
"R 208,244,1280\nW 3\nM 144,196,100,144\nE 183\nM 152,196,116,152\nE 188\nM 160,196,132,160\nR 192,244,1536\nW 3\nM 164,200,132,164\nE 189\n"
"M 156,200,116,156\nE 190\nM 148,200,100,148\nR 196,244,1792\nW 3\nM 144,204,104,144\nT 200,245,8192\nT 202,245,8448\nM 152,204,120,152\nE 191\n"
"M 160,204,136,160\nC 184,184,185\nW 4\nM 164,208,136,164\nT 204,246,8192\nT 206,246,8448\nM 156,208,120,156\nC 185,186,187\nC 170,172,173\n"
"M 148,208,104,148\nC 171,174,175\nW 5\nM 144,192,108,144\nT 208,245,8704\nT 210,245,8960\nC 178,180,181\nM 152,192,124,152\nC 179,182,183\n"
"C 186,188,189\nM 160,192,140,160\nC 187,190,191\nW 6\nM 164,196,140,164\nT 192,246,8704\nT 194,246,8960\nM 156,196,124,156\nM 148,196,108,148\nW 6\n"
"M 0,200,168,0\nT 196,245,9216\nT 198,245,9472\nE 144\nM 212,240,168,212\nE 145\nE 146\nM 32,200,176,32\nE 147\nM 64,200,184,64\nC 144,144,145\nW 6\n"
"M 68,204,184,68\nT 200,246,9216\nT 202,246,9472\nM 36,204,176,36\nC 145,146,147\nM 216,240,176,216\nE 152\nM 4,204,168,4\nW 6\nM 8,208,168,8\n"
"T 204,245,9728\nT 206,245,9984\nM 40,208,176,40\nE 153\nM 72,208,184,72\nE 154\nM 220,240,184,220\nC 152,152,153\nW 6\nM 76,192,184,76\nT 208,246,9728\n"
"T 210,246,9984\nM 44,192,176,44\nE 155\nM 12,192,168,12\nC 153,154,155\nW 6\nM 16,196,168,16\nE 160\nM 48,196,176,48\nE 161\nM 80,196,184,80\n"
"R 192,244,8192\nW 5\nM 84,200,184,84\nE 162\nM 52,200,176,52\nE 163\nM 20,200,168,20\nR 196,244,8448\nW 4\nM 24,204,168,24\nE 148\nM 56,204,176,56\n"
"E 149\nM 88,204,184,88\nR 200,244,8704\nW 3\nM 92,208,184,92\nE 150\nM 60,208,176,60\nE 151\nM 28,208,168,28\nR 204,244,8960\nW 3\nM 168,192,96,228\n"
"E 156\nM 176,192,112,232\nE 157\nM 184,192,128,236\nR 208,244,9216\nW 3\nM 188,196,128,236\nE 158\nM 180,196,112,232\nE 159\nM 172,196,96,228\n"
"R 192,244,9472\nW 3\nM 168,200,100,168\nE 164\nM 176,200,116,176\nE 165\nM 184,200,132,184\nR 196,244,9728\nW 3\nM 188,204,132,188\nE 166\n"
"M 180,204,116,180\nE 167\nM 172,204,100,172\nR 200,244,9984\nW 3\nM 168,208,104,168\nC 160,160,161\nC 161,162,163\nM 176,208,120,176\nC 146,148,149\n"
"C 147,150,151\nM 184,208,136,184\nC 154,156,157\nW 0\ns_barrier\nv_add_u32 v245, 0x10000, v245\nv_add_u32 v246, 0x10000, v246\nT 204,245,0\n"
"T 206,245,256\nM 188,192,136,188\nT 208,246,0\nT 210,246,256\nM 180,192,120,180\nC 155,158,159\nC 162,164,165\nM 172,192,104,172\nC 163,166,167\n"
"M 168,196,108,168\nT 192,245,512\nT 194,245,768\nM 176,196,124,176\nM 184,196,140,184\nM 188,200,140,188\nT 196,246,512\nT 198,246,768\n"
"M 180,200,124,180\nM 172,200,108,172\nW 6\nM 0,204,144,0\nT 200,245,1024\nT 202,245,1280\nE 168\nM 212,240,144,212\nE 169\nE 170\nM 32,204,152,32\n"
"E 171\nM 64,204,160,64\nC 168,168,169\nW 6\nM 68,208,160,68\nT 204,246,1024\nT 206,246,1280\nM 36,208,152,36\nC 169,170,171\nM 216,240,152,216\nE 176\n"
"M 4,208,144,4\nW 6\nM 8,192,144,8\nT 208,245,1536\nT 210,245,1792\nM 40,192,152,40\nE 177\nM 72,192,160,72\nE 178\nM 220,240,160,220\nC 176,176,177\n"
"W 6\nM 76,196,160,76\nT 192,246,1536\nT 194,246,1792\nM 44,196,152,44\nE 179\nM 12,196,144,12\nC 177,178,179\nW 6\nM 16,200,144,16\n"
"v_add_u32 v244, 0x10000, v244\nR 196,244,0\nM 48,200,152,48\nE 184\nM 80,200,160,80\nW 5\nM 84,204,160,84\nE 185\nM 52,204,152,52\nE 186\n"
"M 20,204,144,20\nR 200,244,256\nW 4\nM 24,208,144,24\nE 187\nM 56,208,152,56\nE 172\nM 88,208,160,88\nR 204,244,512\nW 3\nM 92,192,160,92\nE 173\n"
"M 60,192,152,60\nE 174\nM 28,192,144,28\nR 208,244,768\nW 3\nM 144,196,96,228\nE 175\nM 152,196,112,232\nE 180\nM 160,196,128,236\nR 192,244,1024\nW 3\n"
"M 164,200,128,236\nE 181\nM 156,200,112,232\nE 182\nM 148,200,96,228\nR 196,244,1280\nW 3\nM 144,204,100,144\nE 183\nM 152,204,116,152\nE 188\n"
"M 160,204,132,160\nR 200,244,1536\nW 3\nM 164,208,132,164\nE 189\nM 156,208,116,156\nE 190\nM 148,208,100,148\nR 204,244,1792\nW 3\nM 144,192,104,144\n"
"T 208,245,8192\nT 210,245,8448\nM 152,192,120,152\nE 191\nM 160,192,136,160\nC 184,184,185\nW 4\nM 164,196,136,164\nT 192,246,8192\nT 194,246,8448\n"
"M 156,196,120,156\nC 185,186,187\nC 170,172,173\nM 148,196,104,148\nC 171,174,175\nW 5\nM 144,200,108,144\nT 196,245,8704\nT 198,245,8960\n"
"C 178,180,181\nM 152,200,124,152\nC 179,182,183\nC 186,188,189\nM 160,200,140,160\nC 187,190,191\nW 6\nM 164,204,140,164\nT 200,246,8704\n"
"T 202,246,8960\nM 156,204,124,156\nM 148,204,108,148\nW 6\nM 0,208,168,0\nT 204,245,9216\nT 206,245,9472\nE 144\nM 212,240,168,212\nE 145\nE 146\n"
"M 32,208,176,32\nE 147\nM 64,208,184,64\nC 144,144,145\nW 6\nM 68,192,184,68\nT 208,246,9216\nT 210,246,9472\nM 36,192,176,36\nC 145,146,147\n"
"M 216,240,176,216\nE 152\nM 4,192,168,4\nW 6\nM 8,196,168,8\nT 192,245,9728\nT 194,245,9984\nM 40,196,176,40\nE 153\nM 72,196,184,72\nE 154\n"
"M 220,240,184,220\nC 152,152,153\nW 6\nM 76,200,184,76\nT 196,246,9728\nT 198,246,9984\nM 44,200,176,44\nE 155\nM 12,200,168,12\nC 153,154,155\nW 6\n"
"M 16,204,168,16\nE 160\nM 48,204,176,48\nE 161\nM 80,204,184,80\nR 200,244,8192\nW 5\nM 84,208,184,84\nE 162\nM 52,208,176,52\nE 163\nM 20,208,168,20\n"
"R 204,244,8448\nW 4\nM 24,192,168,24\nE 148\nM 56,192,176,56\nE 149\nM 88,192,184,88\nR 208,244,8704\nW 3\nM 92,196,184,92\nE 150\nM 60,196,176,60\n"
"E 151\nM 28,196,168,28\nR 192,244,8960\nW 3\nM 168,200,96,228\nE 156\nM 176,200,112,232\nE 157\nM 184,200,128,236\nR 196,244,9216\nW 3\n"
"M 188,204,128,236\nE 158\nM 180,204,112,232\nE 159\nM 172,204,96,228\nR 200,244,9472\nW 3\nM 168,208,100,168\nE 164\nM 176,208,116,176\nE 165\n"
"M 184,208,132,184\nR 204,244,9728\nW 3\nM 188,192,132,188\nE 166\nM 180,192,116,180\nE 167\nM 172,192,100,172\nR 208,244,9984\nW 3\nM 168,196,104,168\n"
"C 160,160,161\nC 161,162,163\nM 176,196,120,176\nC 146,148,149\nC 147,150,151\nM 184,196,136,184\nC 154,156,157\nW 0\ns_barrier\n"
"v_add_u32 v245, 0xfffe8000, v245\nv_add_u32 v246, 0xfffe8000, v246\nT 192,245,0\nT 194,245,256\nM 188,200,136,188\nT 196,246,0\nT 198,246,256\n"
"C 155,158,159\nM 180,200,120,180\nC 162,164,165\nC 163,166,167\nM 172,200,104,172\nM 168,204,108,168\nT 200,245,512\nT 202,245,768\nM 176,204,124,176\n"
"M 184,204,140,184\nM 188,208,140,188\nT 204,246,512\nT 206,246,768\nM 180,208,124,180\nM 172,208,108,172\nW 6\nM 0,192,144,0\nT 208,245,1024\n"
"T 210,245,1280\nE 168\nM 212,240,144,212\nE 169\nE 170\nM 32,192,152,32\nE 171\nC 168,168,169\nM 64,192,160,64\nC 169,170,171\nE 176\nW 6\n"
"M 68,196,160,68\nT 192,246,1024\nT 194,246,1280\nE 177\nM 36,196,152,36\nC 176,176,177\nE 178\nM 216,240,152,216\nE 179\nE 184\nM 4,196,144,4\n"
"C 177,178,179\nE 185\nW 6\nM 8,200,144,8\nT 196,245,1536\nT 198,245,1792\nC 184,184,185\nM 40,200,152,40\nE 186\nE 187\nM 72,200,160,72\nE 172\n"
"C 185,186,187\nM 220,240,160,220\nE 173\nW 6\nM 76,204,160,76\nT 200,246,1536\nT 202,246,1792\nC 170,172,173\nM 44,204,152,44\nE 174\nE 175\n"
"M 12,204,144,12\nC 171,174,175\nE 180\nW 6\nM 16,208,144,16\nT 204,245,8192\nT 206,245,8448\nE 181\nM 48,208,152,48\nE 182\nC 178,180,181\n"
"M 80,208,160,80\nE 183\nW 6\nM 84,192,160,84\nT 208,246,8192\nT 210,246,8448\nC 179,182,183\nM 52,192,152,52\nE 188\nM 20,192,144,20\nE 189\nW 6\n"
"M 24,196,144,24\nT 192,245,8704\nT 194,245,8960\nC 186,188,189\nM 56,196,152,56\nE 190\nM 88,196,160,88\nW 6\nM 92,200,160,92\nT 196,246,8704\n"
"T 198,246,8960\nE 191\nM 60,200,152,60\nC 187,190,191\nM 28,200,144,28\ns_cmp_eq_u32 s57, 1\ns_cbranch_scc1 .Lc1_195_%=\nW 6\nM 0,204,168,0\n"
"T 200,245,9216\nT 202,245,9472\nM 212,240,168,212\nv_add_u32 v244, s72, v252\nv_lshrrev_b32 v151, 2, v252\nM 32,204,176,32\nv_and_b32 v151, 60, v151\n"
"v_add_u32 v150, s73, v151\nM 64,204,184,64\nv_add_u32 v151, s75, v151\nW 6\nM 68,208,184,68\nT 204,246,9216\nT 206,246,9472\nM 36,208,176,36\n"
"v_mov_b32 v144, 0\nv_mov_b32 v145, 0\nM 216,240,176,216\nv_mov_b32 v146, v224\nv_mov_b32 v147, v225\nM 4,208,168,4\nv_mov_b32 v148, 0\nW 6\n"
"M 8,192,168,8\nT 208,245,9728\nT 210,245,9984\nM 40,192,176,40\nv_mov_b32 v149, 0\nv_add_u32 v152, s76, v252\nM 72,192,184,72\nv_mov_b32 v153, 161280\n"
"M 220,240,184,220\nW 6\nM 76,196,184,76\nT 192,246,9728\nT 194,246,9984\nM 44,196,176,44\nM 12,196,168,12\nW 6\nM 16,200,168,16\n"
"ds_write_b32 v150, v212\nds_write_b32 v150, v216 offset:64\nM 48,200,176,48\nds_write_b32 v150, v220 offset:128\nv_add_u32 v150, s74, v252\n"
"M 80,200,184,80\nW 7\nM 84,204,184,84\nM 52,204,176,52\nM 20,204,168,20\nW 5\nM 24,208,168,24\nM 56,208,176,56\nM 88,208,184,88\nW 3\nM 92,192,184,92\n"
"C 16,16,17\nC 17,18,19\nC 18,20,21\nM 60,192,176,60\nC 19,22,23\nds_write_b128 v244, v[16:19]\nC 48,48,49\nM 28,192,168,28\nC 49,50,51\nC 50,52,53\n"
"C 51,54,55\nds_write_b128 v244, v[48:51] offset:2048\nC 80,80,81\nC 81,82,83\nC 82,84,85\nC 83,86,87\nds_write_b128 v244, v[80:83] offset:4096\n"
"C 24,24,25\nC 25,26,27\nC 26,28,29\nC 27,30,31\nds_write_b128 v244, v[24:27] offset:1024\nC 56,56,57\nC 57,58,59\nC 58,60,61\nC 59,62,63\n"
"ds_write_b128 v244, v[56:59] offset:3072\nC 88,88,89\nC 89,90,91\nC 90,92,93\nC 91,94,95\nds_write_b128 v244, v[88:91] offset:5120\n"
"v_mov_b32 v240, v212\nv_mov_b32 v241, v216\nv_mov_b32 v242, v220\nW 0\ns_barrier\nds_read_b32 v176, v151\nds_read_b32 v177, v151 offset:64\n"
"ds_read_b32 v178, v151 offset:128\nR 192,150,0\nR 196,150,1024\nR 200,150,2048\nR 204,150,3072\nR 208,150,4096\nR 212,150,5120\nW 8\n"
"v_add_f32 v240, v240, v176\nW 7\nv_add_f32 v241, v241, v177\nW 6\nv_add_f32 v242, v242, v178\nv_add_f32 v176, v240, v241\nv_add_f32 v176, v176, v242\n"
"v_cmp_ngt_f32 vcc, 0x53800000, v176\nv_rcp_f32 v170, v240\nv_rcp_f32 v171, v240\nv_rcp_f32 v172, v241\nv_rcp_f32 v173, v241\nv_rcp_f32 v174, v242\n"
"v_rcp_f32 v175, v242\ns_cmp_lg_u64 vcc, 0\ns_cbranch_scc0 .Lw_196_%=\nds_write_b32 v153, v153 offset:4\n.Lw_196_%=:\n"
"buffer_load_dword v160, v226, s[48:51], s66 offen sc1\nW 5\nM 0,146,192,0\nM 4,144,192,4\nW 4\nM 8,146,196,8\nM 12,144,196,12\nW 3\nM 32,146,200,32\n"
"M 36,144,200,36\ns_nop 0\nv_pk_mul_f32 v[0:1], v[0:1], v[170:171]\nW 2\nM 40,146,204,40\nv_pk_mul_f32 v[2:3], v[2:3], v[170:171]\n"
"v_pk_mul_f32 v[4:5], v[4:5], v[170:171]\nM 44,144,204,44\nv_pk_mul_f32 v[6:7], v[6:7], v[170:171]\nC 0,0,1\nC 1,2,3\nC 2,4,5\nC 3,6,7\n"
"buffer_store_dwordx4 v[0:3], v152, s[48:51], s64 offen sc1\nv_pk_mul_f32 v[8:9], v[8:9], v[170:171]\nW 1\nM 64,146,208,64\n"
"v_pk_mul_f32 v[10:11], v[10:11], v[170:171]\nv_pk_mul_f32 v[12:13], v[12:13], v[170:171]\nM 68,144,208,68\nv_pk_mul_f32 v[14:15], v[14:15], v[170:171]\n"
"C 8,8,9\nC 9,10,11\nC 10,12,13\nC 11,14,15\nbuffer_store_dwordx4 v[8:11], v152, s[48:51], s64 offen offset:1024 sc1\n"
"v_pk_mul_f32 v[32:33], v[32:33], v[172:173]\nW 0\nM 72,146,212,72\nv_pk_mul_f32 v[34:35], v[34:35], v[172:173]\n"
"v_pk_mul_f32 v[36:37], v[36:37], v[172:173]\nM 76,144,212,76\nv_pk_mul_f32 v[38:39], v[38:39], v[172:173]\nC 32,32,33\nC 33,34,35\nC 34,36,37\n"
"C 35,38,39\ns_add_u32 s53, s64, 4096\nbuffer_store_dwordx4 v[32:35], v152, s[48:51], s53 offen sc1\nv_pk_mul_f32 v[40:41], v[40:41], v[172:173]\n"
"v_pk_mul_f32 v[42:43], v[42:43], v[172:173]\nv_pk_mul_f32 v[44:45], v[44:45], v[172:173]\nv_pk_mul_f32 v[46:47], v[46:47], v[172:173]\nC 40,40,41\n"
"C 41,42,43\nC 42,44,45\nC 43,46,47\nbuffer_store_dwordx4 v[40:43], v152, s[48:51], s53 offen offset:1024 sc1\n"
"v_pk_mul_f32 v[64:65], v[64:65], v[174:175]\nv_pk_mul_f32 v[66:67], v[66:67], v[174:175]\nv_pk_mul_f32 v[68:69], v[68:69], v[174:175]\n"
"v_pk_mul_f32 v[70:71], v[70:71], v[174:175]\nC 64,64,65\nC 65,66,67\nC 66,68,69\nC 67,70,71\ns_add_u32 s53, s64, 8192\n"
"buffer_store_dwordx4 v[64:67], v152, s[48:51], s53 offen sc1\nv_pk_mul_f32 v[72:73], v[72:73], v[174:175]\nv_pk_mul_f32 v[74:75], v[74:75], v[174:175]\n"
"v_pk_mul_f32 v[76:77], v[76:77], v[174:175]\nv_pk_mul_f32 v[78:79], v[78:79], v[174:175]\nC 72,72,73\nC 73,74,75\nC 74,76,77\nC 75,78,79\n"
"buffer_store_dwordx4 v[72:75], v152, s[48:51], s53 offen offset:1024 sc1\nds_write_b128 v152, v[0:3]\nds_write_b128 v152, v[8:11] offset:1024\n"
"ds_write_b128 v152, v[32:35] offset:4096\nds_write_b128 v152, v[40:43] offset:5120\nds_write_b128 v152, v[64:67] offset:8192\n"
"ds_write_b128 v152, v[72:75] offset:9216\nv_add_u32 v154, s78, v252\nv_xor_b32 v155, 4096, v154\nv_add_u32 v156, s79, v252\nv_xor_b32 v157, 1024, v154\n"
"v_xor_b32 v158, 1024, v155\ns_branch .Lt0_9_%=\n.Lc1_195_%=:\nW 6\nM 0,204,168,0\nT 200,245,9216\nT 202,245,9472\nM 212,240,168,212\n"
"v_add_u32 v244, s72, v252\nv_lshrrev_b32 v151, 2, v252\nM 32,204,176,32\nv_and_b32 v151, 60, v151\nv_add_u32 v150, s73, v151\nM 64,204,184,64\n"
"v_add_u32 v151, s75, v151\nW 6\nM 68,208,184,68\nT 204,246,9216\nT 206,246,9472\nM 36,208,176,36\nv_mov_b32 v144, 0\nv_mov_b32 v145, 0\n"
"M 216,240,176,216\nv_mov_b32 v146, v224\nv_mov_b32 v147, v225\nM 4,208,168,4\nv_mov_b32 v148, 0\nW 6\nM 8,192,168,8\nT 208,245,9728\nT 210,245,9984\n"
"M 40,192,176,40\nv_mov_b32 v149, 0\nv_add_u32 v152, s76, v252\nM 72,192,184,72\nv_mov_b32 v153, 161280\nM 220,240,184,220\nW 6\nM 76,196,184,76\n"
"T 192,246,9728\nT 194,246,9984\nM 44,196,176,44\nC 0,0,1\nC 1,2,3\nM 12,196,168,12\nC 2,4,5\nW 6\nM 16,200,168,16\nC 3,6,7\nds_write_b128 v244, v[0:3]\n"
"M 48,200,176,48\nC 32,32,33\nC 33,34,35\nM 80,200,184,80\nC 34,36,37\nW 5\nM 84,204,184,84\nC 35,38,39\nds_write_b128 v244, v[32:35] offset:2048\n"
"C 64,64,65\nM 52,204,176,52\nC 65,66,67\nC 66,68,69\nC 67,70,71\nM 20,204,168,20\nds_write_b128 v244, v[64:67] offset:4096\nds_write_b32 v150, v212\n"
"W 6\nM 24,208,168,24\nds_write_b32 v150, v216 offset:64\nds_write_b32 v150, v220 offset:128\nv_add_u32 v150, s74, v252\nM 56,208,176,56\nC 8,8,9\n"
"C 9,10,11\nC 10,12,13\nM 88,208,184,88\nC 11,14,15\nds_write_b128 v244, v[8:11] offset:1024\nW 7\nM 92,192,184,92\nC 40,40,41\nC 41,42,43\nC 42,44,45\n"
"M 60,192,176,60\nC 43,46,47\nds_write_b128 v244, v[40:43] offset:3072\nC 72,72,73\nM 28,192,168,28\nC 73,74,75\nC 74,76,77\nC 75,78,79\n"
"ds_write_b128 v244, v[72:75] offset:5120\nv_mov_b32 v240, v212\nv_mov_b32 v241, v216\nv_mov_b32 v242, v220\nW 0\ns_barrier\nds_read_b32 v176, v151\n"
"ds_read_b32 v177, v151 offset:64\nds_read_b32 v178, v151 offset:128\nR 192,150,0\nR 196,150,1024\nR 200,150,2048\nR 204,150,3072\nR 208,150,4096\n"
"R 212,150,5120\nW 8\nv_add_f32 v240, v240, v176\nW 7\nv_add_f32 v241, v241, v177\nW 6\nv_add_f32 v242, v242, v178\nv_add_f32 v176, v240, v241\n"
"v_add_f32 v176, v176, v242\nv_cmp_ngt_f32 vcc, 0x53800000, v176\nv_rcp_f32 v170, v240\nv_rcp_f32 v171, v240\nv_rcp_f32 v172, v241\n"
"v_rcp_f32 v173, v241\nv_rcp_f32 v174, v242\nv_rcp_f32 v175, v242\ns_cmp_lg_u64 vcc, 0\ns_cbranch_scc0 .Lw_197_%=\nds_write_b32 v153, v153 offset:4\n"
".Lw_197_%=:\nbuffer_load_dword v160, v226, s[48:51], s66 offen sc1\nW 5\nM 16,146,192,16\nM 20,144,192,20\nW 4\nM 24,146,196,24\nM 28,144,196,28\nW 3\n"
"M 48,146,200,48\nM 52,144,200,52\ns_nop 0\nv_pk_mul_f32 v[16:17], v[16:17], v[170:171]\nW 2\nM 56,146,204,56\n"
"v_pk_mul_f32 v[18:19], v[18:19], v[170:171]\nv_pk_mul_f32 v[20:21], v[20:21], v[170:171]\nM 60,144,204,60\nv_pk_mul_f32 v[22:23], v[22:23], v[170:171]\n"
"C 16,16,17\nC 17,18,19\nC 18,20,21\nC 19,22,23\nbuffer_store_dwordx4 v[16:19], v152, s[48:51], s64 offen sc1\n"
"v_pk_mul_f32 v[24:25], v[24:25], v[170:171]\nW 1\nM 80,146,208,80\nv_pk_mul_f32 v[26:27], v[26:27], v[170:171]\n"
"v_pk_mul_f32 v[28:29], v[28:29], v[170:171]\nM 84,144,208,84\nv_pk_mul_f32 v[30:31], v[30:31], v[170:171]\nC 24,24,25\nC 25,26,27\nC 26,28,29\n"
"C 27,30,31\nbuffer_store_dwordx4 v[24:27], v152, s[48:51], s64 offen offset:1024 sc1\nv_pk_mul_f32 v[48:49], v[48:49], v[172:173]\nW 0\n"
"M 88,146,212,88\nv_pk_mul_f32 v[50:51], v[50:51], v[172:173]\nv_pk_mul_f32 v[52:53], v[52:53], v[172:173]\nM 92,144,212,92\n"
"v_pk_mul_f32 v[54:55], v[54:55], v[172:173]\nC 48,48,49\nC 49,50,51\nC 50,52,53\nC 51,54,55\ns_add_u32 s53, s64, 4096\n"
"buffer_store_dwordx4 v[48:51], v152, s[48:51], s53 offen sc1\nv_pk_mul_f32 v[56:57], v[56:57], v[172:173]\nv_pk_mul_f32 v[58:59], v[58:59], v[172:173]\n"
"v_pk_mul_f32 v[60:61], v[60:61], v[172:173]\nv_pk_mul_f32 v[62:63], v[62:63], v[172:173]\nC 56,56,57\nC 57,58,59\nC 58,60,61\nC 59,62,63\n"
"buffer_store_dwordx4 v[56:59], v152, s[48:51], s53 offen offset:1024 sc1\nv_pk_mul_f32 v[80:81], v[80:81], v[174:175]\n"
"v_pk_mul_f32 v[82:83], v[82:83], v[174:175]\nv_pk_mul_f32 v[84:85], v[84:85], v[174:175]\nv_pk_mul_f32 v[86:87], v[86:87], v[174:175]\nC 80,80,81\n"
"C 81,82,83\nC 82,84,85\nC 83,86,87\ns_add_u32 s53, s64, 8192\nbuffer_store_dwordx4 v[80:83], v152, s[48:51], s53 offen sc1\n"
"v_pk_mul_f32 v[88:89], v[88:89], v[174:175]\nv_pk_mul_f32 v[90:91], v[90:91], v[174:175]\nv_pk_mul_f32 v[92:93], v[92:93], v[174:175]\n"
"v_pk_mul_f32 v[94:95], v[94:95], v[174:175]\nC 88,88,89\nC 89,90,91\nC 90,92,93\nC 91,94,95\n"
"buffer_store_dwordx4 v[88:91], v152, s[48:51], s53 offen offset:1024 sc1\nds_write_b128 v152, v[16:19]\nds_write_b128 v152, v[24:27] offset:1024\n"
"ds_write_b128 v152, v[48:51] offset:4096\nds_write_b128 v152, v[56:59] offset:5120\nds_write_b128 v152, v[80:83] offset:8192\n"
"ds_write_b128 v152, v[88:91] offset:9216\nv_add_u32 v154, s78, v252\nv_xor_b32 v155, 4096, v154\nv_add_u32 v156, s79, v252\nv_xor_b32 v157, 1024, v154\n"
"v_xor_b32 v158, 1024, v155\ns_branch .Lt0_9_%=\n.Lv1_6_%=:\nR 192,244,8192\nR 196,244,8448\nR 200,244,8704\nR 204,244,8960\ns_cmp_eq_u32 s35, 0\n"
"s_cbranch_scc0 .Lw_198_%=\nv_max3_f32 v180, v168, v169, v170\nv_max_f32 v180, v180, v171\nv_max3_f32 v181, v172, v173, v174\n"
"v_max_f32 v181, v181, v175\nv_max3_f32 v182, v176, v177, v178\nv_max_f32 v182, v182, v179\nv_mov_b32 v183, v180\nv_mov_b32 v184, v181\n"
"v_mov_b32 v185, v182\nv_permlane16_swap_b32 v180, v183\nv_permlane16_swap_b32 v181, v184\nv_permlane16_swap_b32 v182, v185\nv_max_f32 v180, v180, v183\n"
"v_max_f32 v181, v181, v184\nv_max_f32 v182, v182, v185\nv_mov_b32 v183, v180\nv_mov_b32 v184, v181\nv_mov_b32 v185, v182\n"
"v_permlane32_swap_b32 v180, v183\nv_permlane32_swap_b32 v181, v184\nv_permlane32_swap_b32 v182, v185\nv_max_f32 v180, v180, v183\n"
"v_max_f32 v181, v181, v184\nv_max_f32 v182, v182, v185\nv_sub_f32 v228, 0, v180\nv_sub_f32 v229, 0, v180\nv_sub_f32 v230, 0, v180\n"
"v_sub_f32 v231, 0, v180\nv_sub_f32 v232, 0, v181\nv_sub_f32 v233, 0, v181\nv_sub_f32 v234, 0, v181\nv_sub_f32 v235, 0, v181\nv_sub_f32 v236, 0, v182\n"
"v_sub_f32 v237, 0, v182\nv_sub_f32 v238, 0, v182\nv_sub_f32 v239, 0, v182\n.Lw_198_%=:\nW 3\nM 144,192,96,228\nM 152,192,112,232\nM 160,192,128,236\n"
"R 208,244,9216\ns_bitcmp1_b32 s21, 0\ns_cbranch_scc0 .Lw_199_%=\nds_write_b128 v251, v[0:3]\nds_write_b128 v251, v[4:7] offset:1024\n"
"ds_write_b128 v251, v[8:11] offset:2048\n.Lw_199_%=:\nW 3\nM 164,196,128,236\nM 156,196,112,232\nM 148,196,96,228\nR 192,244,9472\n"
"s_bitcmp1_b32 s21, 0\ns_cbranch_scc0 .Lw_200_%=\nds_write_b128 v251, v[12:15] offset:3072\nds_write_b128 v251, v[16:19] offset:4096\n"
"ds_write_b128 v251, v[20:23] offset:5120\n.Lw_200_%=:\nW 3\nM 144,200,100,144\nM 152,200,116,152\nM 160,200,132,160\nR 196,244,9728\n"
"s_bitcmp1_b32 s21, 0\ns_cbranch_scc0 .Lw_201_%=\nds_write_b128 v251, v[24:27] offset:6144\nds_write_b128 v251, v[28:31] offset:7168\n"
"ds_write_b128 v251, v[32:35] offset:32768\n.Lw_201_%=:\nW 3\nM 164,204,132,164\nM 156,204,116,156\nM 148,204,100,148\nR 200,244,9984\n"
"s_bitcmp1_b32 s21, 0\ns_cbranch_scc0 .Lw_202_%=\nds_write_b128 v251, v[36:39] offset:33792\nds_write_b128 v251, v[40:43] offset:34816\n"
"ds_write_b128 v251, v[44:47] offset:35840\n.Lw_202_%=:\nW 3\nM 144,208,104,144\nM 152,208,120,152\nM 160,208,136,160\nR 204,244,16384\n"
"s_bitcmp1_b32 s21, 0\ns_cbranch_scc0 .Lw_203_%=\nds_write_b128 v251, v[48:51] offset:36864\nds_write_b128 v251, v[52:55] offset:37888\n"
"ds_write_b128 v251, v[56:59] offset:38912\n.Lw_203_%=:\nW 3\nM 164,192,136,164\nM 156,192,120,156\nM 148,192,104,148\nR 208,244,16640\n"
"s_bitcmp1_b32 s21, 0\ns_cbranch_scc0 .Lw_204_%=\nds_write_b128 v251, v[60:63] offset:39936\n.Lw_204_%=:\nW 3\nM 144,196,108,144\nM 152,196,124,152\n"
"M 160,196,140,160\nR 192,244,16896\nW 3\nM 164,200,140,164\nM 156,200,124,156\nM 148,200,108,148\nR 196,244,17152\nW 3\nM 168,204,96,228\nE 144\nE 145\n"
"M 176,204,112,232\nE 146\nC 144,144,145\nM 184,204,128,236\nR 200,244,17408\nE 147\nW 3\nM 188,208,128,236\nE 152\nC 145,146,147\nM 180,208,112,232\n"
"E 153\nE 154\nM 172,208,96,228\nR 204,244,17664\nC 152,152,153\nW 3\nM 168,192,100,168\nE 155\nE 160\nM 176,192,116,176\nE 161\nC 153,154,155\n"
"M 184,192,132,184\nR 208,244,17920\nC 160,160,161\nW 3\nM 188,196,132,188\nE 162\nE 163\nM 180,196,116,180\nE 148\nC 161,162,163\nM 172,196,100,172\n"
"R 192,244,18176\nE 149\nW 3\nM 168,200,104,168\nE 150\nC 146,148,149\nM 176,200,120,176\nE 151\nE 156\nM 184,200,136,184\nC 147,150,151\nE 157\nW 0\n"
"s_barrier\nT 196,245,8192\nT 198,245,8448\nM 188,204,136,188\nT 200,246,8192\nT 202,246,8448\nC 154,156,157\nM 180,204,120,180\nE 158\nE 159\n"
"M 172,204,104,172\nC 155,158,159\nE 164\nM 168,208,108,168\nT 204,245,8704\nT 206,245,8960\nE 165\nM 176,208,124,176\nE 166\nC 162,164,165\n"
"M 184,208,140,184\nE 167\nM 188,192,140,188\nT 208,246,8704\nT 210,246,8960\nC 163,166,167\nM 180,192,124,180\nM 172,192,108,172\nW 6\n"
"v_mfma_f32_16x16x32_bf16 v[0:3], v[196:199], v[144:147], 0\nT 192,245,9216\nT 194,245,9472\nE 168\n"
"v_mfma_f32_16x16x32_bf16 v[212:215], v[240:243], v[144:147], 0\nE 169\nE 170\nv_mfma_f32_16x16x32_bf16 v[32:35], v[196:199], v[152:155], 0\nE 171\n"
"v_mfma_f32_16x16x32_bf16 v[64:67], v[196:199], v[160:163], 0\nC 168,168,169\nW 6\nv_mfma_f32_16x16x32_bf16 v[68:71], v[200:203], v[160:163], 0\n"
"T 196,246,9216\nT 198,246,9472\nv_mfma_f32_16x16x32_bf16 v[36:39], v[200:203], v[152:155], 0\nC 169,170,171\n"
"v_mfma_f32_16x16x32_bf16 v[216:219], v[240:243], v[152:155], 0\nE 176\nv_mfma_f32_16x16x32_bf16 v[4:7], v[200:203], v[144:147], 0\nW 6\n"
"v_mfma_f32_16x16x32_bf16 v[8:11], v[204:207], v[144:147], 0\nT 200,245,9728\nT 202,245,9984\n"
"v_mfma_f32_16x16x32_bf16 v[40:43], v[204:207], v[152:155], 0\nE 177\nv_mfma_f32_16x16x32_bf16 v[72:75], v[204:207], v[160:163], 0\nE 178\n"
"v_mfma_f32_16x16x32_bf16 v[220:223], v[240:243], v[160:163], 0\nC 176,176,177\nW 6\nv_mfma_f32_16x16x32_bf16 v[76:79], v[208:211], v[160:163], 0\n"
"T 204,246,9728\nT 206,246,9984\nv_mfma_f32_16x16x32_bf16 v[44:47], v[208:211], v[152:155], 0\nE 179\n"
"v_mfma_f32_16x16x32_bf16 v[12:15], v[208:211], v[144:147], 0\nC 177,178,179\nW 6\nv_mfma_f32_16x16x32_bf16 v[16:19], v[192:195], v[144:147], 0\n"
"v_add_u32 v244, 0x10000, v244\nR 208,244,8192\nv_mfma_f32_16x16x32_bf16 v[48:51], v[192:195], v[152:155], 0\nE 184\n"
"v_mfma_f32_16x16x32_bf16 v[80:83], v[192:195], v[160:163], 0\nW 5\nv_mfma_f32_16x16x32_bf16 v[84:87], v[196:199], v[160:163], 0\nE 185\n"
"v_mfma_f32_16x16x32_bf16 v[52:55], v[196:199], v[152:155], 0\nE 186\nv_mfma_f32_16x16x32_bf16 v[20:23], v[196:199], v[144:147], 0\nR 192,244,8448\nW 4\n"
"v_mfma_f32_16x16x32_bf16 v[24:27], v[200:203], v[144:147], 0\nE 187\nv_mfma_f32_16x16x32_bf16 v[56:59], v[200:203], v[152:155], 0\nE 172\n"
"v_mfma_f32_16x16x32_bf16 v[88:91], v[200:203], v[160:163], 0\nR 196,244,8704\nW 3\nv_mfma_f32_16x16x32_bf16 v[92:95], v[204:207], v[160:163], 0\nE 173\n"
"v_mfma_f32_16x16x32_bf16 v[60:63], v[204:207], v[152:155], 0\nE 174\nv_mfma_f32_16x16x32_bf16 v[28:31], v[204:207], v[144:147], 0\nR 200,244,8960\nW 3\n"
"M 144,208,96,228\nE 175\nM 152,208,112,232\nE 180\nM 160,208,128,236\nR 204,244,9216\nW 3\nM 164,192,128,236\nE 181\nM 156,192,112,232\nE 182\n"
"M 148,192,96,228\nR 208,244,9472\nW 3\nM 144,196,100,144\nE 183\nM 152,196,116,152\nE 188\nM 160,196,132,160\nR 192,244,9728\nW 3\nM 164,200,132,164\n"
"E 189\nM 156,200,116,156\nE 190\nM 148,200,100,148\nR 196,244,9984\nW 3\nM 144,204,104,144\nT 200,245,16384\nT 202,245,16640\nM 152,204,120,152\nE 191\n"
"M 160,204,136,160\nC 184,184,185\nW 4\nM 164,208,136,164\nT 204,246,16384\nT 206,246,16640\nM 156,208,120,156\nC 185,186,187\nC 170,172,173\n"
"M 148,208,104,148\nC 171,174,175\nW 5\nM 144,192,108,144\nT 208,245,16896\nT 210,245,17152\nC 178,180,181\nM 152,192,124,152\nC 179,182,183\n"
"C 186,188,189\nM 160,192,140,160\nC 187,190,191\nW 6\nM 164,196,140,164\nT 192,246,16896\nT 194,246,17152\nM 156,196,124,156\nM 148,196,108,148\nW 6\n"
"M 0,200,168,0\nT 196,245,17408\nT 198,245,17664\nE 144\nM 212,240,168,212\nE 145\nE 146\nM 32,200,176,32\nE 147\nM 64,200,184,64\nC 144,144,145\nW 6\n"
"M 68,204,184,68\nT 200,246,17408\nT 202,246,17664\nM 36,204,176,36\nC 145,146,147\nM 216,240,176,216\nE 152\nM 4,204,168,4\nW 6\nM 8,208,168,8\n"
"T 204,245,17920\nT 206,245,18176\nM 40,208,176,40\nE 153\nM 72,208,184,72\nE 154\nM 220,240,184,220\nC 152,152,153\nW 6\nM 76,192,184,76\n"
"T 208,246,17920\nT 210,246,18176\nM 44,192,176,44\nE 155\nM 12,192,168,12\nC 153,154,155\nW 6\nM 16,196,168,16\nE 160\nM 48,196,176,48\nE 161\n"
"M 80,196,184,80\nC 160,160,161\nW 0\ns_barrier\nv_add_u32 v244, 0x10000, v244\nR 192,244,8192\nM 84,200,184,84\nE 162\nM 52,200,176,52\nE 163\n"
"M 20,200,168,20\nR 196,244,8448\nM 24,204,168,24\nE 148\nM 56,204,176,56\nE 149\nM 88,204,184,88\nR 200,244,8704\nM 92,208,184,92\nE 150\n"
"M 60,208,176,60\nE 151\nM 28,208,168,28\nR 204,244,8960\nW 3\nM 168,192,96,228\nE 156\nM 176,192,112,232\nE 157\nM 184,192,128,236\nR 208,244,9216\n"
"W 3\nM 188,196,128,236\nE 158\nM 180,196,112,232\nE 159\nM 172,196,96,228\nR 192,244,9472\nW 3\nM 168,200,100,168\nE 164\nM 176,200,116,176\nE 165\n"
"M 184,200,132,184\nR 196,244,9728\nW 3\nM 188,204,132,188\nE 166\nM 180,204,116,180\nE 167\nM 172,204,100,172\nR 200,244,9984\nW 3\nM 168,208,104,168\n"
"v_add_u32 v245, 0x10000, v245\nv_add_u32 v246, 0x10000, v246\nM 176,208,120,176\nT 204,245,8192\nT 206,245,8448\nM 184,208,136,184\nC 161,162,163\nW 4\n"
"M 188,192,136,188\nT 208,246,8192\nT 210,246,8448\nM 180,192,120,180\nC 146,148,149\nC 147,150,151\nM 172,192,104,172\nC 154,156,157\nW 5\n"
"M 168,196,108,168\nT 192,245,8704\nT 194,245,8960\nM 176,196,124,176\nC 155,158,159\nC 162,164,165\nM 184,196,140,184\nC 163,166,167\nW 6\n"
"M 188,200,140,188\nT 196,246,8704\nT 198,246,8960\nM 180,200,124,180\nM 172,200,108,172\nW 6\nM 0,204,144,0\nT 200,245,9216\nT 202,245,9472\nE 168\n"
"M 212,240,144,212\nE 169\nE 170\nM 32,204,152,32\nE 171\nM 64,204,160,64\nC 168,168,169\nW 6\nM 68,208,160,68\nT 204,246,9216\nT 206,246,9472\n"
"M 36,208,152,36\nC 169,170,171\nM 216,240,152,216\nE 176\nM 4,208,144,4\nW 6\nM 8,192,144,8\nT 208,245,9728\nT 210,245,9984\nM 40,192,152,40\nE 177\n"
"M 72,192,160,72\nE 178\nM 220,240,160,220\nC 176,176,177\nW 6\nM 76,196,160,76\nT 192,246,9728\nT 194,246,9984\nM 44,196,152,44\nE 179\n"
"M 12,196,144,12\nC 177,178,179\nW 6\nM 16,200,144,16\nE 184\nM 48,200,152,48\nE 185\nM 80,200,160,80\nR 196,244,16384\nW 5\nM 84,204,160,84\nE 186\n"
"M 52,204,152,52\nE 187\nM 20,204,144,20\nR 200,244,16640\nW 4\nM 24,208,144,24\nE 172\nM 56,208,152,56\nE 173\nM 88,208,160,88\nR 204,244,16896\nW 3\n"
"M 92,192,160,92\nE 174\nM 60,192,152,60\nE 175\nM 28,192,144,28\nR 208,244,17152\nW 3\nM 144,196,96,228\nE 180\nM 152,196,112,232\nE 181\n"
"M 160,196,128,236\nR 192,244,17408\nW 3\nM 164,200,128,236\nE 182\nM 156,200,112,232\nE 183\nM 148,200,96,228\nR 196,244,17664\nW 3\nM 144,204,100,144\n"
"E 188\nM 152,204,116,152\nE 189\nM 160,204,132,160\nR 200,244,17920\nW 3\nM 164,208,132,164\nE 190\nM 156,208,116,156\nE 191\nM 148,208,100,148\n"
"R 204,244,18176\nW 3\nM 144,192,104,144\nC 184,184,185\nC 185,186,187\nM 152,192,120,152\nC 170,172,173\nC 171,174,175\nM 160,192,136,160\n"
"C 178,180,181\nW 0\ns_barrier\nv_add_u32 v245, 0xfffe8000, v245\nv_add_u32 v246, 0xfffe8000, v246\nT 208,245,8192\nT 210,245,8448\nM 164,196,136,164\n"
"T 192,246,8192\nT 194,246,8448\nM 156,196,120,156\nC 179,182,183\nC 186,188,189\nM 148,196,104,148\nC 187,190,191\nM 144,200,108,144\nT 196,245,8704\n"
"T 198,245,8960\nM 152,200,124,152\nM 160,200,140,160\nM 164,204,140,164\nT 200,246,8704\nT 202,246,8960\nM 156,204,124,156\nM 148,204,108,148\nW 6\n"
"M 0,208,168,0\nT 204,245,9216\nT 206,245,9472\nE 144\nM 212,240,168,212\nE 145\nE 146\nM 32,208,176,32\nE 147\nM 64,208,184,64\nC 144,144,145\nW 6\n"
"M 68,192,184,68\nT 208,246,9216\nT 210,246,9472\nM 36,192,176,36\nC 145,146,147\nM 216,240,176,216\nE 152\nM 4,192,168,4\nW 6\nM 8,196,168,8\n"
"T 192,245,9728\nT 194,245,9984\nM 40,196,176,40\nE 153\nM 72,196,184,72\nE 154\nM 220,240,184,220\nC 152,152,153\nW 6\nM 76,200,184,76\nT 196,246,9728\n"
"T 198,246,9984\nM 44,200,176,44\nE 155\nM 12,200,168,12\nC 153,154,155\nW 6\nM 16,204,168,16\nv_add_u32 v244, 0xfffe8000, v244\nR 200,244,8192\n"
"M 48,204,176,48\nE 160\nM 80,204,184,80\nW 5\nM 84,208,184,84\nE 161\nM 52,208,176,52\nE 162\nM 20,208,168,20\nR 204,244,8448\nW 4\nM 24,192,168,24\n"
"E 163\nM 56,192,176,56\nE 148\nM 88,192,184,88\nR 208,244,8704\nW 3\nM 92,196,184,92\nE 149\nM 60,196,176,60\nE 150\nM 28,196,168,28\nR 192,244,8960\n"
"W 3\nM 168,200,96,228\nE 151\nM 176,200,112,232\nE 156\nM 184,200,128,236\nR 196,244,9216\nW 3\nM 188,204,128,236\nE 157\nM 180,204,112,232\nE 158\n"
"M 172,204,96,228\nR 200,244,9472\nW 3\nM 168,208,100,168\nE 159\nM 176,208,116,176\nE 164\nM 184,208,132,184\nR 204,244,9728\nW 3\nM 188,192,132,188\n"
"E 165\nM 180,192,116,180\nE 166\nM 172,192,100,172\nR 208,244,9984\nW 3\nM 168,196,104,168\nT 192,245,16384\nT 194,245,16640\nM 176,196,120,176\nE 167\n"
"M 184,196,136,184\nC 160,160,161\nW 4\nM 188,200,136,188\nT 196,246,16384\nT 198,246,16640\nM 180,200,120,180\nC 161,162,163\nC 146,148,149\n"
"M 172,200,104,172\nC 147,150,151\nW 5\nM 168,204,108,168\nT 200,245,16896\nT 202,245,17152\nC 154,156,157\nM 176,204,124,176\nC 155,158,159\n"
"C 162,164,165\nM 184,204,140,184\nC 163,166,167\nW 6\nM 188,208,140,188\nT 204,246,16896\nT 206,246,17152\nM 180,208,124,180\nM 172,208,108,172\nW 6\n"
"M 0,192,144,0\nT 208,245,17408\nT 210,245,17664\nE 168\nM 212,240,144,212\nE 169\nE 170\nM 32,192,152,32\nE 171\nM 64,192,160,64\nC 168,168,169\nW 6\n"
"M 68,196,160,68\nT 192,246,17408\nT 194,246,17664\nM 36,196,152,36\nC 169,170,171\nM 216,240,152,216\nE 176\nM 4,196,144,4\nW 6\nM 8,200,144,8\n"
"T 196,245,17920\nT 198,245,18176\nM 40,200,152,40\nE 177\nM 72,200,160,72\nE 178\nM 220,240,160,220\nC 176,176,177\nW 6\nM 76,204,160,76\n"
"T 200,246,17920\nT 202,246,18176\nM 44,204,152,44\nE 179\nM 12,204,144,12\nC 177,178,179\nW 6\nM 16,208,144,16\nE 184\nM 48,208,152,48\nE 185\n"
"M 80,208,160,80\nC 184,184,185\ns_and_b32 s71, s21, 3\ns_lshl_b32 s71, s71, 1\ns_mul_i32 s28, s58, 160\ns_mul_i32 s27, s59, 1310720\n"
"s_add_u32 s27, s27, s28\ns_add_u32 s44, s12, s27\ns_addc_u32 s45, s13, 0\ns_and_b32 s45, s45, 65535\ns_mov_b32 s46, 0\ns_mov_b32 s47, 131072\n"
"s_sub_u32 s46, 1310720, s28\ns_mul_i32 s31, s20, 5120\ns_mul_i32 s27, s21, 184320\ns_add_u32 s27, s27, s28\ns_add_u32 s40, s14, s27\n"
"s_addc_u32 s41, s15, 0\ns_and_b32 s41, s41, 65535\ns_mov_b32 s42, 0\ns_mov_b32 s43, 131072\ns_sub_u32 s42, 184320, s28\ns_add_u32 s48, s16, 0\n"
"s_addc_u32 s49, s17, 0\ns_and_b32 s49, s49, 65535\ns_mov_b32 s50, 67108864\ns_mov_b32 s51, 131072\ns_mul_i32 s64, s56, 36864\n"
"s_and_b32 s26, s56, 4294967288\ns_mul_i32 s65, s26, 36864\ns_lshl_b32 s66, s26, 8\ns_add_u32 s66, s66, 33554432\ns_lshl_b32 s26, s21, 3\n"
"s_add_u32 s26, s26, s58\ns_mul_i32 s67, s26, 196608\ns_add_u32 s67, s67, 16777216\ns_mov_b32 s72, 98304\ns_mul_i32 s26, s57, 24576\n"
"s_add_u32 s72, s72, s26\ns_mov_b32 s75, 159744\ns_mul_i32 s26, s57, 192\ns_add_u32 s75, s75, s26\ns_mov_b32 s76, 0\ns_mul_i32 s26, s57, 12288\n"
"s_add_u32 s76, s76, s26\ns_mov_b32 s73, 159744\ns_mul_i32 s26, s19, 192\ns_add_u32 s73, s73, s26\ns_add_u32 s26, s71, 0\ns_cmp_ge_u32 s26, 7\n"
"s_cselect_b32 s27, -6, 1\ns_add_u32 s26, s26, s27\ns_add_u32 s26, s26, s58\ns_and_b32 s26, s26, 7\ns_mul_i32 s5, s26, 36864\ns_add_u32 s5, s5, s65\n"
"s_add_u32 s26, s71, 1\ns_cmp_ge_u32 s26, 7\ns_cselect_b32 s27, -6, 1\ns_add_u32 s26, s26, s27\ns_add_u32 s26, s26, s58\ns_and_b32 s26, s26, 7\n"
"s_mul_i32 s6, s26, 36864\ns_add_u32 s6, s6, s65\ns_add_u32 s26, s71, 2\ns_cmp_ge_u32 s26, 7\ns_cselect_b32 s27, -6, 1\ns_add_u32 s26, s26, s27\n"
"s_add_u32 s26, s26, s58\ns_and_b32 s26, s26, 7\ns_mul_i32 s7, s26, 36864\ns_add_u32 s7, s7, s65\ns_add_u32 s26, s71, 3\ns_cmp_ge_u32 s26, 7\n"
"s_cselect_b32 s27, -6, 1\ns_add_u32 s26, s26, s27\ns_add_u32 s26, s26, s58\ns_and_b32 s26, s26, 7\ns_mul_i32 s8, s26, 36864\ns_add_u32 s8, s8, s65\n"
"s_add_u32 s26, s71, 4\ns_cmp_ge_u32 s26, 7\ns_cselect_b32 s27, -6, 1\ns_add_u32 s26, s26, s27\ns_add_u32 s26, s26, s58\ns_and_b32 s26, s26, 7\n"
"s_mul_i32 s9, s26, 36864\ns_add_u32 s9, s9, s65\ns_add_u32 s26, s71, 5\ns_cmp_ge_u32 s26, 7\ns_cselect_b32 s27, -6, 1\ns_add_u32 s26, s26, s27\n"
"s_add_u32 s26, s26, s58\ns_and_b32 s26, s26, 7\ns_mul_i32 s10, s26, 36864\ns_add_u32 s10, s10, s65\ns_add_u32 s26, s71, 6\ns_cmp_ge_u32 s26, 7\n"
"s_cselect_b32 s27, -6, 1\ns_add_u32 s26, s26, s27\ns_add_u32 s26, s26, s58\ns_and_b32 s26, s26, 7\ns_mul_i32 s11, s26, 36864\ns_add_u32 s11, s11, s65\n"
"s_mov_b32 s4, 0\ns_mov_b32 s77, 0\ns_mov_b32 s78, 9216\ns_mul_i32 s26, s57, 6144\ns_add_u32 s78, s78, s26\ns_mov_b32 s79, 33792\n"
"s_mul_i32 s26, s57, 2048\ns_add_u32 s79, s79, s26\ns_mov_b32 s80, 5120\ns_mul_i32 s26, s57, 10240\ns_add_u32 s80, s80, s26\ns_mul_i32 s28, s59, 12288\n"
"s_lshl_b32 s56, s19, 10\ns_lshl_b32 s29, s28, 2\ns_add_u32 s29, s29, s67\ns_add_u32 s28, s28, s67\ns_xor_b32 s26, s59, 1\ns_mul_i32 s26, s26, 12288\n"
"s_add_u32 s23, s26, s29\ns_lshl_b32 s26, s26, 2\ns_add_u32 s26, s26, s28\ns_add_u32 s12, s26, s56\ns_xor_b32 s26, s59, 2\ns_mul_i32 s26, s26, 12288\n"
"s_add_u32 s24, s26, s29\ns_lshl_b32 s26, s26, 2\ns_add_u32 s26, s26, s28\ns_add_u32 s13, s26, s56\ns_xor_b32 s26, s59, 3\ns_mul_i32 s26, s26, 12288\n"
"s_add_u32 s54, s26, s29\ns_lshl_b32 s26, s26, 2\ns_add_u32 s26, s26, s28\ns_add_u32 s14, s26, s56\ns_lshl_b32 s26, s57, 10\ns_add_u32 s26, s26, 9216\n"
"s_mul_i32 s27, s25, 49152\ns_add_u32 s27, s27, s28\ns_add_u32 s15, s26, s27\ns_cmp_eq_u32 s59, s25\ns_cselect_b32 s69, s26, 268435456\n"
"s_cselect_b32 s99, s34, 0\ns_add_u32 s22, s28, 11264\ns_or_b32 s26, s59, s19\ns_cselect_b32 s98, 268435456, 0\ns_cselect_b32 s67, 0, s34\n"
"s_add_u32 s98, s98, 11264\ns_lshl_b32 s26, s59, 1\nv_add_u32 v254, 0, v255\nv_mul_u32_u24 v254, 205, v254\nv_lshrrev_b32 v252, 11, v254\n"
"v_add_u32 v247, 0, v255\nv_mul_u32_u24 v254, 10, v252\nv_sub_u32 v253, v247, v254\nv_xor_b32 v254, s26, v253\nv_cmp_gt_u32 vcc, 8, v253\ns_nop 1\n"
"v_cndmask_b32 v253, v253, v254, vcc\nv_mul_u32_u24 v247, 1280, v252\nv_lshlrev_b32 v254, 4, v253\nv_add_u32 v247, v247, v254\n"
"v_subrev_u32 v247, 0, v247\nv_add_u32 v254, 64, v255\nv_mul_u32_u24 v254, 205, v254\nv_lshrrev_b32 v252, 11, v254\nv_add_u32 v248, 64, v255\n"
"v_mul_u32_u24 v254, 10, v252\nv_sub_u32 v253, v248, v254\nv_xor_b32 v254, s26, v253\nv_cmp_gt_u32 vcc, 8, v253\ns_nop 1\n"
"v_cndmask_b32 v253, v253, v254, vcc\nv_mul_u32_u24 v248, 1280, v252\nv_lshlrev_b32 v254, 4, v253\nv_add_u32 v248, v248, v254\n"
"v_subrev_u32 v248, 1024, v248\nv_add_u32 v254, 128, v255\nv_mul_u32_u24 v254, 205, v254\nv_lshrrev_b32 v252, 11, v254\nv_add_u32 v249, 128, v255\n"
"v_mul_u32_u24 v254, 10, v252\nv_sub_u32 v253, v249, v254\nv_xor_b32 v254, s26, v253\nv_cmp_gt_u32 vcc, 8, v253\ns_nop 1\n"
"v_cndmask_b32 v253, v253, v254, vcc\nv_mul_u32_u24 v249, 1280, v252\nv_lshlrev_b32 v254, 4, v253\nv_add_u32 v249, v249, v254\n"
"v_subrev_u32 v249, 2048, v249\nv_add_u32 v254, 192, v255\nv_mul_u32_u24 v254, 205, v254\nv_lshrrev_b32 v252, 11, v254\nv_add_u32 v250, 192, v255\n"
"v_mul_u32_u24 v254, 10, v252\nv_sub_u32 v253, v250, v254\nv_xor_b32 v254, s26, v253\nv_cmp_gt_u32 vcc, 8, v253\ns_nop 1\n"
"v_cndmask_b32 v253, v253, v254, vcc\nv_mul_u32_u24 v250, 1280, v252\nv_lshlrev_b32 v254, 4, v253\nv_add_u32 v250, v250, v254\n"
"v_subrev_u32 v250, 3072, v250\nv_add_u32 v254, 256, v255\nv_mul_u32_u24 v254, 205, v254\nv_lshrrev_b32 v252, 11, v254\nv_add_u32 v251, 256, v255\n"
"v_mul_u32_u24 v254, 10, v252\nv_sub_u32 v253, v251, v254\nv_xor_b32 v254, s26, v253\nv_cmp_gt_u32 vcc, 8, v253\ns_nop 1\n"
"v_cndmask_b32 v253, v253, v254, vcc\nv_mul_u32_u24 v251, 1280, v252\nv_lshlrev_b32 v254, 4, v253\nv_add_u32 v251, v251, v254\n"
"v_subrev_u32 v251, 0, v251\nv_lshrrev_b32 v254, 4, v255\nv_lshrrev_b32 v253, 2, v255\nv_xor_b32 v254, v254, v253\nv_and_b32 v224, 3, v254\n"
"v_cmp_eq_u32 vcc, 0, v224\nv_and_b32 v254, 1, v255\nv_lshlrev_b32 v254, 4, v254\nv_mov_b32 v253, 16256\nv_lshlrev_b32 v225, v254, v253\n"
"v_cndmask_b32 v225, 0, v225, vcc\nv_and_b32 v224, 2, v255\nv_cmp_eq_u32 vcc, 0, v224\ns_nop 1\nv_cndmask_b32 v224, 0, v225, vcc\n"
"v_xor_b32 v225, v225, v224\nv_lshrrev_b32 v254, 2, v255\nv_mul_u32_u24 v254, 160, v254\nv_and_b32 v252, 3, v255\nv_lshlrev_b32 v252, 3, v252\n"
"v_add_u32 v254, v254, v252\nv_add_u32 v254, s80, v254\nv_add_u32 v253, 36864, v254\nv_add_u32 v227, -5120, v253\nv_and_b32 v254, 15, v255\n"
"v_mul_u32_u24 v254, 1280, v254\nv_lshrrev_b32 v252, 4, v255\nv_lshl_add_u32 v254, v252, 3, v254\nv_lshlrev_b32 v252, 4, v255\nW 0\ns_barrier\n"
"v_add_u32 v244, 0x10000, v244\nR 204,244,8192\nM 84,192,160,84\nE 186\nM 52,192,152,52\nE 187\nM 20,192,144,20\nR 208,244,8448\nM 24,196,144,24\nE 172\n"
"M 56,196,152,56\nE 173\nM 88,196,160,88\nR 192,244,8704\nM 92,200,160,92\nE 174\nM 60,200,152,60\nE 175\nM 28,200,144,28\nR 196,244,8960\nW 3\n"
"M 144,204,96,228\nE 180\nM 152,204,112,232\nE 181\nM 160,204,128,236\nR 200,244,9216\nW 3\nM 164,208,128,236\nE 182\nM 156,208,112,232\nE 183\n"
"M 148,208,96,228\nR 204,244,9472\nW 3\nM 144,192,100,144\nE 188\nM 152,192,116,152\nE 189\nM 160,192,132,160\nR 208,244,9728\nW 3\nM 164,196,132,164\n"
"E 190\nM 156,196,116,156\nE 191\nM 148,196,100,148\nR 192,244,9984\nW 3\nM 144,200,104,144\nv_add_u32 v245, 0x10000, v245\n"
"v_add_u32 v246, 0x10000, v246\nM 152,200,120,152\nT 196,245,8192\nT 198,245,8448\nM 160,200,136,160\nC 185,186,187\nW 4\nM 164,204,136,164\n"
"T 200,246,8192\nT 202,246,8448\nM 156,204,120,156\nC 170,172,173\nC 171,174,175\nM 148,204,104,148\nC 178,180,181\nW 5\nM 144,208,108,144\n"
"T 204,245,8704\nT 206,245,8960\nM 152,208,124,152\nC 179,182,183\nC 186,188,189\nM 160,208,140,160\nC 187,190,191\nW 6\nM 164,192,140,164\n"
"T 208,246,8704\nT 210,246,8960\nM 156,192,124,156\nM 148,192,108,148\nW 6\nM 0,196,168,0\nT 192,245,9216\nT 194,245,9472\nE 144\nM 212,240,168,212\n"
"E 145\nE 146\nM 32,196,176,32\nE 147\nM 64,196,184,64\nC 144,144,145\nW 6\nM 68,200,184,68\nT 196,246,9216\nT 198,246,9472\nM 36,200,176,36\n"
"C 145,146,147\nM 216,240,176,216\nE 152\nM 4,200,168,4\nW 6\nM 8,204,168,8\nT 200,245,9728\nT 202,245,9984\nM 40,204,176,40\nE 153\nM 72,204,184,72\n"
"E 154\nM 220,240,184,220\nC 152,152,153\nW 6\nM 76,208,184,76\nT 204,246,9728\nT 206,246,9984\nM 44,208,176,44\nE 155\nM 12,208,168,12\nC 153,154,155\n"
"W 6\nM 16,192,168,16\nE 160\nM 48,192,176,48\nE 161\nM 80,192,184,80\nR 208,244,16384\nW 5\nM 84,196,184,84\nE 162\nM 52,196,176,52\nE 163\n"
"M 20,196,168,20\nR 192,244,16640\nW 4\nM 24,200,168,24\nE 148\nM 56,200,176,56\nE 149\nM 88,200,184,88\nR 196,244,16896\nW 3\nM 92,204,184,92\nE 150\n"
"M 60,204,176,60\nE 151\nM 28,204,168,28\nR 200,244,17152\nW 3\nM 168,208,96,228\nE 156\nM 176,208,112,232\nE 157\nM 184,208,128,236\nR 204,244,17408\n"
"W 3\nM 188,192,128,236\nE 158\nM 180,192,112,232\nE 159\nM 172,192,96,228\nR 208,244,17664\nW 3\nM 168,196,100,168\nE 164\nM 176,196,116,176\nE 165\n"
"M 184,196,132,184\nR 192,244,17920\nW 3\nM 188,200,132,188\nE 166\nM 180,200,116,180\nE 167\nM 172,200,100,172\nR 196,244,18176\nW 3\n"
"M 168,204,104,168\nC 160,160,161\nC 161,162,163\nM 176,204,120,176\nC 146,148,149\nC 147,150,151\nM 184,204,136,184\nC 154,156,157\nW 0\ns_barrier\n"
"v_add_u32 v245, 0x10000, v245\nv_add_u32 v246, 0x10000, v246\nT 200,245,8192\nT 202,245,8448\nM 188,208,136,188\nT 204,246,8192\nT 206,246,8448\n"
"M 180,208,120,180\nC 155,158,159\nC 162,164,165\nM 172,208,104,172\nC 163,166,167\nM 168,192,108,168\nT 208,245,8704\nT 210,245,8960\n"
"M 176,192,124,176\nM 184,192,140,184\nM 188,196,140,188\nT 192,246,8704\nT 194,246,8960\nM 180,196,124,180\nM 172,196,108,172\nW 6\nM 0,200,144,0\n"
"T 196,245,9216\nT 198,245,9472\nE 168\nM 212,240,144,212\nE 169\nE 170\nM 32,200,152,32\nE 171\nM 64,200,160,64\nC 168,168,169\nW 6\nM 68,204,160,68\n"
"T 200,246,9216\nT 202,246,9472\nM 36,204,152,36\nC 169,170,171\nM 216,240,152,216\nE 176\nM 4,204,144,4\nW 6\nM 8,208,144,8\nT 204,245,9728\n"
"T 206,245,9984\nM 40,208,152,40\nE 177\nM 72,208,160,72\nE 178\nM 220,240,160,220\nC 176,176,177\nW 6\nM 76,192,160,76\nT 208,246,9728\nT 210,246,9984\n"
"M 44,192,152,44\nE 179\nM 12,192,144,12\nC 177,178,179\nW 6\nM 16,196,144,16\nv_add_u32 v244, 0xfffe8000, v244\nR 192,244,8192\nM 48,196,152,48\nE 184\n"
"M 80,196,160,80\nW 5\nM 84,200,160,84\nE 185\nM 52,200,152,52\nE 186\nM 20,200,144,20\nR 196,244,8448\nW 4\nM 24,204,144,24\nE 187\nM 56,204,152,56\n"
"E 172\nM 88,204,160,88\nR 200,244,8704\nW 3\nM 92,208,160,92\nE 173\nM 60,208,152,60\nE 174\nM 28,208,144,28\nR 204,244,8960\nW 3\nM 144,192,96,228\n"
"E 175\nM 152,192,112,232\nE 180\nM 160,192,128,236\nR 208,244,9216\nW 3\nM 164,196,128,236\nE 181\nM 156,196,112,232\nE 182\nM 148,196,96,228\n"
"R 192,244,9472\nW 3\nM 144,200,100,144\nE 183\nM 152,200,116,152\nE 188\nM 160,200,132,160\nR 196,244,9728\nW 3\nM 164,204,132,164\nE 189\n"
"M 156,204,116,156\nE 190\nM 148,204,100,148\nR 200,244,9984\nW 3\nM 144,208,104,144\nT 204,245,16384\nT 206,245,16640\nM 152,208,120,152\nE 191\n"
"M 160,208,136,160\nC 184,184,185\nW 4\nM 164,192,136,164\nT 208,246,16384\nT 210,246,16640\nM 156,192,120,156\nC 185,186,187\nC 170,172,173\n"
"M 148,192,104,148\nC 171,174,175\nW 5\nM 144,196,108,144\nT 192,245,16896\nT 194,245,17152\nC 178,180,181\nM 152,196,124,152\nC 179,182,183\n"
"C 186,188,189\nM 160,196,140,160\nC 187,190,191\nW 6\nM 164,200,140,164\nT 196,246,16896\nT 198,246,17152\nM 156,200,124,156\nM 148,200,108,148\nW 6\n"
"M 0,204,168,0\nT 200,245,17408\nT 202,245,17664\nE 144\nM 212,240,168,212\nE 145\nE 146\nM 32,204,176,32\nE 147\nM 64,204,184,64\nC 144,144,145\nW 6\n"
"M 68,208,184,68\nT 204,246,17408\nT 206,246,17664\nM 36,208,176,36\nC 145,146,147\nM 216,240,176,216\nE 152\nM 4,208,168,4\nW 6\nM 8,192,168,8\n"
"T 208,245,17920\nT 210,245,18176\nM 40,192,176,40\nE 153\nM 72,192,184,72\nE 154\nM 220,240,184,220\nC 152,152,153\nW 6\nM 76,196,184,76\n"
"T 192,246,17920\nT 194,246,18176\nM 44,196,176,44\nE 155\nM 12,196,168,12\nC 153,154,155\nW 6\nM 16,200,168,16\nE 160\nM 48,200,176,48\nE 161\n"
"M 80,200,184,80\nC 160,160,161\nW 0\ns_barrier\nv_add_u32 v244, 0x10000, v244\nR 196,244,8192\nM 84,204,184,84\nE 162\nM 52,204,176,52\nE 163\n"
"M 20,204,168,20\nR 200,244,8448\nM 24,208,168,24\nE 148\nM 56,208,176,56\nE 149\nM 88,208,184,88\nR 204,244,8704\nM 92,192,184,92\nE 150\n"
"M 60,192,176,60\nE 151\nM 28,192,168,28\nR 208,244,8960\nW 3\nM 168,196,96,228\nE 156\nM 176,196,112,232\nE 157\nM 184,196,128,236\nR 192,244,9216\n"
"W 3\nM 188,200,128,236\nE 158\nM 180,200,112,232\nE 159\nM 172,200,96,228\nR 196,244,9472\nW 3\nM 168,204,100,168\nE 164\nM 176,204,116,176\nE 165\n"
"M 184,204,132,184\nR 200,244,9728\nW 3\nM 188,208,132,188\nE 166\nM 180,208,116,180\nE 167\nM 172,208,100,172\nR 204,244,9984\nW 3\nM 168,192,104,168\n"
"v_add_u32 v245, 0xfffe8000, v245\nv_add_u32 v246, 0xfffe8000, v246\nM 176,192,120,176\nT 208,245,8192\nT 210,245,8448\nM 184,192,136,184\n"
"C 161,162,163\nW 4\nM 188,196,136,188\nT 192,246,8192\nT 194,246,8448\nM 180,196,120,180\nC 146,148,149\nC 147,150,151\nM 172,196,104,172\n"
"C 154,156,157\nW 5\nM 168,200,108,168\nT 196,245,8704\nT 198,245,8960\nM 176,200,124,176\nC 155,158,159\nC 162,164,165\nM 184,200,140,184\n"
"C 163,166,167\nW 6\nM 188,204,140,188\nT 200,246,8704\nT 202,246,8960\nM 180,204,124,180\nM 172,204,108,172\nW 6\nM 0,208,144,0\nT 204,245,9216\n"
"T 206,245,9472\nE 168\nM 212,240,144,212\nE 169\nE 170\nM 32,208,152,32\nE 171\nM 64,208,160,64\nC 168,168,169\nW 6\nM 68,192,160,68\nT 208,246,9216\n"
"T 210,246,9472\nM 36,192,152,36\nC 169,170,171\nM 216,240,152,216\nE 176\nM 4,192,144,4\nW 6\nM 8,196,144,8\nT 192,245,9728\nT 194,245,9984\n"
"M 40,196,152,40\nE 177\nM 72,196,160,72\nE 178\nM 220,240,160,220\nC 176,176,177\nW 6\nM 76,200,160,76\nT 196,246,9728\nT 198,246,9984\n"
"M 44,200,152,44\nE 179\nM 12,200,144,12\nC 177,178,179\nW 6\nM 16,204,144,16\nE 184\nM 48,204,152,48\nE 185\nM 80,204,160,80\nR 200,244,16384\nW 5\n"
"M 84,208,160,84\nE 186\nM 52,208,152,52\nE 187\nM 20,208,144,20\nR 204,244,16640\nW 4\nM 24,192,144,24\nE 172\nM 56,192,152,56\nE 173\nM 88,192,160,88\n"
"R 208,244,16896\nW 3\nM 92,196,160,92\nE 174\nM 60,196,152,60\nE 175\nM 28,196,144,28\nR 192,244,17152\nW 3\nM 144,200,96,228\nE 180\n"
"M 152,200,112,232\nE 181\nM 160,200,128,236\nR 196,244,17408\nW 3\nM 164,204,128,236\nE 182\nM 156,204,112,232\nE 183\nM 148,204,96,228\n"
"R 200,244,17664\nW 3\nM 144,208,100,144\nE 188\nM 152,208,116,152\nE 189\nM 160,208,132,160\nR 204,244,17920\nW 3\nM 164,192,132,164\nE 190\n"
"M 156,192,116,156\nE 191\nM 148,192,100,148\nR 208,244,18176\nW 3\nM 144,196,104,144\nC 184,184,185\nC 185,186,187\nM 152,196,120,152\nC 170,172,173\n"
"C 171,174,175\nM 160,196,136,160\nC 178,180,181\nW 0\ns_barrier\nv_add_u32 v245, 0x10000, v245\nv_add_u32 v246, 0x10000, v246\nT 192,245,8192\n"
"T 194,245,8448\nM 164,200,136,164\nT 196,246,8192\nT 198,246,8448\nM 156,200,120,156\nC 179,182,183\nC 186,188,189\nM 148,200,104,148\nC 187,190,191\n"
"M 144,204,108,144\nT 200,245,8704\nT 202,245,8960\nM 152,204,124,152\nM 160,204,140,160\nM 164,208,140,164\nT 204,246,8704\nT 206,246,8960\n"
"M 156,208,124,156\nM 148,208,108,148\nW 6\nM 0,192,168,0\nT 208,245,9216\nT 210,245,9472\nE 144\nM 212,240,168,212\nE 145\nE 146\nM 32,192,176,32\n"
"E 147\nM 64,192,184,64\nC 144,144,145\nW 6\nM 68,196,184,68\nT 192,246,9216\nT 194,246,9472\nM 36,196,176,36\nC 145,146,147\nM 216,240,176,216\nE 152\n"
"M 4,196,168,4\nW 6\nM 8,200,168,8\nT 196,245,9728\nT 198,245,9984\nM 40,200,176,40\nE 153\nM 72,200,184,72\nE 154\nM 220,240,184,220\nC 152,152,153\n"
"W 6\nM 76,204,184,76\nT 200,246,9728\nT 202,246,9984\nM 44,204,176,44\nE 155\nM 12,204,168,12\nC 153,154,155\nW 6\nM 16,208,168,16\n"
"v_add_u32 v244, 0x10000, v244\nR 204,244,8192\nM 48,208,176,48\nE 160\nM 80,208,184,80\nW 5\nM 84,192,184,84\nE 161\nM 52,192,176,52\nE 162\n"
"M 20,192,168,20\nR 208,244,8448\nW 4\nM 24,196,168,24\nE 163\nM 56,196,176,56\nE 148\nM 88,196,184,88\nR 192,244,8704\nW 3\nM 92,200,184,92\nE 149\n"
"M 60,200,176,60\nE 150\nM 28,200,168,28\nR 196,244,8960\nW 3\nM 168,204,96,228\nE 151\nM 176,204,112,232\nE 156\nM 184,204,128,236\nR 200,244,9216\n"
"W 3\nM 188,208,128,236\nE 157\nM 180,208,112,232\nE 158\nM 172,208,96,228\nR 204,244,9472\nW 3\nM 168,192,100,168\nE 159\nM 176,192,116,176\nE 164\n"
"M 184,192,132,184\nR 208,244,9728\nW 3\nM 188,196,132,188\nE 165\nM 180,196,116,180\nE 166\nM 172,196,100,172\nR 192,244,9984\nW 3\nM 168,200,104,168\n"
"T 196,245,16384\nT 198,245,16640\nM 176,200,120,176\nE 167\nM 184,200,136,184\nC 160,160,161\nW 4\nM 188,204,136,188\nT 200,246,16384\nT 202,246,16640\n"
"C 161,162,163\nM 180,204,120,180\nC 146,148,149\nC 147,150,151\nC 154,156,157\nM 172,204,104,172\nC 155,158,159\nC 162,164,165\nW 5\nM 168,208,108,168\n"
"T 204,245,16896\nT 206,245,17152\nC 163,166,167\nM 176,208,124,176\nM 184,208,140,184\nW 6\nM 188,192,140,188\nT 208,246,16896\nT 210,246,17152\n"
"M 180,192,124,180\nM 172,192,108,172\nW 6\nM 0,196,144,0\nT 192,245,17408\nT 194,245,17664\nE 168\nM 212,240,144,212\nE 169\nE 170\nM 32,196,152,32\n"
"E 171\nC 168,168,169\nM 64,196,160,64\nC 169,170,171\nE 176\nW 6\nM 68,200,160,68\nT 196,246,17408\nT 198,246,17664\nE 177\nM 36,200,152,36\n"
"C 176,176,177\nE 178\nM 216,240,152,216\nE 179\nE 184\nM 4,200,144,4\nC 177,178,179\nE 185\nW 6\nM 8,204,144,8\nT 200,245,17920\nT 202,245,18176\n"
"C 184,184,185\nM 40,204,152,40\nE 186\nE 187\nM 72,204,160,72\nE 172\nC 185,186,187\nM 220,240,160,220\nE 173\nW 6\nM 76,208,160,76\nT 204,246,17920\n"
"T 206,246,18176\nC 170,172,173\nM 44,208,152,44\nE 174\nE 175\nM 12,208,144,12\nC 171,174,175\nE 180\nW 6\nM 16,192,144,16\nE 181\nE 182\n"
"M 48,192,152,48\nE 183\nM 80,192,160,80\nC 178,180,181\nC 179,182,183\nW 0\ns_barrier\nv_add_u32 v245, 0xfffe8000, v245\n"
"v_add_u32 v246, 0xfffe8000, v246\nT 208,245,8192\nT 210,245,8448\nM 84,196,160,84\nT 192,246,8192\nT 194,246,8448\nE 188\nM 52,196,152,52\nE 189\n"
"M 20,196,144,20\nC 186,188,189\nM 24,200,144,24\nT 196,245,8704\nT 198,245,8960\nM 56,200,152,56\nE 190\nM 88,200,160,88\nM 92,204,160,92\n"
"T 200,246,8704\nT 202,246,8960\nE 191\nM 60,204,152,60\nC 187,190,191\nM 28,204,144,28\nW 6\nM 0,208,168,0\nT 204,245,9216\nT 206,245,9472\n"
"M 212,240,168,212\nv_add_u32 v244, s72, v252\nv_lshrrev_b32 v151, 2, v252\nM 32,208,176,32\nv_and_b32 v151, 60, v151\nv_add_u32 v150, s73, v151\n"
"M 64,208,184,64\nv_add_u32 v151, s75, v151\nW 6\nM 68,192,184,68\nT 208,246,9216\nT 210,246,9472\nM 36,192,176,36\nv_mov_b32 v144, 0\n"
"v_mov_b32 v145, 0\nM 216,240,176,216\nv_mov_b32 v146, v224\nv_mov_b32 v147, v225\nM 4,192,168,4\nv_mov_b32 v148, 0\nW 6\nM 8,196,168,8\nT 192,245,9728\n"
"T 194,245,9984\nM 40,196,176,40\nv_mov_b32 v149, 0\nv_add_u32 v152, s76, v252\nM 72,196,184,72\nv_mov_b32 v153, 161280\nM 220,240,184,220\nW 6\n"
"M 76,200,184,76\nT 196,246,9728\nT 198,246,9984\nM 44,200,176,44\nC 32,32,33\nC 33,34,35\nM 12,200,168,12\nC 34,36,37\nW 6\nM 16,204,168,16\n"
"C 35,38,39\nds_write_b128 v244, v[32:35] offset:8192\nM 48,204,176,48\nC 64,64,65\nC 65,66,67\nM 80,204,184,80\nC 66,68,69\nW 5\nM 84,208,184,84\n"
"C 67,70,71\nds_write_b128 v244, v[64:67] offset:16384\nds_write_b32 v150, v212\nM 52,208,176,52\nds_write_b32 v150, v216 offset:64\n"
"ds_write_b32 v150, v220 offset:128\nM 20,208,168,20\nC 40,40,41\nW 7\nM 24,192,168,24\nC 41,42,43\nC 42,44,45\nC 43,46,47\nM 56,192,176,56\n"
"ds_write_b128 v244, v[40:43] offset:10240\nC 72,72,73\nM 88,192,184,88\nC 73,74,75\nW 6\nM 92,196,184,92\nC 74,76,77\nC 75,78,79\n"
"ds_write_b128 v244, v[72:75] offset:18432\nM 60,196,176,60\nC 48,48,49\nC 49,50,51\nC 50,52,53\nM 28,196,168,28\nC 51,54,55\n"
"ds_write_b128 v244, v[48:51] offset:12288\nC 80,80,81\nC 81,82,83\nC 82,84,85\nC 83,86,87\nds_write_b128 v244, v[80:83] offset:20480\nC 56,56,57\n"
"C 57,58,59\nC 58,60,61\nC 59,62,63\nds_write_b128 v244, v[56:59] offset:14336\nC 88,88,89\nC 89,90,91\nC 90,92,93\nC 91,94,95\n"
"ds_write_b128 v244, v[88:91] offset:22528\ns_add_u32 s26, s71, 5\ns_cmp_ge_u32 s26, 7\ns_cselect_b32 s27, -6, 1\ns_add_u32 s26, s26, s27\n"
"s_add_u32 s26, s26, s58\ns_and_b32 s26, s26, 7\ns_lshl_b32 s26, s26, 2\ns_add_u32 s26, s26, s20\ns_mul_i32 s52, s26, 40960\n"
"buffer_load_dwordx4 v[96:99], v247, s[44:47], s52 offen\nbuffer_load_dwordx4 v[100:103], v248, s[44:47], s52 offen offset:1024\n"
"buffer_load_dwordx4 v[104:107], v249, s[44:47], s52 offen offset:2048\nbuffer_load_dwordx4 v[108:111], v250, s[44:47], s52 offen offset:3072\n"
"buffer_load_dwordx4 v[112:115], v251, s[44:47], s52 offen\ns_add_u32 s26, s71, 6\ns_cmp_ge_u32 s26, 7\ns_cselect_b32 s27, -6, 1\n"
"s_add_u32 s26, s26, s27\ns_add_u32 s26, s26, s58\ns_and_b32 s26, s26, 7\ns_lshl_b32 s26, s26, 2\ns_add_u32 s26, s26, s20\ns_mul_i32 s52, s26, 40960\n"
"buffer_load_dwordx4 v[116:119], v247, s[44:47], s52 offen\nbuffer_load_dwordx4 v[120:123], v248, s[44:47], s52 offen offset:1024\n"
"buffer_load_dwordx4 v[124:127], v249, s[44:47], s52 offen offset:2048\nbuffer_load_dwordx4 v[128:131], v250, s[44:47], s52 offen offset:3072\n"
"buffer_load_dwordx4 v[132:135], v251, s[44:47], s52 offen\nv_mov_b32 v240, v212\nW 0\ns_barrier\nds_read_b32 v176, v151 offset:768\n"
"ds_read_b32 v177, v151 offset:1152\nR 192,244,0\nR 196,244,1024\nR 200,244,2048\nR 204,244,3072\nR 208,244,4096\nR 212,244,5120\nR 216,244,6144\n"
"R 220,244,7168\nW 9\nv_add_f32 v240, v240, v176\nW 8\nv_add_f32 v240, v240, v177\nv_cmp_ngt_f32 vcc, 0x53800000, v240\nv_rcp_f32 v170, v240\n"
"v_rcp_f32 v171, v240\ns_cmp_lg_u64 vcc, 0\ns_cbranch_scc0 .Lw_205_%=\nds_write_b32 v153, v153 offset:4\n.Lw_205_%=:\nW 7\nM 0,146,192,0\nM 4,144,192,4\n"
"W 6\nM 0,146,196,0\nM 4,144,196,4\nW 5\nM 8,146,200,8\nM 12,144,200,12\nW 4\nM 8,146,204,8\nM 12,144,204,12\ns_nop 0\n"
"v_pk_mul_f32 v[0:1], v[0:1], v[170:171]\nW 3\nM 16,146,208,16\nv_pk_mul_f32 v[2:3], v[2:3], v[170:171]\nv_pk_mul_f32 v[4:5], v[4:5], v[170:171]\n"
"M 20,144,208,20\nv_pk_mul_f32 v[6:7], v[6:7], v[170:171]\nC 0,0,1\nW 2\nM 16,146,212,16\nC 1,2,3\nC 2,4,5\nM 20,144,212,20\nC 3,6,7\n"
"buffer_store_dwordx4 v[0:3], v152, s[48:51], s64 offen sc1\nv_pk_mul_f32 v[8:9], v[8:9], v[170:171]\nW 1\nM 24,146,216,24\n"
"v_pk_mul_f32 v[10:11], v[10:11], v[170:171]\nv_pk_mul_f32 v[12:13], v[12:13], v[170:171]\nM 28,144,216,28\nv_pk_mul_f32 v[14:15], v[14:15], v[170:171]\n"
"C 8,8,9\nW 0\nM 24,146,220,24\nC 9,10,11\nC 10,12,13\nM 28,144,220,28\nC 11,14,15\n"
"buffer_store_dwordx4 v[8:11], v152, s[48:51], s64 offen offset:1024 sc1\nv_pk_mul_f32 v[16:17], v[16:17], v[170:171]\n"
"v_pk_mul_f32 v[18:19], v[18:19], v[170:171]\nv_pk_mul_f32 v[20:21], v[20:21], v[170:171]\nv_pk_mul_f32 v[22:23], v[22:23], v[170:171]\nC 16,16,17\n"
"C 17,18,19\nC 18,20,21\nC 19,22,23\nbuffer_store_dwordx4 v[16:19], v152, s[48:51], s64 offen offset:2048 sc1\n"
"v_pk_mul_f32 v[24:25], v[24:25], v[170:171]\nv_pk_mul_f32 v[26:27], v[26:27], v[170:171]\nv_pk_mul_f32 v[28:29], v[28:29], v[170:171]\n"
"v_pk_mul_f32 v[30:31], v[30:31], v[170:171]\nC 24,24,25\nC 25,26,27\nC 26,28,29\nC 27,30,31\n"
"buffer_store_dwordx4 v[24:27], v152, s[48:51], s64 offen offset:3072 sc1\nds_write_b128 v152, v[0:3]\nds_write_b128 v152, v[8:11] offset:1024\n"
"ds_write_b128 v152, v[16:19] offset:2048\nds_write_b128 v152, v[24:27] offset:3072\nv_add_u32 v154, s78, v252\nv_xor_b32 v155, 4096, v154\n"
"v_add_u32 v156, s79, v252\nv_xor_b32 v157, 1024, v154\nv_xor_b32 v158, 1024, v155\nv_add_u32 v162, s31, v252\n.Lt0_9_%=:\ns_nop 1\nW 0\ns_barrier\n"
"ds_read_b32 v244, v153 offset:4\nR 184,154,0\nR 188,155,0\nR 200,156,0\nR 192,157,0\nR 196,158,0\nT 60,253,0\nT 62,253,2560\nT 64,253,32\n"
"T 66,253,2592\nT 68,253,64\nT 70,253,2624\nT 72,253,96\nT 74,253,2656\nT 76,253,128\nT 78,253,2688\nW 15\nv_readfirstlane_b32 s28, v244\n"
"s_cmp_eq_u32 s35, 0\ns_cselect_b32 s28, s28, 0\ns_cmp_lg_u32 s28, 0\ns_cbranch_scc1 .Lfb0_11_%=\ns_cmp_eq_u32 s19, 1\ns_cbranch_scc0 .Lw_206_%=\n"
"s_waitcnt vmcnt(6)\n.Lw_206_%=:\ns_cmp_eq_u32 s19, 1\ns_cbranch_scc0 .Lw_207_%=\nv_cmp_eq_u32 vcc, s34, v160\ns_nop 0\ns_and_b32 s26, vcc_lo, 127\n"
"s_ff0_i32_b32 s26, s26\nv_mov_b32 v243, s26\nds_write_b32 v153, v243\n.Lw_207_%=:\ns_cmp_eq_u32 s19, 1\ns_cbranch_scc0 .Lw_208_%=\n"
"buffer_load_dword v160, v226, s[48:51], s66 offen sc1\n.Lw_208_%=:\nW 8\nv_mfma_f32_16x16x32_bf16 v[20:23], v[60:63], v[188:191], 0\nT 80,227,0\n"
"T 82,227,2560\nv_mfma_f32_16x16x32_bf16 v[0:3], v[60:63], v[184:187], 0\nv_add_u32 v84, 61440, v253\nv_add_u32 v85, 61440, v227\n"
"v_mfma_f32_16x16x32_bf16 v[40:43], v[60:63], v[200:203], 0\nv_add_u32 v86, 4294930432, v253\nW 8\n"
"v_mfma_f32_16x16x32_bf16 v[44:47], v[64:67], v[200:203], 0\nT 60,227,32\nT 62,227,2592\nv_mfma_f32_16x16x32_bf16 v[4:7], v[64:67], v[184:187], 0\n"
"v_add_u32 v87, 4294930432, v227\ns_mov_b64 s[60:61], s[48:49]\ns_mov_b32 s63, 131072\nv_mfma_f32_16x16x32_bf16 v[24:27], v[64:67], v[188:191], 0\n"
"v_mov_b32 v245, s34\nv_add_u32 v246, s19, v255\nv_min_u32 v246, 1, v246\nv_lshlrev_b32 v246, 28, v246\ns_lshl_b32 s52, s58, 8\ns_add_u32 s52, s52, s66\n"
"W 8\nv_mfma_f32_16x16x32_bf16 v[28:31], v[68:71], v[188:191], 0\nT 64,227,64\nT 66,227,2624\nv_mfma_f32_16x16x32_bf16 v[8:11], v[68:71], v[184:187], 0\n"
"v_mfma_f32_16x16x32_bf16 v[48:51], v[68:71], v[200:203], 0\nW 8\nv_mfma_f32_16x16x32_bf16 v[52:55], v[72:75], v[200:203], 0\nT 68,227,96\n"
"T 70,227,2656\nv_mfma_f32_16x16x32_bf16 v[12:15], v[72:75], v[184:187], 0\nv_mfma_f32_16x16x32_bf16 v[32:35], v[72:75], v[188:191], 0\nW 8\n"
"v_mfma_f32_16x16x32_bf16 v[36:39], v[76:79], v[188:191], 0\nT 72,227,128\nT 74,227,2688\nv_mfma_f32_16x16x32_bf16 v[16:19], v[76:79], v[184:187], 0\n"
"v_mfma_f32_16x16x32_bf16 v[56:59], v[76:79], v[200:203], 0\nW 8\nM 20,80,196,20\nT 76,253,20480\nT 78,253,23040\nM 0,80,192,0\nW 8\nM 4,60,192,4\n"
"T 80,253,20512\nT 82,253,23072\nM 24,60,196,24\nds_read_b32 v243, v153\nW 0\nv_readfirstlane_b32 s4, v243\ns_cmp_eq_u32 s77, 0\n"
"s_cbranch_scc0 .Lw_209_%=\ns_cmp_ge_u32 s4, 1\ns_cbranch_scc0 .Lw_210_%=\ns_mov_b32 s77, 1\n"
"buffer_load_dwordx4 v[164:167], v154, s[48:51], s5 offen sc1\nbuffer_load_dwordx4 v[168:171], v155, s[48:51], s5 offen sc1\n"
"buffer_load_dwordx4 v[180:183], v156, s[48:51], s5 offen sc1\nbuffer_load_dwordx4 v[172:175], v157, s[48:51], s5 offen sc1\n"
"buffer_load_dwordx4 v[176:179], v158, s[48:51], s5 offen sc1\n.Lw_210_%=:\n.Lw_209_%=:\ns_cmp_eq_u32 s77, 1\ns_cbranch_scc0 .Lw_211_%=\n"
"s_cmp_ge_u32 s4, 2\ns_cbranch_scc0 .Lw_212_%=\ns_mov_b32 s77, 2\nbuffer_load_dwordx4 v[204:207], v154, s[48:51], s6 offen sc1\n"
"buffer_load_dwordx4 v[208:211], v155, s[48:51], s6 offen sc1\nbuffer_load_dwordx4 v[220:223], v156, s[48:51], s6 offen sc1\n"
"buffer_load_dwordx4 v[212:215], v157, s[48:51], s6 offen sc1\nbuffer_load_dwordx4 v[216:219], v158, s[48:51], s6 offen sc1\n.Lw_212_%=:\n.Lw_211_%=:\n"
"M 28,64,196,28\nT 60,253,20544\nT 62,253,23104\nM 8,64,192,8\nM 12,68,192,12\nT 64,253,20576\nT 66,253,23136\nM 32,68,196,32\nM 36,72,196,36\n"
"T 68,253,20608\nT 70,253,23168\nM 16,72,192,16\ns_cmp_eq_u32 s77, 0\ns_cbranch_scc0 .Lw_213_%=\ns_waitcnt vmcnt(0)\n.Lw_213_%=:\ns_cmp_eq_u32 s77, 1\n"
"s_cbranch_scc0 .Lw_214_%=\ns_waitcnt vmcnt(5)\n.Lw_214_%=:\ns_waitcnt vmcnt(10)\ns_cmp_eq_u32 s19, 1\ns_cbranch_scc0 .Lw_215_%=\n"
"v_cmp_eq_u32 vcc, s34, v160\ns_nop 0\ns_and_b32 s26, vcc_lo, 127\ns_ff0_i32_b32 s26, s26\nv_mov_b32 v243, s26\nds_write_b32 v153, v243\n.Lw_215_%=:\n"
"W 0\ns_barrier\nbuffer_store_dword v245, v246, s[48:51], s52 offen sc1\nds_read_b32 v243, v153\nW 0\nv_readfirstlane_b32 s4, v243\ns_cmp_eq_u32 s77, 0\n"
"s_cbranch_scc0 .Lw_216_%=\ns_cmp_ge_u32 s4, 1\ns_cbranch_scc0 .Lw_217_%=\ns_mov_b32 s77, 1\n"
"buffer_load_dwordx4 v[164:167], v154, s[48:51], s5 offen sc1\nbuffer_load_dwordx4 v[168:171], v155, s[48:51], s5 offen sc1\n"
"buffer_load_dwordx4 v[180:183], v156, s[48:51], s5 offen sc1\nbuffer_load_dwordx4 v[172:175], v157, s[48:51], s5 offen sc1\n"
"buffer_load_dwordx4 v[176:179], v158, s[48:51], s5 offen sc1\n.Lw_217_%=:\n.Lw_216_%=:\ns_cmp_eq_u32 s77, 1\ns_cbranch_scc0 .Lw_218_%=\n"
"s_cmp_ge_u32 s4, 2\ns_cbranch_scc0 .Lw_219_%=\ns_mov_b32 s77, 2\nbuffer_load_dwordx4 v[204:207], v154, s[48:51], s6 offen sc1\n"
"buffer_load_dwordx4 v[208:211], v155, s[48:51], s6 offen sc1\nbuffer_load_dwordx4 v[220:223], v156, s[48:51], s6 offen sc1\n"
"buffer_load_dwordx4 v[212:215], v157, s[48:51], s6 offen sc1\nbuffer_load_dwordx4 v[216:219], v158, s[48:51], s6 offen sc1\n.Lw_219_%=:\n.Lw_218_%=:\n"
"s_cmp_eq_u32 s25, 1\ns_cbranch_scc0 .Lw_220_%=\nds_write_b128 v162, v[96:99]\nds_write_b128 v162, v[100:103] offset:1024\n"
"ds_write_b128 v162, v[104:107] offset:2048\nds_write_b128 v162, v[108:111] offset:3072\nds_write_b128 v162, v[112:115] offset:4096\n"
"ds_write_b128 v162, v[116:119] offset:20480\nds_write_b128 v162, v[120:123] offset:21504\nds_write_b128 v162, v[124:127] offset:22528\n"
"ds_write_b128 v162, v[128:131] offset:23552\nds_write_b128 v162, v[132:135] offset:24576\n.Lw_220_%=:\ns_cmp_lg_u32 s4, 7\ns_cbranch_scc1 .Lss_221_%=\n"
"s_waitcnt vmcnt(5)\n.Lsb_222_%=:\nM 20,76,168,20\nT 72,227,20480\nT 74,227,23040\nM 0,76,164,0\ns_add_u32 s26, s77, 1\ns_min_u32 s26, s26, s4\n"
"s_cmp_eq_u32 s26, 3\ns_cselect_b32 s62, 67108864, 0\ns_cselect_b32 s77, 3, s77\nM 40,76,180,40\n"
"buffer_load_dwordx4 v[184:187], v154, s[60:63], s7 offen sc1\nM 44,80,180,44\nT 76,227,20512\nT 78,227,23072\nM 4,80,164,4\n"
"buffer_load_dwordx4 v[188:191], v155, s[60:63], s7 offen sc1\nbuffer_load_dwordx4 v[200:203], v156, s[60:63], s7 offen sc1\nM 24,80,168,24\n"
"buffer_load_dwordx4 v[192:195], v157, s[60:63], s7 offen sc1\nM 28,60,168,28\nT 80,227,20544\nT 82,227,23104\nM 8,60,164,8\n"
"buffer_load_dwordx4 v[196:199], v158, s[60:63], s7 offen sc1\nv_add_u32 v163, s56, v252\nM 48,60,180,48\nv_add_u32 v163, 40960, v163\nM 52,64,180,52\n"
"T 60,227,20576\nT 62,227,23136\nM 12,64,164,12\nv_xor_b32 v244, 1024, v163\nv_mov_b32 v229, s34\nM 32,64,168,32\nv_mov_b32 v231, s34\nM 36,68,168,36\n"
"T 64,227,20608\nT 66,227,23168\nM 16,68,164,16\nv_mov_b32 v233, s34\nv_mov_b32 v235, s34\nM 56,68,180,56\nv_mov_b32 v237, s34\nW 8\nM 20,72,176,20\n"
"T 68,253,40960\nT 70,253,43520\nM 0,72,172,0\nv_mov_b32 v239, s34\nW 8\nM 4,76,172,4\nT 72,253,40992\nT 74,253,43552\nM 24,76,176,24\n"
"v_mov_b32 v144, v224\nW 8\nM 28,80,176,28\nT 76,253,41024\nT 78,253,43584\nM 8,80,172,8\nv_mov_b32 v145, 0\nW 8\nM 12,60,172,12\nT 80,253,41056\n"
"T 82,253,43616\nM 32,60,176,32\nv_mov_b32 v146, v225\nW 8\nM 36,64,176,36\nT 60,253,41088\nT 62,253,43648\nM 16,64,172,16\nv_mov_b32 v147, 0\n"
"s_waitcnt vmcnt(5)\ns_cmp_lg_u32 s4, 7\ns_cbranch_scc1 .Lss_237_%=\n.Lsb_238_%=:\nW 8\nM 20,68,208,20\nT 64,227,40960\nT 66,227,43520\nM 0,68,204,0\n"
"s_add_u32 s26, s77, 1\ns_min_u32 s26, s26, s4\ns_cmp_eq_u32 s26, 4\ns_cselect_b32 s62, 67108864, 0\ns_cselect_b32 s77, 4, s77\nM 40,68,220,40\n"
"buffer_load_dwordx4 v[164:167], v154, s[60:63], s8 offen sc1\nW 8\nM 44,72,220,44\nT 68,227,40992\nT 70,227,43552\nM 4,72,204,4\n"
"buffer_load_dwordx4 v[168:171], v155, s[60:63], s8 offen sc1\nbuffer_load_dwordx4 v[180:183], v156, s[60:63], s8 offen sc1\nM 24,72,208,24\n"
"buffer_load_dwordx4 v[172:175], v157, s[60:63], s8 offen sc1\nW 8\nM 28,76,208,28\nT 72,227,41024\nT 74,227,43584\nM 8,76,204,8\n"
"buffer_load_dwordx4 v[176:179], v158, s[60:63], s8 offen sc1\nv_add_u32 v92, s56, v252\nM 48,76,220,48\nv_add_u32 v93, s69, v252\nW 8\nM 52,80,220,52\n"
"T 76,227,41056\nT 78,227,43616\nM 12,80,204,12\nv_add_u32 v94, s98, v252\ns_mul_i32 s72, s19, 20480\ns_lshl_b32 s73, s59, 5\nM 32,80,208,32\n"
"s_and_b32 s74, s69, 268435456\ns_add_u32 s74, s74, s72\nW 8\nM 36,60,208,36\nT 80,227,41088\nT 82,227,43648\nM 16,60,204,16\nv_add_u32 v90, s74, v254\n"
"s_add_u32 s72, s72, s73\nv_add_u32 v89, s72, v254\nM 56,60,220,56\ns_and_b32 s74, s98, 268435456\ns_add_u32 s74, s74, 163968\nW 8\nM 20,64,216,20\n"
"T 60,84,0\nT 62,84,2560\nM 0,64,212,0\nv_add_u32 v91, s74, v254\nW 8\nM 4,68,212,4\nT 64,84,32\nT 66,84,2592\nM 24,68,216,24\n"
"s_andn2_b32 s74, 268435456, s69\nv_add_u32 v95, s74, v252\nW 8\nM 28,72,216,28\nT 68,84,64\nT 70,84,2624\nM 8,72,212,8\ns_cmp_eq_u32 s19, 0\n"
"s_cselect_b32 s74, 0, 268435456\nW 8\nM 12,76,212,12\nT 72,84,96\nT 74,84,2656\nM 32,76,216,32\nv_add_u32 v88, s74, v252\nW 8\nM 36,80,216,36\n"
"T 76,84,128\nT 78,84,2688\nM 16,80,212,16\ns_waitcnt vmcnt(5)\ns_cmp_lg_u32 s4, 7\ns_cbranch_scc1 .Lss_254_%=\n.Lsb_255_%=:\nW 8\nM 20,60,188,20\n"
"T 80,85,0\nT 82,85,2560\nM 0,60,184,0\ns_add_u32 s26, s77, 1\ns_min_u32 s26, s26, s4\ns_cmp_eq_u32 s26, 5\ns_cselect_b32 s62, 67108864, 0\n"
"s_cselect_b32 s77, 5, s77\nM 40,60,200,40\nbuffer_load_dwordx4 v[204:207], v154, s[60:63], s9 offen sc1\nW 8\nM 44,64,200,44\nT 60,85,32\nT 62,85,2592\n"
"M 4,64,184,4\nbuffer_load_dwordx4 v[208:211], v155, s[60:63], s9 offen sc1\nbuffer_load_dwordx4 v[220:223], v156, s[60:63], s9 offen sc1\n"
"M 24,64,188,24\nbuffer_load_dwordx4 v[212:215], v157, s[60:63], s9 offen sc1\nW 8\nM 28,68,188,28\nT 64,85,64\nT 66,85,2624\nM 8,68,184,8\n"
"buffer_load_dwordx4 v[216:219], v158, s[60:63], s9 offen sc1\nM 48,68,200,48\nW 8\nM 52,72,200,52\nT 68,85,96\nT 70,85,2656\nM 12,72,184,12\n"
"M 32,72,188,32\nW 8\nM 36,76,188,36\nT 72,85,128\nT 74,85,2688\nM 16,76,184,16\nM 56,76,200,56\nW 0\ns_barrier\nM 20,80,196,20\nT 76,84,20512\n"
"T 78,84,23072\nM 0,80,192,0\nM 4,60,192,4\nT 80,84,20544\nT 82,84,23104\nM 24,60,196,24\nM 28,64,196,28\nT 60,84,20576\nT 62,84,23136\nM 8,64,192,8\n"
"M 12,68,192,12\nT 64,84,20608\nT 66,84,23168\nM 32,68,196,32\nM 36,72,196,36\nT 68,85,20512\nT 70,85,23072\nM 16,72,192,16\ns_waitcnt vmcnt(5)\n"
"s_cmp_lg_u32 s4, 7\ns_cbranch_scc1 .Lss_271_%=\n.Lsb_272_%=:\nW 8\nM 44,76,180,44\nT 72,85,20544\nT 74,85,23104\nM 4,76,164,4\ns_add_u32 s26, s77, 1\n"
"s_min_u32 s26, s26, s4\ns_cmp_eq_u32 s26, 6\ns_cselect_b32 s62, 67108864, 0\ns_cselect_b32 s77, 6, s77\nM 24,76,168,24\n"
"buffer_load_dwordx4 v[184:187], v154, s[60:63], s10 offen sc1\nW 8\nM 28,80,168,28\nT 76,85,20576\nT 78,85,23136\nM 8,80,164,8\n"
"buffer_load_dwordx4 v[188:191], v155, s[60:63], s10 offen sc1\nbuffer_load_dwordx4 v[200:203], v156, s[60:63], s10 offen sc1\nM 48,80,180,48\n"
"buffer_load_dwordx4 v[192:195], v157, s[60:63], s10 offen sc1\nW 8\nM 52,60,180,52\nT 80,85,20608\nT 82,85,23168\nM 12,60,164,12\n"
"buffer_load_dwordx4 v[196:199], v158, s[60:63], s10 offen sc1\nM 32,60,168,32\nW 8\nM 36,64,168,36\nT 60,84,40992\nT 62,84,43552\nM 16,64,164,16\n"
"M 56,64,180,56\nW 8\nM 4,68,172,4\nT 64,84,41024\nT 66,84,43584\nM 24,68,176,24\nW 8\nM 28,72,176,28\nT 68,84,41056\nT 70,84,43616\nM 8,72,172,8\nW 8\n"
"M 12,76,172,12\nT 72,84,41088\nT 74,84,43648\nM 32,76,176,32\nW 8\nM 36,80,176,36\nT 76,85,40992\nT 78,85,43552\nM 16,80,172,16\ns_waitcnt vmcnt(5)\n"
"s_cmp_lg_u32 s4, 7\ns_cbranch_scc1 .Lss_288_%=\n.Lsb_289_%=:\nW 8\nM 44,60,220,44\nT 80,85,41024\nT 82,85,43584\nM 4,60,204,4\ns_add_u32 s26, s77, 1\n"
"s_min_u32 s26, s26, s4\ns_cmp_eq_u32 s26, 7\ns_cselect_b32 s62, 67108864, 0\ns_cselect_b32 s77, 7, s77\nM 24,60,208,24\n"
"buffer_load_dwordx4 v[96:99], v154, s[60:63], s11 offen sc1\nW 8\nM 28,64,208,28\nT 60,85,41056\nT 62,85,43616\nM 8,64,204,8\n"
"buffer_load_dwordx4 v[100:103], v155, s[60:63], s11 offen sc1\nbuffer_load_dwordx4 v[112:115], v156, s[60:63], s11 offen sc1\nM 48,64,220,48\n"
"buffer_load_dwordx4 v[104:107], v157, s[60:63], s11 offen sc1\nW 8\nM 52,68,220,52\nT 64,85,41088\nT 66,85,43648\nM 12,68,204,12\n"
"buffer_load_dwordx4 v[108:111], v158, s[60:63], s11 offen sc1\nM 32,68,208,32\nW 8\nM 36,72,208,36\nT 68,86,32\nT 70,86,2592\nM 16,72,204,16\n"
"M 56,72,220,56\nW 8\nM 4,76,212,4\nT 72,87,32\nT 74,87,2592\nM 24,76,216,24\nW 8\nM 28,80,216,28\nT 76,86,20512\nT 78,86,23072\nM 8,80,212,8\nW 8\n"
"M 12,60,212,12\nT 80,87,20512\nT 82,87,23072\nM 32,60,216,32\nW 8\nM 36,64,216,36\nT 60,86,64\nT 62,86,2624\nM 16,64,212,16\ns_cmp_lg_u32 s4, 7\n"
"s_cbranch_scc1 .Lss_305_%=\n.Lsb_306_%=:\ns_waitcnt vmcnt(5)\nW 8\nM 24,68,188,24\nT 64,87,64\nT 66,87,2624\nM 4,68,184,4\nM 44,68,200,44\nW 8\n"
"M 24,72,196,24\nT 68,86,20544\nT 70,86,23104\nM 4,72,192,4\ns_waitcnt vmcnt(0)\nW 8\nM 24,76,100,24\nT 72,87,20544\nT 74,87,23104\nM 4,76,96,4\n"
"M 44,76,112,44\nW 8\nM 24,80,108,24\nT 76,86,96\nT 78,86,2656\nM 4,80,104,4\nv_add_u32 v243, 86016, v252\nW 8\nM 28,60,188,28\nT 80,87,96\n"
"T 82,87,2656\nM 8,60,184,8\nM 48,60,200,48\nW 8\nM 28,64,196,28\nT 60,86,20576\nT 62,86,23136\nM 8,64,192,8\nds_write_b128 v163, v[24:27]\n"
"ds_write_b128 v163, v[44:47] offset:8192\nW 10\nM 28,68,100,28\nT 64,87,20576\nT 66,87,23136\nM 8,68,96,8\nW 2\ns_barrier\nR 24,244,0\nM 48,68,112,48\n"
"M 28,72,108,28\nT 68,86,128\nT 70,86,2688\nM 8,72,104,8\nM 32,76,188,32\nT 72,87,128\nT 74,87,2688\nM 12,76,184,12\nW 4\n"
"v_pk_add_f32 v[4:5], v[4:5], v[24:25]\nv_pk_add_f32 v[6:7], v[6:7], v[26:27]\nM 52,76,200,52\nC 228,4,5\nM 32,80,196,32\nT 76,86,20608\nT 78,86,23168\n"
"M 12,80,192,12\nC 230,6,7\nbuffer_store_dwordx4 v[228:231], v252, s[48:51], s12 offen sc1\nM 32,60,100,32\nT 80,87,20608\nT 82,87,23168\nM 12,60,96,12\n"
"ds_write_b128 v163, v[28:31] offset:12288\nds_write_b128 v163, v[48:51] offset:20480\nM 52,60,112,52\nW 0\ns_barrier\nR 28,244,12288\nM 32,64,108,32\n"
"T 60,84,20480\nT 62,84,23040\nM 12,64,104,12\nM 36,68,188,36\nT 64,85,20480\nT 66,85,23040\nM 16,68,184,16\nW 4\nv_pk_add_f32 v[8:9], v[8:9], v[28:29]\n"
"v_pk_add_f32 v[10:11], v[10:11], v[30:31]\nM 56,68,200,56\nC 232,8,9\nM 36,72,196,36\nT 68,84,40960\nT 70,84,43520\nM 16,72,192,16\nC 234,10,11\n"
"buffer_store_dwordx4 v[232:235], v252, s[48:51], s13 offen sc1\nM 36,76,100,36\nT 72,85,40960\nT 74,85,43520\nM 16,76,96,16\n"
"ds_write_b128 v163, v[32:35] offset:24576\nds_write_b128 v163, v[52:55] offset:32768\nM 56,76,112,56\nW 0\ns_barrier\nR 32,244,24576\nM 36,80,108,36\n"
"T 76,86,0\nT 78,86,2560\nM 16,80,104,16\nM 20,60,168,20\nT 80,87,0\nT 82,87,2560\nM 0,60,164,0\nW 4\nv_pk_add_f32 v[12:13], v[12:13], v[32:33]\n"
"v_pk_add_f32 v[14:15], v[14:15], v[34:35]\nM 40,60,180,40\nC 228,12,13\nM 20,64,176,20\nT 60,86,20480\nT 62,86,23040\nM 0,64,172,0\nC 230,14,15\n"
"buffer_store_dwordx4 v[228:231], v252, s[48:51], s14 offen sc1\nM 20,68,208,20\nT 64,87,20480\nT 66,87,23040\nM 0,68,204,0\n"
"ds_write_b128 v163, v[36:39] offset:36864\nds_write_b128 v163, v[56:59] offset:45056\nM 40,68,220,40\nW 0\ns_barrier\nR 36,244,36864\nM 20,72,216,20\n"
"R 116,243,0\nR 120,243,1024\nM 0,72,212,0\nR 124,243,2048\nR 128,243,3072\nM 20,76,188,20\nW 4\nv_pk_add_f32 v[16:17], v[16:17], v[36:37]\n"
"v_pk_add_f32 v[18:19], v[18:19], v[38:39]\nM 0,76,184,0\nC 232,16,17\nC 234,18,19\nM 40,76,200,40\n"
"buffer_store_dwordx4 v[232:235], v95, s[48:51], s15 offen sc1\nM 20,80,196,20\nW 2\nv_pk_add_f32 v[132:133], v[116:117], v[120:121]\nW 0\n"
"v_pk_add_f32 v[124:125], v[124:125], v[128:129]\nM 0,80,192,0\nv_pk_add_f32 v[134:135], v[118:119], v[122:123]\n"
"v_pk_add_f32 v[126:127], v[126:127], v[130:131]\nM 20,60,100,20\nv_pk_add_f32 v[132:133], v[132:133], v[124:125]\n"
"v_pk_add_f32 v[134:135], v[134:135], v[126:127]\nM 0,60,96,0\nC 236,132,133\nC 238,134,135\nM 40,60,112,40\n"
"buffer_store_dwordx4 v[236:239], v88, s[48:51], s22 offen sc1\nM 20,64,108,20\nM 0,64,104,0\ns_nop 6\nds_write_b128 v163, v[20:23] offset:49152\n"
"ds_write_b128 v163, v[40:43] offset:57344\nW 0\ns_barrier\nR 20,244,49152\nW 0\nv_pk_add_f32 v[0:1], v[0:1], v[20:21]\n"
"v_pk_add_f32 v[2:3], v[2:3], v[22:23]\ns_mov_b32 s68, 0\ns_mov_b32 s77, 7\n.Lgp_319_%=:\ns_bitcmp1_b32 s77, 0\ns_cbranch_scc0 .Lw_321_%=\n"
"buffer_load_dwordx4 v[164:167], v92, s[48:51], s23 offen sc1\nbuffer_load_dwordx4 v[168:171], v93, s[48:51], s23 offen sc1\n"
"buffer_load_dwordx4 v[172:175], v94, s[48:51], s23 offen sc1\n.Lw_321_%=:\ns_bitcmp1_b32 s77, 1\ns_cbranch_scc0 .Lw_322_%=\n"
"buffer_load_dwordx4 v[176:179], v92, s[48:51], s24 offen sc1\nbuffer_load_dwordx4 v[180:183], v93, s[48:51], s24 offen sc1\n"
"buffer_load_dwordx4 v[136:139], v94, s[48:51], s24 offen sc1\n.Lw_322_%=:\ns_bitcmp1_b32 s77, 2\ns_cbranch_scc0 .Lw_323_%=\n"
"buffer_load_dwordx4 v[140:143], v92, s[48:51], s54 offen sc1\nbuffer_load_dwordx4 v[148:151], v93, s[48:51], s54 offen sc1\n"
"buffer_load_dwordx4 v[156:159], v94, s[48:51], s54 offen sc1\n.Lw_323_%=:\ns_waitcnt vmcnt(0)\n.Lgc_320_%=:\ns_bitcmp1_b32 s77, 0\n"
"s_cbranch_scc0 .Lw_324_%=\nv_xor_b32 v60, s34, v165\nv_xor_b32 v61, s34, v167\nv_xor_b32 v62, s99, v169\nv_xor_b32 v63, s99, v171\n"
"v_xor_b32 v64, s67, v173\nv_xor_b32 v65, s67, v175\nv_or3_b32 v60, v60, v61, v62\nv_or3_b32 v63, v63, v64, v65\nv_or_b32 v60, v60, v63\n"
"v_cmp_ne_u32 vcc, 0, v60\ns_nop 0\ns_cmp_eq_u64 vcc, 0\ns_cbranch_scc0 .Lw_325_%=\ns_bitset0_b32 s77, 0\n.Lw_325_%=:\n.Lw_324_%=:\n"
"s_bitcmp1_b32 s77, 1\ns_cbranch_scc0 .Lw_326_%=\nv_xor_b32 v60, s34, v177\nv_xor_b32 v61, s34, v179\nv_xor_b32 v62, s99, v181\n"
"v_xor_b32 v63, s99, v183\nv_xor_b32 v64, s67, v137\nv_xor_b32 v65, s67, v139\nv_or3_b32 v60, v60, v61, v62\nv_or3_b32 v63, v63, v64, v65\n"
"v_or_b32 v60, v60, v63\nv_cmp_ne_u32 vcc, 0, v60\ns_nop 0\ns_cmp_eq_u64 vcc, 0\ns_cbranch_scc0 .Lw_327_%=\ns_bitset0_b32 s77, 1\n.Lw_327_%=:\n"
".Lw_326_%=:\ns_bitcmp1_b32 s77, 2\ns_cbranch_scc0 .Lw_328_%=\nv_xor_b32 v60, s34, v141\nv_xor_b32 v61, s34, v143\nv_xor_b32 v62, s99, v149\n"
"v_xor_b32 v63, s99, v151\nv_xor_b32 v64, s67, v157\nv_xor_b32 v65, s67, v159\nv_or3_b32 v60, v60, v61, v62\nv_or3_b32 v63, v63, v64, v65\n"
"v_or_b32 v60, v60, v63\nv_cmp_ne_u32 vcc, 0, v60\ns_nop 0\ns_cmp_eq_u64 vcc, 0\ns_cbranch_scc0 .Lw_329_%=\ns_bitset0_b32 s77, 2\n.Lw_329_%=:\n"
".Lw_328_%=:\ns_add_u32 s68, s68, 1\ns_cmp_lt_u32 s68, 2097152\ns_cselect_b32 s77, s77, 0\ns_cmp_lg_u32 s77, 0\ns_cbranch_scc0 .Lw_330_%=\ns_sleep 2\n"
"s_branch .Lgp_319_%=\n.Lw_330_%=:\nM 0,144,164,0\nM 16,144,168,16\nM 132,144,172,132\nM 0,144,176,0\nM 16,144,180,16\nM 132,144,136,132\nM 0,144,140,0\n"
"M 16,144,148,16\nM 132,144,156,132\ns_nop 5\nC 0,0,1\nC 1,2,3\nbuffer_store_dwordx2 v[0:1], v89, s[40:43], 0 offen sc1\nC 16,16,17\nC 17,18,19\n"
"buffer_store_dwordx2 v[16:17], v90, s[40:43], 0 offen offset:128 sc1\nC 132,132,133\nC 133,134,135\n"
"buffer_store_dwordx2 v[132:133], v91, s[40:43], 0 offen sc1\ns_or_b32 s26, s58, s19\ns_or_b32 s26, s26, s59\ns_cmp_eq_u32 s26, 0\n"
"s_cbranch_scc0 .Lw_331_%=\ns_lshl_b32 s27, s21, 8\ns_add_u32 s27, s27, 33619968\nv_mov_b32 v245, s34\n"
"buffer_store_dword v245, v246, s[48:51], s27 offen sc1\n.Lw_331_%=:\ns_branch .Lend_2_%=\n.Lss_221_%=:\ns_cmp_lt_u32 s77, 1\ns_cbranch_scc0 .Lw_224_%=\n"
"s_mov_b32 s68, 0\n.Lpoll_225_%=:\ns_cmp_ge_u32 s4, 1\ns_cbranch_scc1 .Lpolld_226_%=\ns_sleep 1\ns_add_u32 s68, s68, 1\ns_cmp_eq_u32 s19, 1\n"
"s_cbranch_scc0 .Lw_227_%=\nbuffer_load_dword v160, v226, s[48:51], s66 offen sc1\ns_waitcnt vmcnt(0)\n.Lw_227_%=:\ns_cmp_eq_u32 s19, 1\n"
"s_cbranch_scc0 .Lw_228_%=\nv_cmp_eq_u32 vcc, s34, v160\ns_nop 0\ns_and_b32 s26, vcc_lo, 127\ns_ff0_i32_b32 s26, s26\ns_cmp_ge_u32 s68, 2097152\n"
"s_cbranch_scc0 .Lw_229_%=\ns_mov_b32 s26, 7\n.Lw_229_%=:\nv_mov_b32 v243, s26\nds_write_b32 v153, v243\n.Lw_228_%=:\nds_read_b32 v243, v153\nW 0\n"
"v_readfirstlane_b32 s4, v243\ns_branch .Lpoll_225_%=\n.Lpolld_226_%=:\ns_mov_b32 s77, 1\nbuffer_load_dwordx4 v[164:167], v154, s[48:51], s5 offen sc1\n"
"buffer_load_dwordx4 v[168:171], v155, s[48:51], s5 offen sc1\nbuffer_load_dwordx4 v[180:183], v156, s[48:51], s5 offen sc1\n"
"buffer_load_dwordx4 v[172:175], v157, s[48:51], s5 offen sc1\nbuffer_load_dwordx4 v[176:179], v158, s[48:51], s5 offen sc1\ns_cmp_eq_u32 s77, 1\n"
"s_cbranch_scc0 .Lw_230_%=\ns_cmp_ge_u32 s4, 2\ns_cbranch_scc0 .Lw_231_%=\ns_mov_b32 s77, 2\n"
"buffer_load_dwordx4 v[204:207], v154, s[48:51], s6 offen sc1\nbuffer_load_dwordx4 v[208:211], v155, s[48:51], s6 offen sc1\n"
"buffer_load_dwordx4 v[220:223], v156, s[48:51], s6 offen sc1\nbuffer_load_dwordx4 v[212:215], v157, s[48:51], s6 offen sc1\n"
"buffer_load_dwordx4 v[216:219], v158, s[48:51], s6 offen sc1\n.Lw_231_%=:\n.Lw_230_%=:\ns_cmp_eq_u32 s77, 1\ns_cbranch_scc0 .Lw_232_%=\n"
"s_waitcnt vmcnt(0)\n.Lw_232_%=:\ns_cmp_eq_u32 s77, 2\ns_cbranch_scc0 .Lw_233_%=\ns_waitcnt vmcnt(5)\n.Lw_233_%=:\ns_branch .Ln_223_%=\n.Lw_224_%=:\n"
"s_cmp_eq_u32 s77, 1\ns_cbranch_scc0 .Lw_234_%=\ns_waitcnt vmcnt(0)\n.Lw_234_%=:\ns_cmp_eq_u32 s77, 2\ns_cbranch_scc0 .Lw_235_%=\ns_waitcnt vmcnt(5)\n"
".Lw_235_%=:\n.Ln_223_%=:\ns_cmp_eq_u32 s19, 1\ns_cbranch_scc0 .Lw_236_%=\nbuffer_load_dword v160, v226, s[48:51], s66 offen sc1\n.Lw_236_%=:\n"
"s_branch .Lsb_222_%=\n.Lss_237_%=:\ns_cmp_eq_u32 s19, 1\ns_cbranch_scc0 .Lw_239_%=\nv_cmp_eq_u32 vcc, s34, v160\ns_nop 0\ns_and_b32 s26, vcc_lo, 127\n"
"s_ff0_i32_b32 s26, s26\nv_mov_b32 v243, s26\nds_write_b32 v153, v243\n.Lw_239_%=:\nds_read_b32 v243, v153\nW 0\nv_readfirstlane_b32 s4, v243\n"
"s_cmp_lt_u32 s77, 2\ns_cbranch_scc0 .Lw_241_%=\ns_mov_b32 s68, 0\n.Lpoll_242_%=:\ns_cmp_ge_u32 s4, 2\ns_cbranch_scc1 .Lpolld_243_%=\ns_sleep 1\n"
"s_add_u32 s68, s68, 1\ns_cmp_eq_u32 s19, 1\ns_cbranch_scc0 .Lw_244_%=\nbuffer_load_dword v160, v226, s[48:51], s66 offen sc1\ns_waitcnt vmcnt(0)\n"
".Lw_244_%=:\ns_cmp_eq_u32 s19, 1\ns_cbranch_scc0 .Lw_245_%=\nv_cmp_eq_u32 vcc, s34, v160\ns_nop 0\ns_and_b32 s26, vcc_lo, 127\ns_ff0_i32_b32 s26, s26\n"
"s_cmp_ge_u32 s68, 2097152\ns_cbranch_scc0 .Lw_246_%=\ns_mov_b32 s26, 7\n.Lw_246_%=:\nv_mov_b32 v243, s26\nds_write_b32 v153, v243\n.Lw_245_%=:\n"
"ds_read_b32 v243, v153\nW 0\nv_readfirstlane_b32 s4, v243\ns_branch .Lpoll_242_%=\n.Lpolld_243_%=:\ns_mov_b32 s77, 2\n"
"buffer_load_dwordx4 v[204:207], v154, s[48:51], s6 offen sc1\nbuffer_load_dwordx4 v[208:211], v155, s[48:51], s6 offen sc1\n"
"buffer_load_dwordx4 v[220:223], v156, s[48:51], s6 offen sc1\nbuffer_load_dwordx4 v[212:215], v157, s[48:51], s6 offen sc1\n"
"buffer_load_dwordx4 v[216:219], v158, s[48:51], s6 offen sc1\ns_cmp_eq_u32 s77, 2\ns_cbranch_scc0 .Lw_247_%=\ns_cmp_ge_u32 s4, 3\n"
"s_cbranch_scc0 .Lw_248_%=\ns_mov_b32 s77, 3\nbuffer_load_dwordx4 v[184:187], v154, s[48:51], s7 offen sc1\n"
"buffer_load_dwordx4 v[188:191], v155, s[48:51], s7 offen sc1\nbuffer_load_dwordx4 v[200:203], v156, s[48:51], s7 offen sc1\n"
"buffer_load_dwordx4 v[192:195], v157, s[48:51], s7 offen sc1\nbuffer_load_dwordx4 v[196:199], v158, s[48:51], s7 offen sc1\n.Lw_248_%=:\n.Lw_247_%=:\n"
"s_cmp_eq_u32 s77, 2\ns_cbranch_scc0 .Lw_249_%=\ns_waitcnt vmcnt(0)\n.Lw_249_%=:\ns_cmp_eq_u32 s77, 3\ns_cbranch_scc0 .Lw_250_%=\ns_waitcnt vmcnt(5)\n"
".Lw_250_%=:\ns_branch .Ln_240_%=\n.Lw_241_%=:\ns_cmp_eq_u32 s77, 2\ns_cbranch_scc0 .Lw_251_%=\ns_cmp_ge_u32 s4, 3\ns_cbranch_scc0 .Lw_252_%=\n"
"s_mov_b32 s77, 3\nbuffer_load_dwordx4 v[184:187], v154, s[48:51], s7 offen sc1\nbuffer_load_dwordx4 v[188:191], v155, s[48:51], s7 offen sc1\n"
"buffer_load_dwordx4 v[200:203], v156, s[48:51], s7 offen sc1\nbuffer_load_dwordx4 v[192:195], v157, s[48:51], s7 offen sc1\n"
"buffer_load_dwordx4 v[196:199], v158, s[48:51], s7 offen sc1\n.Lw_252_%=:\n.Lw_251_%=:\n.Ln_240_%=:\ns_cmp_eq_u32 s19, 1\ns_cbranch_scc0 .Lw_253_%=\n"
"buffer_load_dword v160, v226, s[48:51], s66 offen sc1\n.Lw_253_%=:\ns_branch .Lsb_238_%=\n.Lss_254_%=:\ns_cmp_eq_u32 s19, 1\ns_cbranch_scc0 .Lw_256_%=\n"
"v_cmp_eq_u32 vcc, s34, v160\ns_nop 0\ns_and_b32 s26, vcc_lo, 127\ns_ff0_i32_b32 s26, s26\nv_mov_b32 v243, s26\nds_write_b32 v153, v243\n.Lw_256_%=:\n"
"ds_read_b32 v243, v153\nW 0\nv_readfirstlane_b32 s4, v243\ns_cmp_lt_u32 s77, 3\ns_cbranch_scc0 .Lw_258_%=\ns_mov_b32 s68, 0\n.Lpoll_259_%=:\n"
"s_cmp_ge_u32 s4, 3\ns_cbranch_scc1 .Lpolld_260_%=\ns_sleep 1\ns_add_u32 s68, s68, 1\ns_cmp_eq_u32 s19, 1\ns_cbranch_scc0 .Lw_261_%=\n"
"buffer_load_dword v160, v226, s[48:51], s66 offen sc1\ns_waitcnt vmcnt(0)\n.Lw_261_%=:\ns_cmp_eq_u32 s19, 1\ns_cbranch_scc0 .Lw_262_%=\n"
"v_cmp_eq_u32 vcc, s34, v160\ns_nop 0\ns_and_b32 s26, vcc_lo, 127\ns_ff0_i32_b32 s26, s26\ns_cmp_ge_u32 s68, 2097152\ns_cbranch_scc0 .Lw_263_%=\n"
"s_mov_b32 s26, 7\n.Lw_263_%=:\nv_mov_b32 v243, s26\nds_write_b32 v153, v243\n.Lw_262_%=:\nds_read_b32 v243, v153\nW 0\nv_readfirstlane_b32 s4, v243\n"
"s_branch .Lpoll_259_%=\n.Lpolld_260_%=:\ns_mov_b32 s77, 3\nbuffer_load_dwordx4 v[184:187], v154, s[48:51], s7 offen sc1\n"
"buffer_load_dwordx4 v[188:191], v155, s[48:51], s7 offen sc1\nbuffer_load_dwordx4 v[200:203], v156, s[48:51], s7 offen sc1\n"
"buffer_load_dwordx4 v[192:195], v157, s[48:51], s7 offen sc1\nbuffer_load_dwordx4 v[196:199], v158, s[48:51], s7 offen sc1\ns_cmp_eq_u32 s77, 3\n"
"s_cbranch_scc0 .Lw_264_%=\ns_cmp_ge_u32 s4, 4\ns_cbranch_scc0 .Lw_265_%=\ns_mov_b32 s77, 4\n"
"buffer_load_dwordx4 v[164:167], v154, s[48:51], s8 offen sc1\nbuffer_load_dwordx4 v[168:171], v155, s[48:51], s8 offen sc1\n"
"buffer_load_dwordx4 v[180:183], v156, s[48:51], s8 offen sc1\nbuffer_load_dwordx4 v[172:175], v157, s[48:51], s8 offen sc1\n"
"buffer_load_dwordx4 v[176:179], v158, s[48:51], s8 offen sc1\n.Lw_265_%=:\n.Lw_264_%=:\ns_cmp_eq_u32 s77, 3\ns_cbranch_scc0 .Lw_266_%=\n"
"s_waitcnt vmcnt(0)\n.Lw_266_%=:\ns_cmp_eq_u32 s77, 4\ns_cbranch_scc0 .Lw_267_%=\ns_waitcnt vmcnt(5)\n.Lw_267_%=:\ns_branch .Ln_257_%=\n.Lw_258_%=:\n"
"s_cmp_eq_u32 s77, 3\ns_cbranch_scc0 .Lw_268_%=\ns_cmp_ge_u32 s4, 4\ns_cbranch_scc0 .Lw_269_%=\ns_mov_b32 s77, 4\n"
"buffer_load_dwordx4 v[164:167], v154, s[48:51], s8 offen sc1\nbuffer_load_dwordx4 v[168:171], v155, s[48:51], s8 offen sc1\n"
"buffer_load_dwordx4 v[180:183], v156, s[48:51], s8 offen sc1\nbuffer_load_dwordx4 v[172:175], v157, s[48:51], s8 offen sc1\n"
"buffer_load_dwordx4 v[176:179], v158, s[48:51], s8 offen sc1\n.Lw_269_%=:\n.Lw_268_%=:\n.Ln_257_%=:\ns_cmp_eq_u32 s19, 1\ns_cbranch_scc0 .Lw_270_%=\n"
"buffer_load_dword v160, v226, s[48:51], s66 offen sc1\n.Lw_270_%=:\ns_branch .Lsb_255_%=\n.Lss_271_%=:\ns_cmp_eq_u32 s19, 1\ns_cbranch_scc0 .Lw_273_%=\n"
"v_cmp_eq_u32 vcc, s34, v160\ns_nop 0\ns_and_b32 s26, vcc_lo, 127\ns_ff0_i32_b32 s26, s26\nv_mov_b32 v243, s26\nds_write_b32 v153, v243\n.Lw_273_%=:\n"
"ds_read_b32 v243, v153\nW 0\nv_readfirstlane_b32 s4, v243\ns_cmp_lt_u32 s77, 4\ns_cbranch_scc0 .Lw_275_%=\ns_mov_b32 s68, 0\n.Lpoll_276_%=:\n"
"s_cmp_ge_u32 s4, 4\ns_cbranch_scc1 .Lpolld_277_%=\ns_sleep 1\ns_add_u32 s68, s68, 1\ns_cmp_eq_u32 s19, 1\ns_cbranch_scc0 .Lw_278_%=\n"
"buffer_load_dword v160, v226, s[48:51], s66 offen sc1\ns_waitcnt vmcnt(0)\n.Lw_278_%=:\ns_cmp_eq_u32 s19, 1\ns_cbranch_scc0 .Lw_279_%=\n"
"v_cmp_eq_u32 vcc, s34, v160\ns_nop 0\ns_and_b32 s26, vcc_lo, 127\ns_ff0_i32_b32 s26, s26\ns_cmp_ge_u32 s68, 2097152\ns_cbranch_scc0 .Lw_280_%=\n"
"s_mov_b32 s26, 7\n.Lw_280_%=:\nv_mov_b32 v243, s26\nds_write_b32 v153, v243\n.Lw_279_%=:\nds_read_b32 v243, v153\nW 0\nv_readfirstlane_b32 s4, v243\n"
"s_branch .Lpoll_276_%=\n.Lpolld_277_%=:\ns_mov_b32 s77, 4\nbuffer_load_dwordx4 v[164:167], v154, s[48:51], s8 offen sc1\n"
"buffer_load_dwordx4 v[168:171], v155, s[48:51], s8 offen sc1\nbuffer_load_dwordx4 v[180:183], v156, s[48:51], s8 offen sc1\n"
"buffer_load_dwordx4 v[172:175], v157, s[48:51], s8 offen sc1\nbuffer_load_dwordx4 v[176:179], v158, s[48:51], s8 offen sc1\ns_cmp_eq_u32 s77, 4\n"
"s_cbranch_scc0 .Lw_281_%=\ns_cmp_ge_u32 s4, 5\ns_cbranch_scc0 .Lw_282_%=\ns_mov_b32 s77, 5\n"
"buffer_load_dwordx4 v[204:207], v154, s[48:51], s9 offen sc1\nbuffer_load_dwordx4 v[208:211], v155, s[48:51], s9 offen sc1\n"
"buffer_load_dwordx4 v[220:223], v156, s[48:51], s9 offen sc1\nbuffer_load_dwordx4 v[212:215], v157, s[48:51], s9 offen sc1\n"
"buffer_load_dwordx4 v[216:219], v158, s[48:51], s9 offen sc1\n.Lw_282_%=:\n.Lw_281_%=:\ns_cmp_eq_u32 s77, 4\ns_cbranch_scc0 .Lw_283_%=\n"
"s_waitcnt vmcnt(0)\n.Lw_283_%=:\ns_cmp_eq_u32 s77, 5\ns_cbranch_scc0 .Lw_284_%=\ns_waitcnt vmcnt(5)\n.Lw_284_%=:\ns_branch .Ln_274_%=\n.Lw_275_%=:\n"
"s_cmp_eq_u32 s77, 4\ns_cbranch_scc0 .Lw_285_%=\ns_cmp_ge_u32 s4, 5\ns_cbranch_scc0 .Lw_286_%=\ns_mov_b32 s77, 5\n"
"buffer_load_dwordx4 v[204:207], v154, s[48:51], s9 offen sc1\nbuffer_load_dwordx4 v[208:211], v155, s[48:51], s9 offen sc1\n"
"buffer_load_dwordx4 v[220:223], v156, s[48:51], s9 offen sc1\nbuffer_load_dwordx4 v[212:215], v157, s[48:51], s9 offen sc1\n"
"buffer_load_dwordx4 v[216:219], v158, s[48:51], s9 offen sc1\n.Lw_286_%=:\n.Lw_285_%=:\n.Ln_274_%=:\ns_cmp_eq_u32 s19, 1\ns_cbranch_scc0 .Lw_287_%=\n"
"buffer_load_dword v160, v226, s[48:51], s66 offen sc1\n.Lw_287_%=:\ns_branch .Lsb_272_%=\n.Lss_288_%=:\ns_cmp_eq_u32 s19, 1\ns_cbranch_scc0 .Lw_290_%=\n"
"v_cmp_eq_u32 vcc, s34, v160\ns_nop 0\ns_and_b32 s26, vcc_lo, 127\ns_ff0_i32_b32 s26, s26\nv_mov_b32 v243, s26\nds_write_b32 v153, v243\n.Lw_290_%=:\n"
"ds_read_b32 v243, v153\nW 0\nv_readfirstlane_b32 s4, v243\ns_cmp_lt_u32 s77, 5\ns_cbranch_scc0 .Lw_292_%=\ns_mov_b32 s68, 0\n.Lpoll_293_%=:\n"
"s_cmp_ge_u32 s4, 5\ns_cbranch_scc1 .Lpolld_294_%=\ns_sleep 1\ns_add_u32 s68, s68, 1\ns_cmp_eq_u32 s19, 1\ns_cbranch_scc0 .Lw_295_%=\n"
"buffer_load_dword v160, v226, s[48:51], s66 offen sc1\ns_waitcnt vmcnt(0)\n.Lw_295_%=:\ns_cmp_eq_u32 s19, 1\ns_cbranch_scc0 .Lw_296_%=\n"
"v_cmp_eq_u32 vcc, s34, v160\ns_nop 0\ns_and_b32 s26, vcc_lo, 127\ns_ff0_i32_b32 s26, s26\ns_cmp_ge_u32 s68, 2097152\ns_cbranch_scc0 .Lw_297_%=\n"
"s_mov_b32 s26, 7\n.Lw_297_%=:\nv_mov_b32 v243, s26\nds_write_b32 v153, v243\n.Lw_296_%=:\nds_read_b32 v243, v153\nW 0\nv_readfirstlane_b32 s4, v243\n"
"s_branch .Lpoll_293_%=\n.Lpolld_294_%=:\ns_mov_b32 s77, 5\nbuffer_load_dwordx4 v[204:207], v154, s[48:51], s9 offen sc1\n"
"buffer_load_dwordx4 v[208:211], v155, s[48:51], s9 offen sc1\nbuffer_load_dwordx4 v[220:223], v156, s[48:51], s9 offen sc1\n"
"buffer_load_dwordx4 v[212:215], v157, s[48:51], s9 offen sc1\nbuffer_load_dwordx4 v[216:219], v158, s[48:51], s9 offen sc1\ns_cmp_eq_u32 s77, 5\n"
"s_cbranch_scc0 .Lw_298_%=\ns_cmp_ge_u32 s4, 6\ns_cbranch_scc0 .Lw_299_%=\ns_mov_b32 s77, 6\n"
"buffer_load_dwordx4 v[184:187], v154, s[48:51], s10 offen sc1\nbuffer_load_dwordx4 v[188:191], v155, s[48:51], s10 offen sc1\n"
"buffer_load_dwordx4 v[200:203], v156, s[48:51], s10 offen sc1\nbuffer_load_dwordx4 v[192:195], v157, s[48:51], s10 offen sc1\n"
"buffer_load_dwordx4 v[196:199], v158, s[48:51], s10 offen sc1\n.Lw_299_%=:\n.Lw_298_%=:\ns_cmp_eq_u32 s77, 5\ns_cbranch_scc0 .Lw_300_%=\n"
"s_waitcnt vmcnt(0)\n.Lw_300_%=:\ns_cmp_eq_u32 s77, 6\ns_cbranch_scc0 .Lw_301_%=\ns_waitcnt vmcnt(5)\n.Lw_301_%=:\ns_branch .Ln_291_%=\n.Lw_292_%=:\n"
"s_cmp_eq_u32 s77, 5\ns_cbranch_scc0 .Lw_302_%=\ns_cmp_ge_u32 s4, 6\ns_cbranch_scc0 .Lw_303_%=\ns_mov_b32 s77, 6\n"
"buffer_load_dwordx4 v[184:187], v154, s[48:51], s10 offen sc1\nbuffer_load_dwordx4 v[188:191], v155, s[48:51], s10 offen sc1\n"
"buffer_load_dwordx4 v[200:203], v156, s[48:51], s10 offen sc1\nbuffer_load_dwordx4 v[192:195], v157, s[48:51], s10 offen sc1\n"
"buffer_load_dwordx4 v[196:199], v158, s[48:51], s10 offen sc1\n.Lw_303_%=:\n.Lw_302_%=:\n.Ln_291_%=:\ns_cmp_eq_u32 s19, 1\ns_cbranch_scc0 .Lw_304_%=\n"
"buffer_load_dword v160, v226, s[48:51], s66 offen sc1\n.Lw_304_%=:\ns_branch .Lsb_289_%=\n.Lss_305_%=:\ns_waitcnt vmcnt(5)\ns_cmp_eq_u32 s19, 1\n"
"s_cbranch_scc0 .Lw_307_%=\nv_cmp_eq_u32 vcc, s34, v160\ns_nop 0\ns_and_b32 s26, vcc_lo, 127\ns_ff0_i32_b32 s26, s26\nv_mov_b32 v243, s26\n"
"ds_write_b32 v153, v243\n.Lw_307_%=:\nds_read_b32 v243, v153\nW 0\nv_readfirstlane_b32 s4, v243\ns_cmp_eq_u32 s77, 5\ns_cbranch_scc0 .Lw_308_%=\n"
"s_cmp_ge_u32 s4, 6\ns_cbranch_scc0 .Lw_309_%=\ns_mov_b32 s77, 6\nbuffer_load_dwordx4 v[184:187], v154, s[48:51], s10 offen sc1\n"
"buffer_load_dwordx4 v[188:191], v155, s[48:51], s10 offen sc1\nbuffer_load_dwordx4 v[200:203], v156, s[48:51], s10 offen sc1\n"
"buffer_load_dwordx4 v[192:195], v157, s[48:51], s10 offen sc1\nbuffer_load_dwordx4 v[196:199], v158, s[48:51], s10 offen sc1\n.Lw_309_%=:\n.Lw_308_%=:\n"
"s_mov_b32 s68, 0\n.Lpoll_310_%=:\ns_cmp_ge_u32 s4, 7\ns_cbranch_scc1 .Lpolld_311_%=\ns_sleep 1\ns_add_u32 s68, s68, 1\ns_cmp_eq_u32 s19, 1\n"
"s_cbranch_scc0 .Lw_312_%=\nbuffer_load_dword v160, v226, s[48:51], s66 offen sc1\ns_waitcnt vmcnt(0)\n.Lw_312_%=:\ns_cmp_eq_u32 s19, 1\n"
"s_cbranch_scc0 .Lw_313_%=\nv_cmp_eq_u32 vcc, s34, v160\ns_nop 0\ns_and_b32 s26, vcc_lo, 127\ns_ff0_i32_b32 s26, s26\ns_cmp_ge_u32 s68, 2097152\n"
"s_cbranch_scc0 .Lw_314_%=\ns_mov_b32 s26, 7\n.Lw_314_%=:\nv_mov_b32 v243, s26\nds_write_b32 v153, v243\n.Lw_313_%=:\nds_read_b32 v243, v153\nW 0\n"
"v_readfirstlane_b32 s4, v243\ns_branch .Lpoll_310_%=\n.Lpolld_311_%=:\ns_cmp_eq_u32 s77, 5\ns_cbranch_scc0 .Lw_315_%=\ns_cmp_ge_u32 s4, 6\n"
"s_cbranch_scc0 .Lw_316_%=\ns_mov_b32 s77, 6\nbuffer_load_dwordx4 v[184:187], v154, s[48:51], s10 offen sc1\n"
"buffer_load_dwordx4 v[188:191], v155, s[48:51], s10 offen sc1\nbuffer_load_dwordx4 v[200:203], v156, s[48:51], s10 offen sc1\n"
"buffer_load_dwordx4 v[192:195], v157, s[48:51], s10 offen sc1\nbuffer_load_dwordx4 v[196:199], v158, s[48:51], s10 offen sc1\n.Lw_316_%=:\n.Lw_315_%=:\n"
"s_cmp_eq_u32 s77, 6\ns_cbranch_scc0 .Lw_317_%=\ns_cmp_ge_u32 s4, 7\ns_cbranch_scc0 .Lw_318_%=\ns_mov_b32 s77, 7\n"
"buffer_load_dwordx4 v[96:99], v154, s[48:51], s11 offen sc1\nbuffer_load_dwordx4 v[100:103], v155, s[48:51], s11 offen sc1\n"
"buffer_load_dwordx4 v[112:115], v156, s[48:51], s11 offen sc1\nbuffer_load_dwordx4 v[104:107], v157, s[48:51], s11 offen sc1\n"
"buffer_load_dwordx4 v[108:111], v158, s[48:51], s11 offen sc1\n.Lw_318_%=:\n.Lw_317_%=:\ns_branch .Lsb_306_%=\n.Lfb0_11_%=:\n"
"s_waitcnt vmcnt(0) lgkmcnt(0)\ns_nop 15\ns_mov_b32 s35, 1\ns_barrier\ns_branch .Lrs_16_%=\n.Lmx_13_%=:\ns_nop 11\nv_mov_b32 v180, 4286578687\n"
"v_mov_b32 v181, 4286578687\nv_mov_b32 v182, 4286578687\nv_and_b32 v254, 15, v255\nv_lshlrev_b32 v254, 8, v254\nv_lshrrev_b32 v253, 4, v255\n"
"v_lshlrev_b32 v253, 4, v253\nv_add_u32 v254, v254, v253\nv_add_u32 v186, 1024, v254\ns_mov_b32 s53, 0\ns_nop 11\n.Lfbl_332_%=:\n"
"buffer_load_dwordx4 v[192:195], v186, s[60:63], s53 offen\nbuffer_load_dwordx4 v[196:199], v186, s[60:63], s53 offen offset:64\n"
"buffer_load_dwordx4 v[200:203], v186, s[60:63], s53 offen offset:128\nbuffer_load_dwordx4 v[204:207], v186, s[60:63], s53 offen offset:192\n"
"s_waitcnt vmcnt(3)\nv_mfma_f32_16x16x32_bf16 v[144:147], v[192:195], v[96:99], 0\nv_mfma_f32_16x16x32_bf16 v[148:151], v[192:195], v[112:115], 0\n"
"v_mfma_f32_16x16x32_bf16 v[152:155], v[192:195], v[128:131], 0\ns_waitcnt vmcnt(2)\nM 144,196,100,144\nM 148,196,116,148\nM 152,196,132,152\n"
"s_waitcnt vmcnt(1)\nM 144,200,104,144\nM 148,200,120,148\nM 152,200,136,152\ns_waitcnt vmcnt(0)\nM 144,204,108,144\nM 148,204,124,148\n"
"M 152,204,140,152\ns_nop 5\nv_max3_f32 v180, v180, v144, v145\nv_max3_f32 v180, v180, v146, v147\nv_max3_f32 v181, v181, v148, v149\n"
"v_max3_f32 v181, v181, v150, v151\nv_max3_f32 v182, v182, v152, v153\nv_max3_f32 v182, v182, v154, v155\ns_add_u32 s53, s53, 4096\n"
"s_cmp_lt_u32 s53, 262144\ns_nop 3\ns_cbranch_scc1 .Lfbl_332_%=\nv_mov_b32 v183, v180\nv_mov_b32 v184, v181\nv_mov_b32 v185, v182\n"
"v_permlane16_swap_b32 v180, v183\nv_permlane16_swap_b32 v181, v184\nv_permlane16_swap_b32 v182, v185\nv_max_f32 v180, v180, v183\n"
"v_max_f32 v181, v181, v184\nv_max_f32 v182, v182, v185\nv_mov_b32 v183, v180\nv_mov_b32 v184, v181\nv_mov_b32 v185, v182\n"
"v_permlane32_swap_b32 v180, v183\nv_permlane32_swap_b32 v181, v184\nv_permlane32_swap_b32 v182, v185\nv_max_f32 v180, v180, v183\n"
"v_max_f32 v181, v181, v184\nv_max_f32 v182, v182, v185\nv_sub_f32 v228, 0, v180\nv_sub_f32 v229, 0, v180\nv_sub_f32 v230, 0, v180\n"
"v_sub_f32 v231, 0, v180\nv_sub_f32 v232, 0, v181\nv_sub_f32 v233, 0, v181\nv_sub_f32 v234, 0, v181\nv_sub_f32 v235, 0, v181\nv_sub_f32 v236, 0, v182\n"
"v_sub_f32 v237, 0, v182\nv_sub_f32 v238, 0, v182\nv_sub_f32 v239, 0, v182\ns_cmp_lt_u32 s19, 4\ns_nop 1\ns_cbranch_scc1 .Lmr0_14_%=\n"
"s_branch .Lmr1_15_%=\n.Lend_2_%=:\n.purgem M\n.purgem R\n.purgem T\n.purgem E\n.purgem C\n.purgem W\n"
    : "+{v0}"(tid)
    : "{s[0:1]}"(__builtin_amdgcn_kernarg_segment_ptr()), "{s18}"(blockIdx.x),
      "s"((unsigned)(size_t)(__attribute__((address_space(3))) char*)smem)
    : "v1", "v2", "v3", "v4", "v5", "v6", "v7", "v8", "v9", "v10", "v11", "v12", "v13", "v14", "v15", "v16", "v17", "v18", "v19", "v20", "v21", "v22", "v23", "v24", "v25", "v26", "v27", "v28", "v29", "v30", "v31", "v32", "v33", "v34", "v35", "v36", "v37", "v38", "v39", "v40", "v41", "v42", "v43", "v44", "v45", "v46", "v47", "v48", "v49", "v50", "v51", "v52", "v53", "v54", "v55", "v56", "v57", "v58", "v59", "v60", "v61", "v62", "v63", "v64", "v65", "v66", "v67", "v68", "v69", "v70", "v71", "v72", "v73", "v74", "v75", "v76", "v77", "v78", "v79", "v80", "v81", "v82", "v83", "v84", "v85", "v86", "v87", "v88", "v89", "v90", "v91", "v92", "v93", "v94", "v95", "v96", "v97", "v98", "v99", "v100", "v101", "v102", "v103", "v104", "v105", "v106", "v107", "v108", "v109", "v110", "v111", "v112", "v113", "v114", "v115", "v116", "v117", "v118", "v119", "v120", "v121", "v122", "v123", "v124", "v125", "v126", "v127", "v128", "v129", "v130", "v131", "v132", "v133", "v134", "v135", "v136", "v137", "v138", "v139", "v140", "v141", "v142", "v143", "v144", "v145", "v146", "v147", "v148", "v149", "v150", "v151", "v152", "v153", "v154", "v155", "v156", "v157", "v158", "v159", "v160", "v161", "v162", "v163", "v164", "v165", "v166", "v167", "v168", "v169", "v170", "v171", "v172", "v173", "v174", "v175", "v176", "v177", "v178", "v179", "v180", "v181", "v182", "v183", "v184", "v185", "v186", "v187", "v188", "v189", "v190", "v191", "v192", "v193", "v194", "v195", "v196", "v197", "v198", "v199", "v200", "v201", "v202", "v203", "v204", "v205", "v206", "v207", "v208", "v209", "v210", "v211", "v212", "v213", "v214", "v215", "v216", "v217", "v218", "v219", "v220", "v221", "v222", "v223", "v224", "v225", "v226", "v227", "v228", "v229", "v230", "v231", "v232", "v233", "v234", "v235", "v236", "v237", "v238", "v239", "v240", "v241", "v242", "v243", "v244", "v245", "v246", "v247", "v248", "v249", "v250", "v251", "v252", "v253", "v254", "v255", "s4", "s5", "s6", "s7", "s8", "s9", "s10", "s11", "s12", "s13", "s14", "s15", "s16", "s17", "s19", "s20", "s21", "s22", "s23", "s24", "s25", "s26", "s27", "s28", "s29", "s30", "s31", "s34", "s35", "s36", "s37", "s38", "s39", "s40", "s41", "s42", "s43", "s44", "s45", "s46", "s47", "s48", "s49", "s50", "s51", "s52", "s53", "s54", "s55", "s56", "s57", "s58", "s59", "s60", "s61", "s62", "s63", "s64", "s65", "s66", "s67", "s68", "s69", "s70", "s71", "s72", "s73", "s74", "s75", "s76", "s77", "s78", "s79", "s80", "s81", "s82", "s83", "s84", "s85", "s86", "s87", "s88", "s89", "s90", "s91", "s92", "s93", "s94", "s95", "s96", "s97", "s98", "s99", "vcc", "scc", "memory");
}

extern "C" void kernel_launch(void* const* d_in, const int* in_sizes, int n_in, void* d_out, int out_size, void* d_ws, size_t ws_size, hipStream_t stream) {
  gqa_block_kernel<<<256, 512, 0, stream>>>((const u16*)d_in[0], (const u16*)d_in[1], (const u16*)d_in[2], (const u16*)d_in[3], (const u16*)d_in[4], (u16*)d_out, (char*)d_ws);
}
```
